```python
import torch

def _softmax(x, axis=-1):
    return torch.softmax(x, dim=axis)
def _gelu(x):
    return torch.nn.functional.gelu(x, approximate="tanh")
def _silu(x):
    return torch.nn.functional.silu(x)
def _sigmoid(x):
    return torch.sigmoid(x)
def _layernorm(x, axis=-1, eps=1e-5):
    m = x.mean(dim=axis, keepdim=True)
    v = x.var(dim=axis, keepdim=True, unbiased=False)
    return (x - m) / torch.sqrt(v + eps)
def _scatter_add(tgt, idx, src, axis=0):
    return tgt.clone().index_add_(axis, idx, src)
def _segment_sum(src, seg, n):
    out = torch.zeros((n,) + src.shape[1:], dtype=src.dtype)
    return out.index_add_(0, seg, src)
def _pad(x, pw):
    flat = tuple(v for pair in reversed(pw) for v in pair)
    return torch.nn.functional.pad(x, flat)

kernel_name = 'sequential_preLN__ssm_conv_s4d__swiglu_ffn__prompt'

def setup_inputs():

    _w = (lambda a: (lambda t: t.to(torch.bfloat16) if t.is_floating_point() else t)(torch.as_tensor(a)))
    def _A_setup():
        Bsz, L, D, N = 1, 2048, 512, 128
        return {
            "u": _w((lambda *s: torch.randn(*s, dtype=torch.bfloat16) * 0.05)(Bsz, L, D)),
            "log_A_real": _w((lambda *s: torch.randn(*s, dtype=torch.bfloat16) * 6.4)(D, N) * 0.5),
            "A_imag": _w((lambda *s: torch.randn(*s, dtype=torch.bfloat16) * 0.05)(D, N) * 3.14),
            "C_real": _w((lambda *s: torch.randn(*s, dtype=torch.bfloat16) * 0.05)(D, N) / N ** 0.5),
            "C_imag": _w((lambda *s: torch.randn(*s, dtype=torch.bfloat16) * 0.05)(D, N) / N ** 0.5),
            "log_dt": _w((lambda *s: torch.randn(*s, dtype=torch.bfloat16) * 0.05)(D) * 0.5 - 4.0),
            "D_skip": _w((lambda *s: torch.randn(*s, dtype=torch.bfloat16) * 0.05)(D)),
        }
    _A_in = _A_setup()

    def _B_setup():
        B, S, D, F = 1, 2048, 512, 1792
        return {
            "x": _w((lambda *s: torch.randn(*s, dtype=torch.bfloat16) * 0.05)(B, S, D)),
            "w_gate": _w((lambda *s: torch.randn(*s, dtype=torch.bfloat16) * 0.05)(D, F)),
            "w_up": _w((lambda *s: torch.randn(*s, dtype=torch.bfloat16) * 0.05)(D, F)),
            "w_down": _w((lambda *s: torch.randn(*s, dtype=torch.bfloat16) * 0.05)(F, D)),
        }
    _B_in = _B_setup()

    return {
        "A_u": _A_in["u"],
        "A_log_A_real": _A_in["log_A_real"],
        "A_A_imag": _A_in["A_imag"],
        "A_C_real": _A_in["C_real"],
        "A_C_imag": _A_in["C_imag"],
        "A_log_dt": _A_in["log_dt"],
        "A_D_skip": _A_in["D_skip"],
        "B_x": _B_in["x"],
        "B_w_gate": _B_in["w_gate"],
        "B_w_up": _B_in["w_up"],
        "B_w_down": _B_in["w_down"],
    }

def reference(A_u, A_log_A_real, A_A_imag, A_C_real, A_C_imag, A_log_dt, A_D_skip, B_x, B_w_gate, B_w_up, B_w_down):
    def _A_ref(u, log_A_real, A_imag, C_real, C_imag, log_dt, D_skip):
        L = u.shape[1]
        dt = torch.exp(log_dt)
        A = -torch.exp(log_A_real) + 1j * A_imag
        C = (C_real + 1j * C_imag) * dt[:, None]
        pos = (lambda x, dt: x.to(dt))(torch.arange(L), log_dt.dtype)
        P = torch.exp((A * dt[:, None])[:, :, None] * pos[None, None, :])
        K = 2.0 * torch.einsum("dn,dnl->dl", C, P).real
        L2 = 2 * L
        u_t = (lambda x, *d: x.permute(*d))(u, 0, 2, 1)
        Kf = (lambda x, *a, **k: torch.fft.rfft(x.float(), *a, **k))(K, L2)
        Uf = (lambda x, *a, **k: torch.fft.rfft(x.float(), *a, **k))(u_t, L2)
        y = (lambda x, *a, **k: torch.fft.irfft(x, *a, **k).to(torch.bfloat16))(Uf * Kf[None, :, :], L2)[:, :, :L]
        y = (lambda x, *d: x.permute(*d))(y, 0, 2, 1)
        return y + u * D_skip[None, None, :]

    def _B_ref(x, w_gate, w_up, w_down):
        g = _silu(x @ w_gate)
        u = x @ w_up
        return (g * u) @ w_down

    x = A_u
    h = x + _A_ref(_layernorm(x, axis=-1), A_log_A_real, A_A_imag, A_C_real, A_C_imag, A_log_dt, A_D_skip)
    h2 = h + _B_ref(_layernorm(h, axis=-1), B_w_gate, B_w_up, B_w_down)
    return h2
```

```cpp
#include <hip/hip_runtime.h>
#include <hip/hip_bf16.h>
#include <stdint.h>
typedef unsigned short u16;
static __device__ __forceinline__ float bf2f(u16 b){ return __uint_as_float(((unsigned)b)<<16); }
static __device__ __forceinline__ float rbf(float f){ return (float)(__bf16)f; }
static __device__ __forceinline__ u16 f2bf(float f){ __bf16 b=(__bf16)f; return __builtin_bit_cast(u16,b); }
constexpr int L=2048,D=512,N=128,F=1792;

__global__ void ln_k(const u16* x, u16* o){
  int row=blockIdx.x; int t=threadIdx.x;
  float v[8]; float s=0;
  for(int i=0;i<8;i++){ v[i]=bf2f(x[row*D+t*8+i]); s+=v[i]; }
  for(int o2=32;o2;o2>>=1) s+=__shfl_xor(s,o2);
  float m=s/D; float q=0;
  for(int i=0;i<8;i++){ float d=v[i]-m; q+=d*d; }
  for(int o2=32;o2;o2>>=1) q+=__shfl_xor(q,o2);
  float mb=rbf(m), vb=rbf(q/D); float sd=rbf(sqrtf(rbf(vb+1e-5f)));
  for(int i=0;i<8;i++) o[row*D+t*8+i]=f2bf(rbf(v[i]-mb)/sd);
}
__global__ void kgen_k(const u16* lar,const u16* ai,const u16* cr,const u16* ci,const u16* ldt,float* K){
  int d=blockIdx.y; int l=blockIdx.x*256+threadIdx.x;
  float dt=rbf(expf(bf2f(ldt[d]))); float pos=rbf((float)l);
  float s=0;
  for(int n=0;n<N;n++){
    float ar=-rbf(expf(bf2f(lar[d*N+n])))*dt, aim=bf2f(ai[d*N+n])*dt;
    float e=expf(ar*pos); float sn,cs; sincosf(aim*pos,&sn,&cs);
    s+= bf2f(cr[d*N+n])*dt*e*cs - bf2f(ci[d*N+n])*dt*e*sn;
  }
  K[d*L+l]=2.f*s;
}
__global__ void conv_k(const u16* x,const u16* u,const float* K,const u16* dsk,u16* h){
  int l=blockIdx.y; int d=blockIdx.x*256+threadIdx.x;
  float s=0;
  for(int j=0;j<=l;j++) s+=K[d*L+j]*bf2f(u[(l-j)*D+d]);
  float y=rbf(s); float sk=rbf(bf2f(u[l*D+d])*bf2f(dsk[d]));
  h[l*D+d]=f2bf(bf2f(x[l*D+d])+rbf(y+sk));
}
__global__ void g1_k(const u16* a,const u16* wg,const u16* wu,u16* act){
  int l=blockIdx.y; int f=blockIdx.x*256+threadIdx.x;
  float g=0,u=0;
  for(int k=0;k<D;k++){ float x=bf2f(a[l*D+k]); g+=x*bf2f(wg[k*F+f]); u+=x*bf2f(wu[k*F+f]); }
  g=rbf(g); u=rbf(u); float sg=rbf(g/(1.f+expf(-g)));
  act[l*F+f]=f2bf(sg*u);
}
__global__ void g2_k(const u16* act,const u16* wd,const u16* h,u16* out){
  int l=blockIdx.y; int d=blockIdx.x*256+threadIdx.x;
  float s=0;
  for(int k=0;k<F;k++) s+=bf2f(act[l*F+k])*bf2f(wd[k*D+d]);
  out[l*D+d]=f2bf(bf2f(h[l*D+d])+rbf(s));
}
extern "C" void kernel_launch(void* const* d_in, const int* in_sizes, int n_in, void* d_out, int out_size, void* d_ws, size_t ws_size, hipStream_t stream){
  const u16* x=(const u16*)d_in[0];
  char* ws=(char*)d_ws;
  u16* u=(u16*)ws; float* K=(float*)(ws+(4<<20)); u16* h=(u16*)(ws+(16<<20)); u16* hn=(u16*)(ws+(20<<20)); u16* act=(u16*)(ws+(32<<20));
  ln_k<<<L,64,0,stream>>>(x,u);
  kgen_k<<<dim3(L/256,D),256,0,stream>>>((const u16*)d_in[1],(const u16*)d_in[2],(const u16*)d_in[3],(const u16*)d_in[4],(const u16*)d_in[5],K);
  conv_k<<<dim3(D/256,L),256,0,stream>>>(x,u,K,(const u16*)d_in[6],h);
  ln_k<<<L,64,0,stream>>>(h,hn);
  g1_k<<<dim3(F/256,L),256,0,stream>>>(hn,(const u16*)d_in[8],(const u16*)d_in[9],act);
  g2_k<<<dim3(D/256,L),256,0,stream>>>(act,(const u16*)d_in[10],h,(u16*)d_out);
}
```

```cpp
#include <hip/hip_runtime.h>
#include <hip/hip_bf16.h>
#include <stdint.h>

typedef unsigned short u16;
typedef unsigned int u32;
typedef short v4s __attribute__((ext_vector_type(4)));
typedef short v8s __attribute__((ext_vector_type(8)));
typedef __bf16 v2bf __attribute__((ext_vector_type(2)));
typedef __bf16 v8bf __attribute__((ext_vector_type(8)));
typedef _Float16 v2h __attribute__((ext_vector_type(2)));
typedef _Float16 v8h __attribute__((ext_vector_type(8)));
typedef float v2f __attribute__((ext_vector_type(2)));
typedef float v4f __attribute__((ext_vector_type(4)));
typedef float v16f __attribute__((ext_vector_type(16)));
typedef u32 v2u __attribute__((ext_vector_type(2)));
typedef u32 v3u __attribute__((ext_vector_type(3)));
typedef u32 v4u __attribute__((ext_vector_type(4)));
#define LAS __attribute__((address_space(3)))
#define GAS __attribute__((address_space(1)))
#define DEV __device__ __forceinline__

constexpr int L = 2048, D = 512, NS = 128, F = 1792;

constexpr size_t WS_STATS = 0;
constexpr size_t WS_CSUM  = 64 << 10;
constexpr size_t WS_HT    = 1 << 20;
constexpr size_t WS_WT1   = 4 << 20;
constexpr size_t WS_WT2   = 8 << 20;
constexpr size_t WS_ACT   = 12 << 20;
constexpr size_t WS_DBG   = 32 << 20;

DEV float bf2f(u32 b) { return __uint_as_float(b << 16); }
DEV float bflo(u32 p) { return __uint_as_float(p << 16); }
DEV float bfhi(u32 p) { return __uint_as_float(p & 0xffff0000u); }
DEV u32 pkbf(float a, float b) { v2f f = {a, b}; return __builtin_bit_cast(u32, __builtin_convertvector(f, v2bf)); }
DEV u32 pkh(float a, float b) { v2f f = {a, b}; return __builtin_bit_cast(u32, __builtin_convertvector(f, v2h)); }
DEV float rbf(float a) { return bflo(pkbf(a, a)); }
DEV float dot2bf(u32 a, u32 b, float c) { return __builtin_amdgcn_fdot2_f32_bf16(__builtin_bit_cast(v2bf, a), __builtin_bit_cast(v2bf, b), c, false); }
DEV v2u trread(LAS char* p) { return __builtin_bit_cast(v2u, __builtin_amdgcn_ds_read_tr16_b64_v4i16((LAS v4s*)p)); }
DEV void glds16(const void* g, LAS char* l) { __builtin_amdgcn_global_load_lds((const GAS void*)g, (LAS void*)l, 16, 0, 0); }
#define WAITV(n) asm volatile("s_waitcnt vmcnt(" #n ")" ::: "memory")
#define WAITL(n) asm volatile("s_waitcnt lgkmcnt(" #n ")" ::: "memory")
#define BAR() asm volatile("s_barrier" ::: "memory")

#define STAMP(ws, kid, slot) do {} while (0)

DEV float wave_sum(float v) {
#pragma unroll
  for (int o = 32; o; o >>= 1) v += __shfl_xor(v, o);
  return v;
}
DEV float wave_max(float v) {
#pragma unroll
  for (int o = 32; o; o >>= 1) v = fmaxf(v, __shfl_xor(v, o));
  return v;
}

__global__ __launch_bounds__(256) void k_prep(const u16* __restrict__ x, const u16* __restrict__ wg, const u16* __restrict__ wu,
                                              const u16* __restrict__ wd, char* __restrict__ ws) {
  __shared__ __attribute__((aligned(16))) char sm_[128 * 144];
  LAS char* sm = (LAS char*)sm_;
  const int t = threadIdx.x, lane = t & 63, w = t >> 6;
  STAMP(ws, 0, 0);
  if (blockIdx.x < 256) {
    const int row0 = blockIdx.x * 8 + w * 2;
    v4u a[2];
#pragma unroll
    for (int i = 0; i < 2; ++i) a[i] = *(const v4u*)(x + (size_t)(row0 + i) * D + lane * 8);
#pragma unroll
    for (int i = 0; i < 2; ++i) {
      float v[8];
#pragma unroll
      for (int j = 0; j < 4; ++j) { v[2 * j] = bflo(a[i][j]); v[2 * j + 1] = bfhi(a[i][j]); }
      float s = 0;
#pragma unroll
      for (int j = 0; j < 8; ++j) s += v[j];
      s = wave_sum(s);
      const float m = s * (1.0f / D);
      float q = 0;
#pragma unroll
      for (int j = 0; j < 8; ++j) { const float d = v[j] - m; q += d * d; }
      q = wave_sum(q);
      const float vb = rbf(q * (1.0f / D));
      const float sd = __builtin_sqrtf(rbf(vb + 1e-5f));
      if (lane == 0) ((u32*)(ws + WS_STATS))[row0 + i] = pkbf(m, sd);
    }
  } else {
    const int rb = blockIdx.x - 256;
    const int G = lane >> 4, q4 = (lane >> 2) & 3, p4 = lane & 3, i16 = lane & 15;
    const int mat = rb / 112, rem = rb % 112, cg = rem % 28, q = rem / 28;
    const u16* W = mat ? wu : wg;
    v4u a[4], b[2];
#pragma unroll
    for (int i = 0; i < 4; ++i) a[i] = *(const v4u*)(W + (size_t)(128 * q + (t >> 3) + 32 * i) * F + 64 * cg + (t & 7) * 8);
    const int dg = rb & 7, fb = rb >> 3;
#pragma unroll
    for (int i = 0; i < 2; ++i) b[i] = *(const v4u*)(wd + (size_t)(64 * fb + (t >> 3) + 32 * i) * D + 64 * dg + (t & 7) * 8);
#pragma unroll
    for (int i = 0; i < 4; ++i) *(LAS v4u*)(sm + ((t >> 3) + 32 * i) * 144 + (t & 7) * 16) = a[i];
    __syncthreads();
    {
      const int g16 = 4 * cg + w, j = g16 / 7, pt = g16 % 7;
      float cs = 0;
#pragma unroll
      for (int it = 0; it < 4; ++it) {
        const int ksl = 2 * it + (G >> 1), h = G & 1;
        LAS char* p = sm + (16 * ksl + 8 * h + q4) * 144 + 32 * w + 8 * p4;
        const v2u lo = trread(p), hi = trread(p + 4 * 144);
        const v4u o = {lo[0], lo[1], hi[0], hi[1]};
#pragma unroll
        for (int e = 0; e < 4; ++e) cs = dot2bf(o[e], 0x3f803f80u, cs);
        const int ks = 8 * q + ksl;
        *(v4u*)(ws + WS_WT1 + ((size_t)((j * 32 + ks) * 7 + pt) * 64 + 32 * h + 16 * mat + i16) * 16) = o;
      }
      cs += __shfl_xor(cs, 16);
      cs += __shfl_xor(cs, 32);
      if (lane < 16) ((float*)(ws + WS_CSUM))[j * 1024 + q * 224 + mat * 112 + pt * 16 + lane] = cs;
    }
    __syncthreads();
#pragma unroll
    for (int i = 0; i < 2; ++i) *(LAS v4u*)(sm + ((t >> 3) + 32 * i) * 144 + (t & 7) * 16) = b[i];
    __syncthreads();
    {
#pragma unroll
      for (int it = 0; it < 2; ++it) {
        const int sl = 2 * it + (G >> 1), h = G & 1;
        LAS char* p = sm + (16 * sl + 4 * h + q4) * 144 + 32 * w + 8 * p4;
        const v2u lo = trread(p), hi = trread(p + 8 * 144);
        const v4u o = {lo[0], lo[1], hi[0], hi[1]};
        const int s = 4 * fb + sl, dt = w >> 1;
        const int xr = (i16 & 3) + 8 * (i16 >> 2) + 4 * (w & 1);
        *(v4u*)(ws + WS_WT2 + ((size_t)((dg * 112 + s) * 2 + dt) * 64 + 32 * h + xr) * 16) = o;
      }
    }
  }
  STAMP(ws, 0, 7);
}

constexpr int TCOL = 528;
constexpr int NCOL = 63;
constexpr int TAB_B = NCOL * TCOL;
constexpr int KC_B = 4608 + 32;
constexpr int UC_B = 4224 + 128 - 128;
constexpr int CH_B = TAB_B + 4 * KC_B + 2 * UC_B + 4096;
constexpr int OFF_K = TAB_B, OFF_U = OFF_K + 4 * KC_B, OFF_X = OFF_U + 2 * UC_B;
static_assert(TAB_B % 16 == 0 && KC_B % 16 == 0 && UC_B % 16 == 0 && CH_B % 16 == 0, "align");
constexpr int CONV_LDS = 2 * CH_B;

struct cf { float re, im; };
DEV cf cmul(cf a, cf b) { cf r; r.re = a.re * b.re - a.im * b.im; r.im = a.re * b.im + a.im * b.re; return r; }
DEV cf cexp_p(float ar, float ai, float p) {
  const float e = __builtin_amdgcn_exp2f(ar * p * 1.44269504088896340736f);
  const float xx = ai * p;
  const float chi = 0.15915494309189533577f, clo = 6.4206382e-09f;
  const float th = xx * chi;
  float tl = __builtin_fmaf(xx, chi, -th);
  tl = __builtin_fmaf(xx, clo, tl);
  const float fr = (th - __builtin_rintf(th)) + tl;
  cf r; r.re = e * __builtin_amdgcn_cosf(fr); r.im = e * __builtin_amdgcn_sinf(fr);
  return r;
}

template <int C0, int C1, int C2, int C3>
DEV void conv_main(LAS char* ub, LAS char* kb, v4f (&acc)[4]) {
  constexpr int QN = C3 + 1;
#pragma unroll 1
  for (int r = 0; r < 8; ++r) {
    v8h U[QN], K[QN];
#pragma unroll
    for (int q = 0; q < QN; ++q) U[q] = *(LAS v8h*)(ub + 64 * r + 512 * q);
#pragma unroll
    for (int i = 0; i < QN; ++i) K[i] = *(LAS v8h*)(kb + 64 * r - 512 * i);
#pragma unroll
    for (int q = 0; q < QN; ++q) {
      if (q <= C3) acc[3] = __builtin_amdgcn_mfma_f32_16x16x32_f16(U[q], K[C3 - q], acc[3], 0, 0, 0);
      if (q <= C2) acc[2] = __builtin_amdgcn_mfma_f32_16x16x32_f16(U[q], K[(C2 - q) < 0 ? 0 : (C2 - q)], acc[2], 0, 0, 0);
      if (q <= C1) acc[1] = __builtin_amdgcn_mfma_f32_16x16x32_f16(U[q], K[(C1 - q) < 0 ? 0 : (C1 - q)], acc[1], 0, 0, 0);
      if (q <= C0) acc[0] = __builtin_amdgcn_mfma_f32_16x16x32_f16(U[q], K[(C0 - q) < 0 ? 0 : (C0 - q)], acc[0], 0, 0, 0);
    }
  }
}

__global__ __launch_bounds__(256) void k_conv(const u16* __restrict__ x, const u16* __restrict__ lar_, const u16* __restrict__ aim_,
                                              const u16* __restrict__ cre_, const u16* __restrict__ cim_, const u16* __restrict__ ldt_,
                                              const u16* __restrict__ dsk_, char* __restrict__ ws) {
  extern __shared__ __attribute__((aligned(16))) char smx[];
  LAS char* sm = (LAS char*)smx;
  const int t = threadIdx.x, lane = t & 63, w = __builtin_amdgcn_readfirstlane(t >> 6);
  STAMP(ws, 1, 0);
  const int pr = (blockIdx.x & 7) * 32 + (blockIdx.x >> 3);
  const int cw = w >> 1, nh = w & 1, ch = 2 * pr + cw;
  LAS char* cb = sm + cw * CH_B;
  const int n = 64 * nh + lane;
  const u32 r_lar = lar_[ch * NS + n], r_aim = aim_[ch * NS + n], r_cre = cre_[ch * NS + n], r_cim = cim_[ch * NS + n];
  const u32 r_cre2 = cre_[ch * NS + (n ^ 64)], r_cim2 = cim_[ch * NS + (n ^ 64)];
  const u32 r_ldt = ldt_[ch], r_dsk = dsk_[ch];
  u32 xr[8];
#pragma unroll
  for (int i = 0; i < 8; ++i) xr[i] = *(const u32*)(x + (size_t)(8 * t + i) * D + 2 * pr);
  v4u st[2];
#pragma unroll
  for (int i = 0; i < 2; ++i) st[i] = *(const v4u*)(ws + WS_STATS + t * 32 + i * 16);
  {
    const v4u z = {0, 0, 0, 0};
    const int tt = t & 127;
    LAS char* c2 = sm + (t >> 7) * CH_B;
    for (int i = tt; i < 132; i += 128) *(LAS v4u*)(c2 + OFF_K + (i / 33) * KC_B + (255 + i % 33) * 16) = z;
    if (tt < 16) *(LAS v4u*)(c2 + OFF_U + (tt >> 3) * UC_B + (tt & 7) * 16) = z;
  }
  const float dt = rbf(expf(bf2f(r_ldt)));
  const float ar = -rbf(expf(bf2f(r_lar))) * dt, ai = bf2f(r_aim) * dt;
  float cmax = fmaxf(fmaxf(fabsf(bf2f(r_cre)), fabsf(bf2f(r_cim))), fmaxf(fabsf(bf2f(r_cre2)), fabsf(bf2f(r_cim2))));
  cmax = wave_max(cmax);
  int ex = (int)((__float_as_uint(cmax) >> 23) & 255);
  ex = ex < 8 ? 8 : (ex > 250 ? 250 : ex);
  const float scl = __uint_as_float((u32)(257 - ex) << 23);
  const float yscl = 2.0f * dt * __uint_as_float((u32)(ex - 3) << 23);
  {
    LAS char* tb = cb + n * 4;
#define PUTB(col, v) *(LAS u32*)(tb + (col) * TCOL) = pkh((v).re, (v).im)
    const cf z1 = cexp_p(ar, ai, 1.0f), z16 = cexp_p(ar, ai, 16.0f), z256 = cexp_p(ar, ai, 256.0f);
    cf a = {bf2f(r_cre) * scl, bf2f(r_cim) * scl};
#pragma unroll
    for (int i = 0; i < 16; ++i) {
      *(LAS u32*)(tb + i * TCOL) = pkh(a.re, -a.im);
      if (i < 15) a = cmul(a, z16);
    }
    cf zp[17];
    zp[0] = {1.0f, 0.0f}; zp[1] = z1; zp[16] = z16;
#pragma unroll
    for (int i = 2; i < 16; ++i) zp[i] = (i & 1) ? cmul(zp[i - 1], z1) : cmul(zp[i / 2], zp[i / 2]);
#pragma unroll
    for (int i = 0; i < 16; ++i) PUTB(16 + i, zp[i]);
    cf zb[8];
    zb[1] = z256; zb[2] = cmul(z256, z256); zb[3] = cmul(zb[2], z256); zb[4] = cmul(zb[2], zb[2]);
    zb[5] = cmul(zb[4], z256); zb[6] = cmul(zb[4], zb[2]); zb[7] = cmul(zb[4], zb[3]);
    PUTB(32, zb[1]);
#pragma unroll
    for (int i = 1; i < 9; ++i) { const cf v = cmul(zb[1], zp[2 * i]); PUTB(32 + i, v); }
#pragma unroll
    for (int m = 2; m < 4; ++m) {
      PUTB(41 + 5 * (m - 2), zb[m]);
#pragma unroll
      for (int i = 1; i < 5; ++i) { const cf v = cmul(zb[m], zp[4 * i]); PUTB(41 + 5 * (m - 2) + i, v); }
    }
#pragma unroll
    for (int m = 4; m < 8; ++m) {
      PUTB(51 + 3 * (m - 4), zb[m]);
#pragma unroll
      for (int i = 1; i < 3; ++i) { const cf v = cmul(zb[m], zp[8 * i]); PUTB(51 + 3 * (m - 4) + i, v); }
    }
#undef PUTB
  }
  __syncthreads();
  STAMP(ws, 1, 1);
  {
    const int i16 = lane & 15, g = lane >> 4;
    v8h Bf[8];
#pragma unroll
    for (int ks = 0; ks < 8; ++ks) Bf[ks] = *(LAS v8h*)(cb + i16 * TCOL + ks * 64 + g * 16);
    const int j = i16;
    const int c2 = (j + ((j >> 1) & 1)) >> 1;
    const int c4 = (j + 1 + ((j >> 2) & 1)) >> 2;
    const int c8 = (j + 3 + ((j >> 3) & 1)) >> 3;
#pragma unroll
    for (int mm = 0; mm < 4; ++mm) {
      const int m = 2 * mm + nh;
      int col;
      if (mm == 0) col = nh ? 32 + c2 : 16 + j;
      else if (mm == 1) col = 41 + 5 * nh + c4;
      else col = 51 + 3 * (m - 4) + c8;
      LAS char* ap = cb + col * TCOL + g * 16;
      v4f d = {0, 0, 0, 0};
#pragma unroll
      for (int ks = 0; ks < 8; ++ks) d = __builtin_amdgcn_mfma_f32_16x16x32_f16(*(LAS v8h*)(ap + ks * 64), Bf[ks], d, 0, 0, 0);
      const u32 p0 = pkh(d[3], d[2]), p1 = pkh(d[1], d[0]);
      const int l0 = 256 * m + 16 * i16 + 4 * g;
#pragma unroll
      for (int e = 0; e < 4; ++e) {
        const int xk = 2041 + 2 * e - (l0 + 3);
        LAS char* kp = cb + OFF_K + e * KC_B + 16 + xk * 2;
        if (e & 1) { *(LAS v2u*)kp = v2u{p0, p1}; }
        else { *(LAS u32*)kp = p0; *(LAS u32*)(kp + 4) = p1; }
      }
    }
  }
  STAMP(ws, 1, 2);
  {
    float mm[8], rs[8];
#pragma unroll
    for (int i = 0; i < 8; ++i) { const u32 s = st[i >> 2][i & 3]; mm[i] = bflo(s); rs[i] = __builtin_amdgcn_rcpf(bfhi(s)); }
#pragma unroll
    for (int c = 0; c < 2; ++c) {
      float u[8];
#pragma unroll
      for (int i = 0; i < 8; ++i) {
        const float xv = c ? bfhi(xr[i]) : bflo(xr[i]);
        u[i] = rbf(rbf(xv - mm[i]) * rs[i]);
      }
      LAS char* c2 = sm + c * CH_B;
      *(LAS v4u*)(c2 + OFF_U + UC_B + (8 * t + 56) * 2) = v4u{pkh(u[0], u[1]), pkh(u[2], u[3]), pkh(u[4], u[5]), pkh(u[6], u[7])};
      LAS char* p0 = c2 + OFF_U + (8 * t + 57) * 2;
      const u32 e0 = pkh(u[0], u[7]);
      *(LAS u16*)p0 = (u16)e0;
      *(LAS u32*)(p0 + 2) = pkh(u[1], u[2]);
      *(LAS u32*)(p0 + 6) = pkh(u[3], u[4]);
      *(LAS u32*)(p0 + 10) = pkh(u[5], u[6]);
      *(LAS u16*)(p0 + 14) = (u16)(e0 >> 16);
      v4u xs;
#pragma unroll
      for (int i = 0; i < 4; ++i)
        xs[i] = c ? __builtin_amdgcn_perm(xr[2 * i + 1], xr[2 * i], 0x07060302u) : __builtin_amdgcn_perm(xr[2 * i + 1], xr[2 * i], 0x05040100u);
      *(LAS v4u*)(c2 + OFF_X + t * 16) = xs;
    }
  }
  __syncthreads();
  STAMP(ws, 1, 3);
  v4f acc[4] = {{0, 0, 0, 0}, {0, 0, 0, 0}, {0, 0, 0, 0}, {0, 0, 0, 0}};
  const int i16 = lane & 15, g = lane >> 4;
  {
    LAS char* ub = cb + OFF_U + (i16 & 1) * UC_B + ((i16 >> 1) + g) * 16;
    LAS char* kb = cb + OFF_K + (i16 & 3) * KC_B + 16 + (248 - 8 * (i16 >> 2) + g) * 16;
    if (nh == 0) conv_main<0, 3, 4, 7>(ub, kb, acc);
    else conv_main<1, 2, 5, 6>(ub, kb, acc);
  }
  STAMP(ws, 1, 4);
  {
    const float dsk = bf2f(r_dsk);
    u16* hT = (u16*)(ws + WS_HT) + (size_t)ch * L;
#pragma unroll
    for (int a = 0; a < 4; ++a) {
      const int c = nh == 0 ? (a == 0 ? 0 : a == 1 ? 3 : a == 2 ? 4 : 7) : (a == 0 ? 1 : a == 1 ? 2 : a == 2 ? 5 : 6);
#pragma unroll
      for (int hh = 0; hh < 2; ++hh) {
        const int l = 256 * c + 8 * (2 * g + hh) + 2 * (i16 & 3) + 64 * (i16 >> 2);
        const v2h up = *(LAS v2h*)(cb + OFF_U + UC_B + (l + 56) * 2);
        LAS u32* xp = (LAS u32*)(cb + OFF_X + l * 2);
        const u32 xv = *xp;
        const u32 yb = pkbf(acc[a][2 * hh] * yscl, acc[a][2 * hh + 1] * yscl);
        const u32 sk = pkbf((float)up[0] * dsk, (float)up[1] * dsk);
        const u32 zz = pkbf(bflo(yb) + bflo(sk), bfhi(yb) + bfhi(sk));
        *xp = pkbf(bflo(xv) + bflo(zz), bfhi(xv) + bfhi(zz));
      }
    }
    WAITL(0);
#pragma unroll
    for (int i = 0; i < 2; ++i) {
      const int a = 2 * i + (lane >> 5);
      const int c = nh == 0 ? (a == 0 ? 0 : a == 1 ? 3 : a == 2 ? 4 : 7) : (a == 0 ? 1 : a == 1 ? 2 : a == 2 ? 5 : 6);
      const int l = 256 * c + (lane & 31) * 8;
      *(v4u*)(hT + l) = *(LAS v4u*)(cb + OFF_X + l * 2);
    }
  }
  STAMP(ws, 1, 7);
}

constexpr int G1_SLOT = 16384 + 28672;
constexpr int G1_MISC = 3 * G1_SLOT;
constexpr int G1_LDS = G1_MISC + 4096 + 1024 + 1024;

DEV v16f mfma32(v4u a, v4u b, v16f c) {
  return __builtin_amdgcn_mfma_f32_32x32x16_bf16(__builtin_bit_cast(v8bf, a), __builtin_bit_cast(v8bf, b), c, 0, 0, 0);
}

__global__ __launch_bounds__(256) void k_gemm1(char* __restrict__ ws) {
  extern __shared__ __attribute__((aligned(16))) char smx[];
  LAS char* sm = (LAS char*)smx;
  const int t = threadIdx.x, lane = t & 63, w = __builtin_amdgcn_readfirstlane(t >> 6);
  STAMP(ws, 2, 0);
  const int xcd = blockIdx.x & 7, bi = blockIdx.x >> 3;
  const int mt = 8 * (xcd & 1) + (bi & 7), j = 4 * (xcd >> 1) + (bi >> 3);
  const int tw = w >> 1, fw = w & 1;
  const char* gtok = ws + WS_HT + (size_t)(16 * w + (lane >> 4)) * 4096 + mt * 256 + (((lane & 15) ^ (4 * (lane >> 4))) * 16);
  const char* gw = ws + WS_WT1 + (size_t)j * (32 * 7 * 1024) + w * 7 * 1024 + lane * 16;
  auto stage = [&](int s, int slot) {
    LAS char* sb = sm + slot * G1_SLOT;
#pragma unroll
    for (int i = 0; i < 4; ++i) glds16(gtok + (size_t)(64 * s + 4 * i) * 4096, sb + (4 * w + i) * 1024);
#pragma unroll
    for (int i = 0; i < 7; ++i) glds16(gw + (size_t)s * 28672 + i * 1024, sb + 16384 + (7 * w + i) * 1024);
  };
  stage(0, 0);
  glds16(ws + WS_CSUM + j * 4096 + w * 1024 + lane * 16, sm + G1_MISC + w * 1024);
  stage(1, 1);
  const int G = lane >> 4, q4 = (lane >> 2) & 3, p4 = lane & 3;
  const int tA = 2 * tw + fw, tB = 2 * tw + 1 - fw;
  const int rowoff = (8 * (G >> 1) + q4) * 256 + 8 * (p4 & 1);
  const int offA = rowoff + (((4 * tA + 2 * (G & 1) + (p4 >> 1)) ^ (4 * q4)) * 16);
  const int offB = rowoff + (((4 * tB + 2 * (G & 1) + (p4 >> 1)) ^ (4 * q4)) * 16);
  const int offW = 16384 + fw * 3072 + lane * 16, offW6 = 16384 + 6144 + lane * 16;
  v16f accA[4], accB[3];
#pragma unroll
  for (int i = 0; i < 4; ++i) accA[i] = (v16f)(0.0f);
#pragma unroll
  for (int i = 0; i < 3; ++i) accB[i] = (v16f)(0.0f);
  float sum = 0, ssq = 0;
  v4u fa[2], fb[2], fwt[2][4];
  auto ldfr = [&](int buf, LAS char* sb, int ks) {
    const v2u a0 = trread(sb + offA + ks * 4096), a1 = trread(sb + offA + ks * 4096 + 1024);
    const v2u b0 = trread(sb + offB + ks * 4096), b1 = trread(sb + offB + ks * 4096 + 1024);
    fa[buf] = v4u{a0[0], a0[1], a1[0], a1[1]};
    fb[buf] = v4u{b0[0], b0[1], b1[0], b1[1]};
#pragma unroll
    for (int i = 0; i < 3; ++i) fwt[buf][i] = *(LAS v4u*)(sb + offW + ks * 7168 + i * 1024);
    fwt[buf][3] = *(LAS v4u*)(sb + offW6 + ks * 7168);
  };
  auto mm = [&](int buf) {
#pragma unroll
    for (int i = 0; i < 3; ++i) {
      accA[i] = mfma32(fwt[buf][i], fa[buf], accA[i]);
      accB[i] = mfma32(fwt[buf][i], fb[buf], accB[i]);
    }
    accA[3] = mfma32(fwt[buf][3], fa[buf], accA[3]);
#pragma unroll
    for (int e = 0; e < 4; ++e) { sum = dot2bf(fa[buf][e], 0x3f803f80u, sum); ssq = dot2bf(fa[buf][e], fa[buf][e], ssq); }
  };
  WAITV(12);
  BAR();
  STAMP(ws, 2, 1);
  stage(2, 2);
  ldfr(0, sm, 0);
  int slot = 0;
#pragma unroll 1
  for (int s = 0; s < 8; ++s) {
    LAS char* sb = sm + slot * G1_SLOT;
    ldfr(1, sb, 1); mm(0);
    ldfr(0, sb, 2); mm(1);
    ldfr(1, sb, 3); mm(0);
    const int ns = slot == 2 ? 0 : slot + 1;
    if (s < 7) {
      if (s < 6) WAITV(11); else WAITV(0);
      WAITL(0);
      BAR();
      if (s < 5) stage(s + 3, slot);
      ldfr(0, sm + ns * G1_SLOT, 0);
    }
    mm(1);
    slot = ns;
  }
  STAMP(ws, 2, 2);
  sum += __shfl_xor(sum, 32);
  ssq += __shfl_xor(ssq, 32);
  {
    const float m = sum * (1.0f / D);
    const float var = fmaxf(ssq * (1.0f / D) - m * m, 0.0f);
    const float sd = rbf(__builtin_sqrtf(rbf(rbf(var) + 1e-5f)));
    const float r = 1.0f / sd;
    if (lane < 32) *(LAS v2f*)(sm + G1_MISC + 5120 + (tA * 32 + lane) * 8) = v2f{rbf(m), r};
    if (t < 224) {
      LAS float* cp = (LAS float*)(sm + G1_MISC) + t;
      *(LAS float*)(sm + G1_MISC + 4096 + t * 4) = (cp[0] + cp[224]) + (cp[448] + cp[672]);
    }
  }
  __syncthreads();
  const int h = lane >> 5;
  auto fin = [&](const v16f& a, int tt, int pt) {
    const v2f mr = *(LAS v2f*)(sm + G1_MISC + 5120 + (tt * 32 + (lane & 31)) * 8);
    LAS char* cp = sm + G1_MISC + 4096 + (pt * 16 + 4 * h) * 4;
    const v4f cg0 = *(LAS v4f*)cp, cg1 = *(LAS v4f*)(cp + 32), cu0 = *(LAS v4f*)(cp + 448), cu1 = *(LAS v4f*)(cp + 480);
    float o[8];
#pragma unroll
    for (int e = 0; e < 8; ++e) {
      const float cg = e < 4 ? cg0[e & 3] : cg1[e & 3], cu = e < 4 ? cu0[e & 3] : cu1[e & 3];
      const float g = (a[e] - mr[0] * cg) * mr[1], u = (a[8 + e] - mr[0] * cu) * mr[1];
      const float sg = g * __builtin_amdgcn_rcpf(1.0f + __builtin_amdgcn_exp2f(g * -1.44269504088896340736f));
      o[e] = sg * u;
    }
    *(v4u*)(ws + WS_ACT + ((size_t)((mt * 4 + tt) * 112 + j * 7 + pt) * 64 + lane) * 16) =
        v4u{pkbf(o[0], o[1]), pkbf(o[2], o[3]), pkbf(o[4], o[5]), pkbf(o[6], o[7])};
  };
#pragma unroll
  for (int i = 0; i < 3; ++i) { fin(accA[i], tA, 3 * fw + i); fin(accB[i], tB, 3 * fw + i); }
  fin(accA[3], tA, 6);
  STAMP(ws, 2, 7);
}

constexpr int G2_LDS = 65536 + 8192;
__global__ __launch_bounds__(256) void k_gemm2(char* __restrict__ ws, u16* __restrict__ out) {
  extern __shared__ __attribute__((aligned(16))) char smx[];
  LAS char* sm = (LAS char*)smx;
  const int t = threadIdx.x, lane = t & 63, w = __builtin_amdgcn_readfirstlane(t >> 6);
  STAMP(ws, 3, 0);
  const int xcd = blockIdx.x & 7, bi = blockIdx.x >> 3;
  const int tm = 8 * (xcd >> 1) + (bi & 7), dg = 4 * (xcd & 1) + (bi >> 3);
  const v4u* pa = (const v4u*)(ws + WS_ACT) + ((size_t)(2 * tm) * 112 + 28 * w) * 64 + lane;
  const v4u* pw = (const v4u*)(ws + WS_WT2) + ((size_t)(dg * 112 + 28 * w) * 2) * 64 + lane;
  v4u hr[2];
#pragma unroll
  for (int i = 0; i < 2; ++i) hr[i] = *(const v4u*)(ws + WS_HT + (size_t)(64 * dg + (t >> 3) + 32 * i) * 4096 + tm * 128 + (t & 7) * 16);
  v16f acc[2][2];
#pragma unroll
  for (int i = 0; i < 4; ++i) acc[i >> 1][i & 1] = (v16f)(0.0f);
  constexpr int PF = 12;
  v4u ra[PF][2], rw[PF][2];
#pragma unroll
  for (int s = 0; s < PF; ++s) {
    rw[s][0] = pw[s * 128]; rw[s][1] = pw[s * 128 + 64];
    ra[s][0] = pa[s * 64]; ra[s][1] = pa[(112 + s) * 64];
  }
#pragma unroll
  for (int s = 0; s < 28; ++s) {
    const int b = s % PF;
#pragma unroll
    for (int dt = 0; dt < 2; ++dt)
#pragma unroll
      for (int tt = 0; tt < 2; ++tt) acc[dt][tt] = mfma32(rw[b][dt], ra[b][tt], acc[dt][tt]);
    if (s + PF < 28) {
      rw[b][0] = pw[(s + PF) * 128]; rw[b][1] = pw[(s + PF) * 128 + 64];
      ra[b][0] = pa[(s + PF) * 64]; ra[b][1] = pa[(112 + s + PF) * 64];
    }
    __builtin_amdgcn_sched_barrier(0);
  }
  STAMP(ws, 3, 1);
#pragma unroll
  for (int i = 0; i < 2; ++i) *(LAS v4u*)(sm + 65536 + ((t >> 3) + 32 * i) * 128 + (((t & 7) ^ (4 * ((t >> 4) & 1))) * 16)) = hr[i];
#pragma unroll
  for (int k = 0; k < 4; ++k) {
    if (k != w) {
#pragma unroll
      for (int e = 0; e < 4; ++e) {
        const v16f& a = acc[k >> 1][k & 1];
        *(LAS v4f*)(sm + ((k * 4 + w) * 4 + e) * 1024 + lane * 16) = v4f{a[4 * e], a[4 * e + 1], a[4 * e + 2], a[4 * e + 3]};
      }
    }
  }
  __syncthreads();
  {
    v16f a = w == 0 ? acc[0][0] : w == 1 ? acc[0][1] : w == 2 ? acc[1][0] : acc[1][1];
#pragma unroll
    for (int k = 0; k < 4; ++k) {
      if (k != w) {
#pragma unroll
        for (int e = 0; e < 4; ++e) {
          const v4f p = *(LAS v4f*)(sm + ((w * 4 + k) * 4 + e) * 1024 + lane * 16);
#pragma unroll
          for (int c = 0; c < 4; ++c) a[4 * e + c] += p[c];
        }
      }
    }
    const int dt = w >> 1, tt = w & 1, G = lane >> 4, q4 = (lane >> 2) & 3, p4 = lane & 3;
    u32 o[8];
#pragma unroll
    for (int e = 0; e < 4; ++e) {
      const int row = 32 * dt + 16 * (G >> 1) + 4 * e + q4;
      const int ck = (4 * tt + 2 * (G & 1) + (p4 >> 1)) ^ (4 * ((row >> 1) & 1));
      const v2u hv = trread(sm + 65536 + row * 128 + ck * 16 + 8 * (p4 & 1));
      const u32 f0 = pkbf(a[4 * e], a[4 * e + 1]), f1 = pkbf(a[4 * e + 2], a[4 * e + 3]);
      o[2 * e] = pkbf(bflo(hv[0]) + bflo(f0), bfhi(hv[0]) + bfhi(f0));
      o[2 * e + 1] = pkbf(bflo(hv[1]) + bflo(f1), bfhi(hv[1]) + bfhi(f1));
    }
    u16* op = out + (size_t)(64 * tm + 32 * tt + (lane & 31)) * D + 64 * dg + 32 * dt + 16 * (lane >> 5);
    *(v4u*)op = v4u{o[0], o[1], o[2], o[3]};
    *(v4u*)(op + 8) = v4u{o[4], o[5], o[6], o[7]};
  }
  STAMP(ws, 3, 7);
}

extern "C" void kernel_launch(void* const* d_in, const int* in_sizes, int n_in, void* d_out, int out_size, void* d_ws, size_t ws_size,
                              hipStream_t stream) {
  const u16* x = (const u16*)d_in[0];
  char* ws = (char*)d_ws;
  (void)hipFuncSetAttribute((const void*)k_conv, hipFuncAttributeMaxDynamicSharedMemorySize, CONV_LDS);
  (void)hipFuncSetAttribute((const void*)k_gemm1, hipFuncAttributeMaxDynamicSharedMemorySize, G1_LDS);
  (void)hipFuncSetAttribute((const void*)k_gemm2, hipFuncAttributeMaxDynamicSharedMemorySize, G2_LDS);
  k_prep<<<480, 256, 0, stream>>>(x, (const u16*)d_in[8], (const u16*)d_in[9], (const u16*)d_in[10], ws);
  k_conv<<<256, 256, CONV_LDS, stream>>>(x, (const u16*)d_in[1], (const u16*)d_in[2], (const u16*)d_in[3], (const u16*)d_in[4],
                                         (const u16*)d_in[5], (const u16*)d_in[6], ws);
  k_gemm1<<<256, 256, G1_LDS, stream>>>(ws);
  k_gemm2<<<256, 256, G2_LDS, stream>>>(ws, (u16*)d_out);
}
```

```cpp
#include <hip/hip_runtime.h>
#include <hip/hip_bf16.h>
#include <stdint.h>

typedef unsigned short u16;
typedef unsigned int u32;
typedef short v4s __attribute__((ext_vector_type(4)));
typedef short v8s __attribute__((ext_vector_type(8)));
typedef __bf16 v2bf __attribute__((ext_vector_type(2)));
typedef __bf16 v8bf __attribute__((ext_vector_type(8)));
typedef _Float16 v2h __attribute__((ext_vector_type(2)));
typedef _Float16 v8h __attribute__((ext_vector_type(8)));
typedef float v2f __attribute__((ext_vector_type(2)));
typedef float v4f __attribute__((ext_vector_type(4)));
typedef float v16f __attribute__((ext_vector_type(16)));
typedef u32 v2u __attribute__((ext_vector_type(2)));
typedef u32 v3u __attribute__((ext_vector_type(3)));
typedef u32 v4u __attribute__((ext_vector_type(4)));
#define LAS __attribute__((address_space(3)))
#define GAS __attribute__((address_space(1)))
#define DEV __device__ __forceinline__

#define R4(x) x x x x
#define R16(x) R4(R4(x))
#define R64(x) R4(R16(x))
#define R256(x) R4(R64(x))
#define BLK "v_fma_f32 %0, %0, %0, %1\n v_fma_f32 %1, %1, %1, %2\n v_fma_f32 %2, %2, %2, %3\n v_fma_f32 %3, %3, %3, %0\n"
constexpr int L = 2048, D = 512, NS = 128, F = 1792;

constexpr size_t WS_STATS = 0;
constexpr size_t WS_CSUM  = 64 << 10;
constexpr size_t WS_HT    = 1 << 20;
constexpr size_t WS_WT1   = 4 << 20;
constexpr size_t WS_WT2   = 8 << 20;
constexpr size_t WS_ACT   = 12 << 20;
constexpr size_t WS_DBG   = 32 << 20;

DEV float bf2f(u32 b) { return __uint_as_float(b << 16); }
DEV float bflo(u32 p) { return __uint_as_float(p << 16); }
DEV float bfhi(u32 p) { return __uint_as_float(p & 0xffff0000u); }
DEV u32 pkbf(float a, float b) { v2f f = {a, b}; return __builtin_bit_cast(u32, __builtin_convertvector(f, v2bf)); }
DEV u32 pkh(float a, float b) { v2f f = {a, b}; return __builtin_bit_cast(u32, __builtin_convertvector(f, v2h)); }
DEV float rbf(float a) { return bflo(pkbf(a, a)); }
DEV float dot2bf(u32 a, u32 b, float c) { return __builtin_amdgcn_fdot2_f32_bf16(__builtin_bit_cast(v2bf, a), __builtin_bit_cast(v2bf, b), c, false); }
DEV v2u trread(LAS char* p) { return __builtin_bit_cast(v2u, __builtin_amdgcn_ds_read_tr16_b64_v4i16((LAS v4s*)p)); }
DEV void glds16(const void* g, LAS char* l) { __builtin_amdgcn_global_load_lds((const GAS void*)g, (LAS void*)l, 16, 0, 0); }
#define WAITV(n) asm volatile("s_waitcnt vmcnt(" #n ")" ::: "memory")
#define WAITL(n) asm volatile("s_waitcnt lgkmcnt(" #n ")" ::: "memory")
#define BAR() asm volatile("s_barrier" ::: "memory")

#define STAMP(ws, kid, slot) do {} while (0)

DEV float wave_sum(float v) {
#pragma unroll
  for (int o = 32; o; o >>= 1) v += __shfl_xor(v, o);
  return v;
}
DEV float wave_max(float v) {
#pragma unroll
  for (int o = 32; o; o >>= 1) v = fmaxf(v, __shfl_xor(v, o));
  return v;
}

__global__ __launch_bounds__(256) void k_prep(const u16* __restrict__ x, const u16* __restrict__ wg, const u16* __restrict__ wu,
                                              const u16* __restrict__ wd, char* __restrict__ ws) {
  __shared__ __attribute__((aligned(16))) char sm_[128 * 144];
  LAS char* sm = (LAS char*)sm_;
  const int t = threadIdx.x, lane = t & 63, w = t >> 6;
  STAMP(ws, 0, 0);
  if (blockIdx.x < 256) {
    const int row0 = blockIdx.x * 8 + w * 2;
    v4u a[2];
#pragma unroll
    for (int i = 0; i < 2; ++i) a[i] = *(const v4u*)(x + (size_t)(row0 + i) * D + lane * 8);
#pragma unroll
    for (int i = 0; i < 2; ++i) {
      float v[8];
#pragma unroll
      for (int j = 0; j < 4; ++j) { v[2 * j] = bflo(a[i][j]); v[2 * j + 1] = bfhi(a[i][j]); }
      float s = 0;
#pragma unroll
      for (int j = 0; j < 8; ++j) s += v[j];
      s = wave_sum(s);
      const float m = s * (1.0f / D);
      float q = 0;
#pragma unroll
      for (int j = 0; j < 8; ++j) { const float d = v[j] - m; q += d * d; }
      q = wave_sum(q);
      const float vb = rbf(q * (1.0f / D));
      const float sd = __builtin_sqrtf(rbf(vb + 1e-5f));
      if (lane == 0) ((u32*)(ws + WS_STATS))[row0 + i] = pkbf(m, sd);
    }
  } else {
    const int rb = blockIdx.x - 256;
    const int G = lane >> 4, q4 = (lane >> 2) & 3, p4 = lane & 3, i16 = lane & 15;
    const int mat = rb / 112, rem = rb % 112, cg = rem % 28, q = rem / 28;
    const u16* W = mat ? wu : wg;
    v4u a[4], b[2];
#pragma unroll
    for (int i = 0; i < 4; ++i) a[i] = *(const v4u*)(W + (size_t)(128 * q + (t >> 3) + 32 * i) * F + 64 * cg + (t & 7) * 8);
    const int dg = rb & 7, fb = rb >> 3;
#pragma unroll
    for (int i = 0; i < 2; ++i) b[i] = *(const v4u*)(wd + (size_t)(64 * fb + (t >> 3) + 32 * i) * D + 64 * dg + (t & 7) * 8);
#pragma unroll
    for (int i = 0; i < 4; ++i) *(LAS v4u*)(sm + ((t >> 3) + 32 * i) * 144 + (t & 7) * 16) = a[i];
    __syncthreads();
    {
      const int g16 = 4 * cg + w, j = g16 / 7, pt = g16 % 7;
      float cs = 0;
#pragma unroll
      for (int it = 0; it < 4; ++it) {
        const int ksl = 2 * it + (G >> 1), h = G & 1;
        LAS char* p = sm + (16 * ksl + 8 * h + q4) * 144 + 32 * w + 8 * p4;
        const v2u lo = trread(p), hi = trread(p + 4 * 144);
        const v4u o = {lo[0], lo[1], hi[0], hi[1]};
#pragma unroll
        for (int e = 0; e < 4; ++e) cs = dot2bf(o[e], 0x3f803f80u, cs);
        const int ks = 8 * q + ksl;
        *(v4u*)(ws + WS_WT1 + ((size_t)((j * 32 + ks) * 7 + pt) * 64 + 32 * h + 16 * mat + i16) * 16) = o;
      }
      cs += __shfl_xor(cs, 16);
      cs += __shfl_xor(cs, 32);
      if (lane < 16) ((float*)(ws + WS_CSUM))[j * 1024 + q * 224 + mat * 112 + pt * 16 + lane] = cs;
    }
    __syncthreads();
#pragma unroll
    for (int i = 0; i < 2; ++i) *(LAS v4u*)(sm + ((t >> 3) + 32 * i) * 144 + (t & 7) * 16) = b[i];
    __syncthreads();
    {
#pragma unroll
      for (int it = 0; it < 2; ++it) {
        const int sl = 2 * it + (G >> 1), h = G & 1;
        LAS char* p = sm + (16 * sl + 4 * h + q4) * 144 + 32 * w + 8 * p4;
        const v2u lo = trread(p), hi = trread(p + 8 * 144);
        const v4u o = {lo[0], lo[1], hi[0], hi[1]};
        const int s = 4 * fb + sl, dt = w >> 1;
        const int xr = (i16 & 3) + 8 * (i16 >> 2) + 4 * (w & 1);
        *(v4u*)(ws + WS_WT2 + ((size_t)((dg * 112 + s) * 2 + dt) * 64 + 32 * h + xr) * 16) = o;
      }
    }
  }
  STAMP(ws, 0, 7);
}

constexpr int TCOL = 528;
constexpr int NCOL = 63;
constexpr int TAB_B = NCOL * TCOL;
constexpr int KC_B = 4608 + 32;
constexpr int UC_B = 4224 + 128 - 128;
constexpr int CH_B = TAB_B + 4 * KC_B + 2 * UC_B + 4096;
constexpr int OFF_K = TAB_B, OFF_U = OFF_K + 4 * KC_B, OFF_X = OFF_U + 2 * UC_B;
static_assert(TAB_B % 16 == 0 && KC_B % 16 == 0 && UC_B % 16 == 0 && CH_B % 16 == 0, "align");
constexpr int CONV_LDS = 2 * CH_B;

struct cf { float re, im; };
DEV cf cmul(cf a, cf b) { cf r; r.re = a.re * b.re - a.im * b.im; r.im = a.re * b.im + a.im * b.re; return r; }
DEV cf cexp_p(float ar, float ai, float p) {
  const float e = __builtin_amdgcn_exp2f(ar * p * 1.44269504088896340736f);
  const float xx = ai * p;
  const float chi = 0.15915494309189533577f, clo = 6.4206382e-09f;
  const float th = xx * chi;
  float tl = __builtin_fmaf(xx, chi, -th);
  tl = __builtin_fmaf(xx, clo, tl);
  const float fr = (th - __builtin_rintf(th)) + tl;
  cf r; r.re = e * __builtin_amdgcn_cosf(fr); r.im = e * __builtin_amdgcn_sinf(fr);
  return r;
}

template <int C0, int C1, int C2, int C3>
DEV void conv_main(LAS char* ub, LAS char* kb, v4f (&acc)[4]) {
  constexpr int QN = C3 + 1;
#pragma unroll 1
  for (int r = 0; r < 8; ++r) {
    v8h U[QN], K[QN];
#pragma unroll
    for (int q = 0; q < QN; ++q) U[q] = *(LAS v8h*)(ub + 64 * r + 512 * q);
#pragma unroll
    for (int i = 0; i < QN; ++i) K[i] = *(LAS v8h*)(kb + 64 * r - 512 * i);
#pragma unroll
    for (int q = 0; q < QN; ++q) {
      if (q <= C3) acc[3] = __builtin_amdgcn_mfma_f32_16x16x32_f16(U[q], K[C3 - q], acc[3], 0, 0, 0);
      if (q <= C2) acc[2] = __builtin_amdgcn_mfma_f32_16x16x32_f16(U[q], K[(C2 - q) < 0 ? 0 : (C2 - q)], acc[2], 0, 0, 0);
      if (q <= C1) acc[1] = __builtin_amdgcn_mfma_f32_16x16x32_f16(U[q], K[(C1 - q) < 0 ? 0 : (C1 - q)], acc[1], 0, 0, 0);
      if (q <= C0) acc[0] = __builtin_amdgcn_mfma_f32_16x16x32_f16(U[q], K[(C0 - q) < 0 ? 0 : (C0 - q)], acc[0], 0, 0, 0);
    }
  }
}

__global__ __launch_bounds__(256) void k_conv(const u16* __restrict__ x, const u16* __restrict__ lar_, const u16* __restrict__ aim_,
                                              const u16* __restrict__ cre_, const u16* __restrict__ cim_, const u16* __restrict__ ldt_,
                                              const u16* __restrict__ dsk_, char* __restrict__ ws) {
  extern __shared__ __attribute__((aligned(16))) char smx[];
  LAS char* sm = (LAS char*)smx;
  const int t = threadIdx.x, lane = t & 63, w = __builtin_amdgcn_readfirstlane(t >> 6);
  STAMP(ws, 1, 0);
  const int pr = (blockIdx.x & 7) * 32 + (blockIdx.x >> 3);
  const int cw = w >> 1, nh = w & 1, ch = 2 * pr + cw;
  LAS char* cb = sm + cw * CH_B;
  const int n = 64 * nh + lane;
  const u32 r_lar = lar_[ch * NS + n], r_aim = aim_[ch * NS + n], r_cre = cre_[ch * NS + n], r_cim = cim_[ch * NS + n];
  const u32 r_cre2 = cre_[ch * NS + (n ^ 64)], r_cim2 = cim_[ch * NS + (n ^ 64)];
  const u32 r_ldt = ldt_[ch], r_dsk = dsk_[ch];
  u32 xr[8];
#pragma unroll
  for (int i = 0; i < 8; ++i) xr[i] = *(const u32*)(x + (size_t)(8 * t + i) * D + 2 * pr);
  v4u st[2];
#pragma unroll
  for (int i = 0; i < 2; ++i) st[i] = *(const v4u*)(ws + WS_STATS + t * 32 + i * 16);
  {
    const v4u z = {0, 0, 0, 0};
    const int tt = t & 127;
    LAS char* c2 = sm + (t >> 7) * CH_B;
    for (int i = tt; i < 132; i += 128) *(LAS v4u*)(c2 + OFF_K + (i / 33) * KC_B + (255 + i % 33) * 16) = z;
    if (tt < 16) *(LAS v4u*)(c2 + OFF_U + (tt >> 3) * UC_B + (tt & 7) * 16) = z;
  }
  const float dt = rbf(expf(bf2f(r_ldt)));
  const float ar = -rbf(expf(bf2f(r_lar))) * dt, ai = bf2f(r_aim) * dt;
  float cmax = fmaxf(fmaxf(fabsf(bf2f(r_cre)), fabsf(bf2f(r_cim))), fmaxf(fabsf(bf2f(r_cre2)), fabsf(bf2f(r_cim2))));
  cmax = wave_max(cmax);
  int ex = (int)((__float_as_uint(cmax) >> 23) & 255);
  ex = ex < 8 ? 8 : (ex > 250 ? 250 : ex);
  const float scl = __uint_as_float((u32)(257 - ex) << 23);
  const float yscl = 2.0f * dt * __uint_as_float((u32)(ex - 3) << 23);
  {
    LAS char* tb = cb + n * 4;
#define PUTB(col, v) *(LAS u32*)(tb + (col) * TCOL) = pkh((v).re, (v).im)
    const cf z1 = cexp_p(ar, ai, 1.0f), z16 = cexp_p(ar, ai, 16.0f), z256 = cexp_p(ar, ai, 256.0f);
    cf a = {bf2f(r_cre) * scl, bf2f(r_cim) * scl};
#pragma unroll
    for (int i = 0; i < 16; ++i) {
      *(LAS u32*)(tb + i * TCOL) = pkh(a.re, -a.im);
      if (i < 15) a = cmul(a, z16);
    }
    cf zp[17];
    zp[0] = {1.0f, 0.0f}; zp[1] = z1; zp[16] = z16;
#pragma unroll
    for (int i = 2; i < 16; ++i) zp[i] = (i & 1) ? cmul(zp[i - 1], z1) : cmul(zp[i / 2], zp[i / 2]);
#pragma unroll
    for (int i = 0; i < 16; ++i) PUTB(16 + i, zp[i]);
    cf zb[8];
    zb[1] = z256; zb[2] = cmul(z256, z256); zb[3] = cmul(zb[2], z256); zb[4] = cmul(zb[2], zb[2]);
    zb[5] = cmul(zb[4], z256); zb[6] = cmul(zb[4], zb[2]); zb[7] = cmul(zb[4], zb[3]);
    PUTB(32, zb[1]);
#pragma unroll
    for (int i = 1; i < 9; ++i) { const cf v = cmul(zb[1], zp[2 * i]); PUTB(32 + i, v); }
#pragma unroll
    for (int m = 2; m < 4; ++m) {
      PUTB(41 + 5 * (m - 2), zb[m]);
#pragma unroll
      for (int i = 1; i < 5; ++i) { const cf v = cmul(zb[m], zp[4 * i]); PUTB(41 + 5 * (m - 2) + i, v); }
    }
#pragma unroll
    for (int m = 4; m < 8; ++m) {
      PUTB(51 + 3 * (m - 4), zb[m]);
#pragma unroll
      for (int i = 1; i < 3; ++i) { const cf v = cmul(zb[m], zp[8 * i]); PUTB(51 + 3 * (m - 4) + i, v); }
    }
#undef PUTB
  }
  __syncthreads();
  STAMP(ws, 1, 1);
  {
    const int i16 = lane & 15, g = lane >> 4;
    v8h Bf[8];
#pragma unroll
    for (int ks = 0; ks < 8; ++ks) Bf[ks] = *(LAS v8h*)(cb + i16 * TCOL + ks * 64 + g * 16);
    const int j = i16;
    const int c2 = (j + ((j >> 1) & 1)) >> 1;
    const int c4 = (j + 1 + ((j >> 2) & 1)) >> 2;
    const int c8 = (j + 3 + ((j >> 3) & 1)) >> 3;
#pragma unroll
    for (int mm = 0; mm < 4; ++mm) {
      const int m = 2 * mm + nh;
      int col;
      if (mm == 0) col = nh ? 32 + c2 : 16 + j;
      else if (mm == 1) col = 41 + 5 * nh + c4;
      else col = 51 + 3 * (m - 4) + c8;
      LAS char* ap = cb + col * TCOL + g * 16;
      v4f d = {0, 0, 0, 0};
#pragma unroll
      for (int ks = 0; ks < 8; ++ks) d = __builtin_amdgcn_mfma_f32_16x16x32_f16(*(LAS v8h*)(ap + ks * 64), Bf[ks], d, 0, 0, 0);
      const u32 p0 = pkh(d[3], d[2]), p1 = pkh(d[1], d[0]);
      const int l0 = 256 * m + 16 * i16 + 4 * g;
#pragma unroll
      for (int e = 0; e < 4; ++e) {
        const int xk = 2041 + 2 * e - (l0 + 3);
        LAS char* kp = cb + OFF_K + e * KC_B + 16 + xk * 2;
        if (e & 1) { *(LAS v2u*)kp = v2u{p0, p1}; }
        else { *(LAS u32*)kp = p0; *(LAS u32*)(kp + 4) = p1; }
      }
    }
  }
  STAMP(ws, 1, 2);
  {
    float mm[8], rs[8];
#pragma unroll
    for (int i = 0; i < 8; ++i) { const u32 s = st[i >> 2][i & 3]; mm[i] = bflo(s); rs[i] = __builtin_amdgcn_rcpf(bfhi(s)); }
#pragma unroll
    for (int c = 0; c < 2; ++c) {
      float u[8];
#pragma unroll
      for (int i = 0; i < 8; ++i) {
        const float xv = c ? bfhi(xr[i]) : bflo(xr[i]);
        u[i] = rbf(rbf(xv - mm[i]) * rs[i]);
      }
      LAS char* c2 = sm + c * CH_B;
      *(LAS v4u*)(c2 + OFF_U + UC_B + (8 * t + 56) * 2) = v4u{pkh(u[0], u[1]), pkh(u[2], u[3]), pkh(u[4], u[5]), pkh(u[6], u[7])};
      LAS char* p0 = c2 + OFF_U + (8 * t + 57) * 2;
      const u32 e0 = pkh(u[0], u[7]);
      *(LAS u16*)p0 = (u16)e0;
      *(LAS u32*)(p0 + 2) = pkh(u[1], u[2]);
      *(LAS u32*)(p0 + 6) = pkh(u[3], u[4]);
      *(LAS u32*)(p0 + 10) = pkh(u[5], u[6]);
      *(LAS u16*)(p0 + 14) = (u16)(e0 >> 16);
      v4u xs;
#pragma unroll
      for (int i = 0; i < 4; ++i)
        xs[i] = c ? __builtin_amdgcn_perm(xr[2 * i + 1], xr[2 * i], 0x07060302u) : __builtin_amdgcn_perm(xr[2 * i + 1], xr[2 * i], 0x05040100u);
      *(LAS v4u*)(c2 + OFF_X + t * 16) = xs;
    }
  }
  __syncthreads();
  STAMP(ws, 1, 3);
  v4f acc[4] = {{0, 0, 0, 0}, {0, 0, 0, 0}, {0, 0, 0, 0}, {0, 0, 0, 0}};
  const int i16 = lane & 15, g = lane >> 4;
  {
    LAS char* ub = cb + OFF_U + (i16 & 1) * UC_B + ((i16 >> 1) + g) * 16;
    LAS char* kb = cb + OFF_K + (i16 & 3) * KC_B + 16 + (248 - 8 * (i16 >> 2) + g) * 16;
    if (nh == 0) conv_main<0, 3, 4, 7>(ub, kb, acc);
    else conv_main<1, 2, 5, 6>(ub, kb, acc);
  }
  STAMP(ws, 1, 4);
  {
    const float dsk = bf2f(r_dsk);
    u16* hT = (u16*)(ws + WS_HT) + (size_t)ch * L;
#pragma unroll
    for (int a = 0; a < 4; ++a) {
      const int c = nh == 0 ? (a == 0 ? 0 : a == 1 ? 3 : a == 2 ? 4 : 7) : (a == 0 ? 1 : a == 1 ? 2 : a == 2 ? 5 : 6);
#pragma unroll
      for (int hh = 0; hh < 2; ++hh) {
        const int l = 256 * c + 8 * (2 * g + hh) + 2 * (i16 & 3) + 64 * (i16 >> 2);
        const v2h up = *(LAS v2h*)(cb + OFF_U + UC_B + (l + 56) * 2);
        LAS u32* xp = (LAS u32*)(cb + OFF_X + l * 2);
        const u32 xv = *xp;
        const u32 yb = pkbf(acc[a][2 * hh] * yscl, acc[a][2 * hh + 1] * yscl);
        const u32 sk = pkbf((float)up[0] * dsk, (float)up[1] * dsk);
        const u32 zz = pkbf(bflo(yb) + bflo(sk), bfhi(yb) + bfhi(sk));
        *xp = pkbf(bflo(xv) + bflo(zz), bfhi(xv) + bfhi(zz));
      }
    }
    WAITL(0);
#pragma unroll
    for (int i = 0; i < 2; ++i) {
      const int a = 2 * i + (lane >> 5);
      const int c = nh == 0 ? (a == 0 ? 0 : a == 1 ? 3 : a == 2 ? 4 : 7) : (a == 0 ? 1 : a == 1 ? 2 : a == 2 ? 5 : 6);
      const int l = 256 * c + (lane & 31) * 8;
      *(v4u*)(hT + l) = *(LAS v4u*)(cb + OFF_X + l * 2);
    }
  }
  STAMP(ws, 1, 7);
}

constexpr int G1_SLOT = 16384 + 28672;
constexpr int G1_MISC = 3 * G1_SLOT;
constexpr int G1_LDS = G1_MISC + 4096 + 1024 + 1024;

DEV v16f mfma32(v4u a, v4u b, v16f c) {
  return __builtin_amdgcn_mfma_f32_32x32x16_bf16(__builtin_bit_cast(v8bf, a), __builtin_bit_cast(v8bf, b), c, 0, 0, 0);
}

__global__ __launch_bounds__(256) void k_gemm1(char* __restrict__ ws) {
  extern __shared__ __attribute__((aligned(16))) char smx[];
  LAS char* sm = (LAS char*)smx;
  const int t = threadIdx.x, lane = t & 63, w = __builtin_amdgcn_readfirstlane(t >> 6);
  STAMP(ws, 2, 0);
  const int xcd = blockIdx.x & 7, bi = blockIdx.x >> 3;
  const int mt = 8 * (xcd & 1) + (bi & 7), j = 4 * (xcd >> 1) + (bi >> 3);
  const int tw = w >> 1, fw = w & 1;
  const char* gtok = ws + WS_HT + (size_t)(16 * w + (lane >> 4)) * 4096 + mt * 256 + (((lane & 15) ^ (4 * (lane >> 4))) * 16);
  const char* gw = ws + WS_WT1 + (size_t)j * (32 * 7 * 1024) + w * 7 * 1024 + lane * 16;
  auto stage = [&](int s, int slot) {
    LAS char* sb = sm + slot * G1_SLOT;
#pragma unroll
    for (int i = 0; i < 4; ++i) glds16(gtok + (size_t)(64 * s + 4 * i) * 4096, sb + (4 * w + i) * 1024);
#pragma unroll
    for (int i = 0; i < 7; ++i) glds16(gw + (size_t)s * 28672 + i * 1024, sb + 16384 + (7 * w + i) * 1024);
  };
  stage(0, 0);
  glds16(ws + WS_CSUM + j * 4096 + w * 1024 + lane * 16, sm + G1_MISC + w * 1024);
  stage(1, 1);
  const int G = lane >> 4, q4 = (lane >> 2) & 3, p4 = lane & 3;
  const int tA = 2 * tw + fw, tB = 2 * tw + 1 - fw;
  const int rowoff = (8 * (G >> 1) + q4) * 256 + 8 * (p4 & 1);
  const int offA = rowoff + (((4 * tA + 2 * (G & 1) + (p4 >> 1)) ^ (4 * q4)) * 16);
  const int offB = rowoff + (((4 * tB + 2 * (G & 1) + (p4 >> 1)) ^ (4 * q4)) * 16);
  const int offW = 16384 + fw * 3072 + lane * 16, offW6 = 16384 + 6144 + lane * 16;
  v16f accA[4], accB[3];
#pragma unroll
  for (int i = 0; i < 4; ++i) accA[i] = (v16f)(0.0f);
#pragma unroll
  for (int i = 0; i < 3; ++i) accB[i] = (v16f)(0.0f);
  float sum = 0, ssq = 0;
  v4u fa[2], fb[2], fwt[2][4];
  auto ldfr = [&](int buf, LAS char* sb, int ks) {
    const v2u a0 = trread(sb + offA + ks * 4096), a1 = trread(sb + offA + ks * 4096 + 1024);
    const v2u b0 = trread(sb + offB + ks * 4096), b1 = trread(sb + offB + ks * 4096 + 1024);
    fa[buf] = v4u{a0[0], a0[1], a1[0], a1[1]};
    fb[buf] = v4u{b0[0], b0[1], b1[0], b1[1]};
#pragma unroll
    for (int i = 0; i < 3; ++i) fwt[buf][i] = *(LAS v4u*)(sb + offW + ks * 7168 + i * 1024);
    fwt[buf][3] = *(LAS v4u*)(sb + offW6 + ks * 7168);
  };
  auto mm = [&](int buf) {
#pragma unroll
    for (int i = 0; i < 3; ++i) {
      accA[i] = mfma32(fwt[buf][i], fa[buf], accA[i]);
      accB[i] = mfma32(fwt[buf][i], fb[buf], accB[i]);
    }
    accA[3] = mfma32(fwt[buf][3], fa[buf], accA[3]);
#pragma unroll
    for (int e = 0; e < 4; ++e) { sum = dot2bf(fa[buf][e], 0x3f803f80u, sum); ssq = dot2bf(fa[buf][e], fa[buf][e], ssq); }
  };
  WAITV(12);
  BAR();
  STAMP(ws, 2, 1);
  stage(2, 2);
  ldfr(0, sm, 0);
  int slot = 0;
#pragma unroll 1
  for (int s = 0; s < 8; ++s) {
    LAS char* sb = sm + slot * G1_SLOT;
    ldfr(1, sb, 1); mm(0);
    ldfr(0, sb, 2); mm(1);
    ldfr(1, sb, 3); mm(0);
    const int ns = slot == 2 ? 0 : slot + 1;
    if (s < 7) {
      if (s < 6) WAITV(11); else WAITV(0);
      WAITL(0);
      BAR();
      if (s < 5) stage(s + 3, slot);
      ldfr(0, sm + ns * G1_SLOT, 0);
    }
    mm(1);
    slot = ns;
  }
  STAMP(ws, 2, 2);
  sum += __shfl_xor(sum, 32);
  ssq += __shfl_xor(ssq, 32);
  {
    const float m = sum * (1.0f / D);
    const float var = fmaxf(ssq * (1.0f / D) - m * m, 0.0f);
    const float sd = rbf(__builtin_sqrtf(rbf(rbf(var) + 1e-5f)));
    const float r = 1.0f / sd;
    if (lane < 32) *(LAS v2f*)(sm + G1_MISC + 5120 + (tA * 32 + lane) * 8) = v2f{rbf(m), r};
    if (t < 224) {
      LAS float* cp = (LAS float*)(sm + G1_MISC) + t;
      *(LAS float*)(sm + G1_MISC + 4096 + t * 4) = (cp[0] + cp[224]) + (cp[448] + cp[672]);
    }
  }
  __syncthreads();
  const int h = lane >> 5;
  auto fin = [&](const v16f& a, int tt, int pt) {
    const v2f mr = *(LAS v2f*)(sm + G1_MISC + 5120 + (tt * 32 + (lane & 31)) * 8);
    LAS char* cp = sm + G1_MISC + 4096 + (pt * 16 + 4 * h) * 4;
    const v4f cg0 = *(LAS v4f*)cp, cg1 = *(LAS v4f*)(cp + 32), cu0 = *(LAS v4f*)(cp + 448), cu1 = *(LAS v4f*)(cp + 480);
    float o[8];
#pragma unroll
    for (int e = 0; e < 8; ++e) {
      const float cg = e < 4 ? cg0[e & 3] : cg1[e & 3], cu = e < 4 ? cu0[e & 3] : cu1[e & 3];
      const float g = (a[e] - mr[0] * cg) * mr[1], u = (a[8 + e] - mr[0] * cu) * mr[1];
      const float sg = g * __builtin_amdgcn_rcpf(1.0f + __builtin_amdgcn_exp2f(g * -1.44269504088896340736f));
      o[e] = sg * u;
    }
    *(v4u*)(ws + WS_ACT + ((size_t)((mt * 4 + tt) * 112 + j * 7 + pt) * 64 + lane) * 16) =
        v4u{pkbf(o[0], o[1]), pkbf(o[2], o[3]), pkbf(o[4], o[5]), pkbf(o[6], o[7])};
  };
#pragma unroll
  for (int i = 0; i < 3; ++i) { fin(accA[i], tA, 3 * fw + i); fin(accB[i], tB, 3 * fw + i); }
  fin(accA[3], tA, 6);
  STAMP(ws, 2, 7);
}

constexpr int G2_LDS = 65536 + 8192;
__global__ __launch_bounds__(256) void k_gemm2(char* __restrict__ ws, u16* __restrict__ out) {
  extern __shared__ __attribute__((aligned(16))) char smx[];
  LAS char* sm = (LAS char*)smx;
  const int t = threadIdx.x, lane = t & 63, w = __builtin_amdgcn_readfirstlane(t >> 6);
  STAMP(ws, 3, 0);
  u32 pfv;
  { unsigned long long pc; asm volatile("s_getpc_b64 %0" : "=s"(pc));
    const char* cp = (const char*)(pc & ~63ull) + (blockIdx.x >> 3) * 1024 + (t & 15) * 64;
    asm volatile("global_load_dword %0, %1, off" : "=v"(pfv) : "v"(cp)); }
  const int xcd = blockIdx.x & 7, bi = blockIdx.x >> 3;
  const int tm = 8 * (xcd >> 1) + (bi & 7), dg = 4 * (xcd & 1) + (bi >> 3);
  const v4u* pa = (const v4u*)(ws + WS_ACT) + ((size_t)(2 * tm) * 112 + 28 * w) * 64 + lane;
  const v4u* pw = (const v4u*)(ws + WS_WT2) + ((size_t)(dg * 112 + 28 * w) * 2) * 64 + lane;
  v4u hr[2];
#pragma unroll
  for (int i = 0; i < 2; ++i) hr[i] = *(const v4u*)(ws + WS_HT + (size_t)(64 * dg + (t >> 3) + 32 * i) * 4096 + tm * 128 + (t & 7) * 16);
  v16f acc[2][2];
#pragma unroll
  for (int i = 0; i < 4; ++i) acc[i >> 1][i & 1] = (v16f)(0.0f);
  constexpr int PF = 12;
  v4u ra[PF][2], rw[PF][2];
#pragma unroll
  for (int s = 0; s < PF; ++s) {
    rw[s][0] = pw[s * 128]; rw[s][1] = pw[s * 128 + 64];
    ra[s][0] = pa[s * 64]; ra[s][1] = pa[(112 + s) * 64];
  }
#pragma unroll
  for (int s = 0; s < 28; ++s) {
    const int b = s % PF;
#pragma unroll
    for (int dt = 0; dt < 2; ++dt)
#pragma unroll
      for (int tt = 0; tt < 2; ++tt) acc[dt][tt] = mfma32(rw[b][dt], ra[b][tt], acc[dt][tt]);
    if (s + PF < 28) {
      rw[b][0] = pw[(s + PF) * 128]; rw[b][1] = pw[(s + PF) * 128 + 64];
      ra[b][0] = pa[(s + PF) * 64]; ra[b][1] = pa[(112 + s + PF) * 64];
    }
    __builtin_amdgcn_sched_barrier(0);
  }
  STAMP(ws, 3, 1);
  { float a_=t,b_=1,c_=2,d_=3; asm volatile(R256(BLK) R256(BLK) : "+v"(a_), "+v"(b_), "+v"(c_), "+v"(d_)); }
  asm volatile("" :: "v"(pfv));
#pragma unroll
  for (int i = 0; i < 2; ++i) *(LAS v4u*)(sm + 65536 + ((t >> 3) + 32 * i) * 128 + (((t & 7) ^ (4 * ((t >> 4) & 1))) * 16)) = hr[i];
#pragma unroll
  for (int k = 0; k < 4; ++k) {
    if (k != w) {
#pragma unroll
      for (int e = 0; e < 4; ++e) {
        const v16f& a = acc[k >> 1][k & 1];
        *(LAS v4f*)(sm + ((k * 4 + w) * 4 + e) * 1024 + lane * 16) = v4f{a[4 * e], a[4 * e + 1], a[4 * e + 2], a[4 * e + 3]};
      }
    }
  }
  __syncthreads();
  {
    v16f a = w == 0 ? acc[0][0] : w == 1 ? acc[0][1] : w == 2 ? acc[1][0] : acc[1][1];
#pragma unroll
    for (int k = 0; k < 4; ++k) {
      if (k != w) {
#pragma unroll
        for (int e = 0; e < 4; ++e) {
          const v4f p = *(LAS v4f*)(sm + ((w * 4 + k) * 4 + e) * 1024 + lane * 16);
#pragma unroll
          for (int c = 0; c < 4; ++c) a[4 * e + c] += p[c];
        }
      }
    }
    const int dt = w >> 1, tt = w & 1, G = lane >> 4, q4 = (lane >> 2) & 3, p4 = lane & 3;
    u32 o[8];
#pragma unroll
    for (int e = 0; e < 4; ++e) {
      const int row = 32 * dt + 16 * (G >> 1) + 4 * e + q4;
      const int ck = (4 * tt + 2 * (G & 1) + (p4 >> 1)) ^ (4 * ((row >> 1) & 1));
      const v2u hv = trread(sm + 65536 + row * 128 + ck * 16 + 8 * (p4 & 1));
      const u32 f0 = pkbf(a[4 * e], a[4 * e + 1]), f1 = pkbf(a[4 * e + 2], a[4 * e + 3]);
      o[2 * e] = pkbf(bflo(hv[0]) + bflo(f0), bfhi(hv[0]) + bfhi(f0));
      o[2 * e + 1] = pkbf(bflo(hv[1]) + bflo(f1), bfhi(hv[1]) + bfhi(f1));
    }
    u16* op = out + (size_t)(64 * tm + 32 * tt + (lane & 31)) * D + 64 * dg + 32 * dt + 16 * (lane >> 5);
    *(v4u*)op = v4u{o[0], o[1], o[2], o[3]};
    *(v4u*)(op + 8) = v4u{o[4], o[5], o[6], o[7]};
  }
  STAMP(ws, 3, 7);
}

extern "C" void kernel_launch(void* const* d_in, const int* in_sizes, int n_in, void* d_out, int out_size, void* d_ws, size_t ws_size,
                              hipStream_t stream) {
  const u16* x = (const u16*)d_in[0];
  char* ws = (char*)d_ws;
  (void)hipFuncSetAttribute((const void*)k_conv, hipFuncAttributeMaxDynamicSharedMemorySize, CONV_LDS);
  (void)hipFuncSetAttribute((const void*)k_gemm1, hipFuncAttributeMaxDynamicSharedMemorySize, G1_LDS);
  (void)hipFuncSetAttribute((const void*)k_gemm2, hipFuncAttributeMaxDynamicSharedMemorySize, G2_LDS);
  k_prep<<<480, 256, 0, stream>>>(x, (const u16*)d_in[8], (const u16*)d_in[9], (const u16*)d_in[10], ws);
  k_conv<<<256, 256, CONV_LDS, stream>>>(x, (const u16*)d_in[1], (const u16*)d_in[2], (const u16*)d_in[3], (const u16*)d_in[4],
                                         (const u16*)d_in[5], (const u16*)d_in[6], ws);
  k_gemm1<<<256, 256, G1_LDS, stream>>>(ws);
  k_gemm2<<<256, 256, G2_LDS, stream>>>(ws, (u16*)d_out);
}
```

```cpp
#include <hip/hip_runtime.h>
#include <hip/hip_bf16.h>
#include <stdint.h>
#include <type_traits>

typedef unsigned short u16;
typedef unsigned int u32;
typedef unsigned long long u64;
typedef short v4s __attribute__((ext_vector_type(4)));
typedef __bf16 v2bf __attribute__((ext_vector_type(2)));
typedef _Float16 v2h __attribute__((ext_vector_type(2)));
typedef float v2f __attribute__((ext_vector_type(2)));
typedef float v4f __attribute__((ext_vector_type(4)));
typedef u32 v2u __attribute__((ext_vector_type(2)));
typedef u32 v4u __attribute__((ext_vector_type(4)));
#define LAS __attribute__((address_space(3)))
#define GAS __attribute__((address_space(1)))
#define DEV __device__ __forceinline__

constexpr int L = 2048, D = 512, NS = 128, F = 1792;

constexpr u32 WS_EPOCH = 0;
constexpr u32 WS_WFLAG = 4096;
constexpr u32 WS_GRAN  = 64 << 10;
constexpr u32 WS_CSUM  = 256 << 10;
constexpr u32 WS_HT    = 1 << 20;
constexpr u32 WS_WT1   = 4 << 20;
constexpr u32 WS_WT2   = 8 << 20;
constexpr u32 WS_ACT   = 12 << 20;
constexpr u32 WS_USED  = 20 << 20;
constexpr size_t WS_DBG = 32 << 20;

template <int I> using IC = std::integral_constant<int, I>;
template <int B, int E, class Fn> DEV void sfor(Fn&& f) {
  if constexpr (B < E) { f(IC<B>{}); sfor<B + 1, E>(f); }
}

DEV float bf2f(u32 b) { return __uint_as_float(b << 16); }
DEV float bflo(u32 p) { return __uint_as_float(p << 16); }
DEV float bfhi(u32 p) { return __uint_as_float(p & 0xffff0000u); }
DEV u32 pkbf(float a, float b) { v2f f = {a, b}; return __builtin_bit_cast(u32, __builtin_convertvector(f, v2bf)); }
DEV u32 pkh(float a, float b) { v2f f = {a, b}; return __builtin_bit_cast(u32, __builtin_convertvector(f, v2h)); }
DEV float rbf(float a) { return bflo(pkbf(a, a)); }
DEV float dot2bf(u32 a, u32 b, float c) { return __builtin_amdgcn_fdot2_f32_bf16(__builtin_bit_cast(v2bf, a), __builtin_bit_cast(v2bf, b), c, false); }
DEV v2u trread(LAS char* p) { return __builtin_bit_cast(v2u, __builtin_amdgcn_ds_read_tr16_b64_v4i16((LAS v4s*)p)); }
DEV v4u trfrag(LAS char* p, int second) { const v2u a = trread(p), b = trread(p + second); return v4u{a[0], a[1], b[0], b[1]}; }
#define WAITV(n) asm volatile("s_waitcnt vmcnt(%0)" :: "i"(n) : "memory")
#define WAITL0() asm volatile("s_waitcnt lgkmcnt(0)" ::: "memory")
#define BAR() asm volatile("s_waitcnt lgkmcnt(0)\n\ts_barrier" ::: "memory")
#define BARX() asm volatile("s_barrier" ::: "memory")
#define SB() __builtin_amdgcn_sched_barrier(0)
constexpr int SPIN_CAP = 1 << 20;

#define STAMP(slot) do {} while (0)

template <int CTRL> DEV float dpp(float v) { return __builtin_bit_cast(float, __builtin_amdgcn_update_dpp(0, __builtin_bit_cast(int, v), CTRL, 0xf, 0xf, true)); }
DEV float swap16_other(float v, float& o) { auto r = __builtin_amdgcn_permlane16_swap(__float_as_uint(v), __float_as_uint(v), false, false); o = __uint_as_float(r[1]); return __uint_as_float(r[0]); }
DEV float swap32_other(float v, float& o) { auto r = __builtin_amdgcn_permlane32_swap(__float_as_uint(v), __float_as_uint(v), false, false); o = __uint_as_float(r[1]); return __uint_as_float(r[0]); }
DEV float sum16x(float v) { float o; const float a = swap16_other(v, o); return a + o; }
DEV float sum32x(float v) { float o; const float a = swap32_other(v, o); return a + o; }
DEV float wave_sum(float v) {
  v += dpp<0xB1>(v); v += dpp<0x4E>(v); v += dpp<0x141>(v); v += dpp<0x140>(v);
  return sum32x(sum16x(v));
}
DEV float wave_max(float v) {
  v = fmaxf(v, dpp<0xB1>(v)); v = fmaxf(v, dpp<0x4E>(v)); v = fmaxf(v, dpp<0x141>(v)); v = fmaxf(v, dpp<0x140>(v));
  float o; float a = swap16_other(v, o); v = fmaxf(a, o); a = swap32_other(v, o); return fmaxf(a, o);
}

DEV void dma16(v4u srd, u32 voff, u32 soff, u32 lds) {
  asm volatile("s_mov_b32 m0, %3\n\ts_nop 0\n\tbuffer_load_dwordx4 %0, %1, %2 offen sc1 lds" :: "v"(voff), "s"(srd), "s"(soff), "s"(lds) : "memory");
}
template <int A, int IMM> DEV void ld_agpr(v4u srd, u32 voff, u32 soff) {
  asm volatile("buffer_load_dwordx4 a[%c3:%c4], %0, %1, %2 offen offset:%c5 sc1" :: "v"(voff), "s"(srd), "s"(soff), "i"(A), "i"(A + 3), "i"(IMM) : "memory");
}
template <int IMM> DEV void ld_vgpr(v4u& d, v4u srd, u32 voff, u32 soff) {
  asm volatile("buffer_load_dwordx4 %0, %1, %2, %3 offen offset:%c4 sc1" : "=v"(d) : "v"(voff), "s"(srd), "s"(soff), "i"(IMM) : "memory");
}
template <int ACC, int A, bool FIRST> DEV void mfma_aav(const v4u& b) {
  if constexpr (FIRST) asm volatile("v_mfma_f32_32x32x16_bf16 a[%c1:%c2], a[%c3:%c4], %0, 0" :: "v"(b), "i"(ACC), "i"(ACC + 15), "i"(A), "i"(A + 3));
  else asm volatile("v_mfma_f32_32x32x16_bf16 a[%c1:%c2], a[%c3:%c4], %0, a[%c1:%c2]" :: "v"(b), "i"(ACC), "i"(ACC + 15), "i"(A), "i"(A + 3));
}
template <int ACC, bool FIRST> DEV void mfma_avv(const v4u& a, const v4u& b) {
  if constexpr (FIRST) asm volatile("v_mfma_f32_32x32x16_bf16 a[%c2:%c3], %0, %1, 0" :: "v"(a), "v"(b), "i"(ACC), "i"(ACC + 15));
  else asm volatile("v_mfma_f32_32x32x16_bf16 a[%c2:%c3], %0, %1, a[%c2:%c3]" :: "v"(a), "v"(b), "i"(ACC), "i"(ACC + 15));
}
template <int A, int OFF> DEV void dsw_agpr(u32 ad) {
  asm volatile("ds_write_b128 %0, a[%c1:%c2] offset:%c3" :: "v"(ad), "i"(A), "i"(A + 3), "i"(OFF) : "memory");
}
DEV void ld_flag(u32& d, v4u srd, u32 voff, u32 soff) {
  asm volatile("buffer_load_dword %0, %1, %2, %3 offen sc1" : "=v"(d) : "v"(voff), "s"(srd), "s"(soff) : "memory");
}
DEV void wait0_pin(u32& d) { asm volatile("s_waitcnt vmcnt(0)" : "+v"(d) :: "memory"); }
template <int A> DEV float accrd() { float v; asm volatile("v_accvgpr_read_b32 %0, a%c1" : "=v"(v) : "i"(A)); return v; }
DEV void mfma16(v4f& c, const v4u& a, const v4u& b) { asm volatile("v_mfma_f32_16x16x32_f16 %0, %1, %2, %0" : "+v"(c) : "v"(a), "v"(b)); }
DEV void mfma16z(v4f& c, const v4u& a, const v4u& b) { asm volatile("v_mfma_f32_16x16x32_f16 %0, %1, %2, 0" : "=v"(c) : "v"(a), "v"(b)); }
constexpr int A_W = 0, A_ACC1 = 128, A_ACC2 = 192;

constexpr int TCOL = 528, NCOL = 63, TAB_B = NCOL * TCOL;
constexpr int KC_B = 4640, UC_B = 4224;
constexpr int OFF_K = TAB_B, OFF_U = OFF_K + 4 * KC_B, OFF_X = OFF_U + 2 * UC_B, CH_B = OFF_X + 4096;
constexpr int L_RT1 = 2 * CH_B, L_RT2 = L_RT1 + 128 * 144, L_MISC = L_RT2 + 64 * 144;
constexpr int L_W2 = 131072;
constexpr int L_CSP = L_MISC, L_CS = L_CSP + 4096, L_MR = L_CS + 1024, L_VOTE = L_MR + 1024, LDS_B = L_VOTE + 64;
static_assert(L_W2 >= L_RT1 && L_W2 + 24576 <= L_MISC && LDS_B <= 163840 && L_MISC % 16 == 0, "lds map");

struct cf { float re, im; };
DEV cf cmul(cf a, cf b) { cf r; r.re = a.re * b.re - a.im * b.im; r.im = a.re * b.im + a.im * b.re; return r; }
DEV cf cexp_p(float ar, float ai, float p) {
  const float e = __builtin_amdgcn_exp2f(ar * p * 1.44269504088896340736f);
  const float xx = ai * p;
  const float chi = 0.15915493667125702f, clo = 6.4206383e-09f;
  const float th = xx * chi;
  float tl = __builtin_fmaf(xx, chi, -th);
  tl = __builtin_fmaf(xx, clo, tl);
  const float fr = (th - __builtin_rintf(th)) + tl;
  cf r; r.re = e * __builtin_amdgcn_cosf(fr); r.im = e * __builtin_amdgcn_sinf(fr);
  return r;
}

struct ConvSched { int nm, ma[18], mq[18], mk[18]; int nr, ru[16], ri[16]; };
constexpr ConvSched mk_sched(int c0, int c1, int c2, int c3) {
  ConvSched s{};
  const int c[4] = {c0, c1, c2, c3};
  bool hu[8] = {}, hk[8] = {};
  for (int q = 0; q <= c3; ++q)
    for (int a = 3; a >= 0; --a)
      if (q <= c[a]) {
        const int k = c[a] - q;
        s.ma[s.nm] = a; s.mq[s.nm] = q; s.mk[s.nm] = k; ++s.nm;
        if (!hu[q]) { hu[q] = true; s.ru[s.nr] = 1; s.ri[s.nr] = q; ++s.nr; }
        if (!hk[k]) { hk[k] = true; s.ru[s.nr] = 0; s.ri[s.nr] = k; ++s.nr; }
      }
  return s;
}
template <int V> struct CS { static constexpr ConvSched s = V == 0 ? mk_sched(0, 3, 4, 7) : mk_sched(1, 2, 5, 6); };

template <int V, class Ex> DEV void conv_main(LAS char* ub, LAS char* kb, v4f (&acc)[4], Ex&& extra) {
  constexpr ConvSched S = CS<V>::s;
  v4u U[2][8], K[2][8];
  sfor<0, S.nr>([&](auto i) {
    if constexpr (S.ru[i]) U[0][S.ri[i]] = *(LAS v4u*)(ub + 512 * S.ri[i]);
    else K[0][S.ri[i]] = *(LAS v4u*)(kb - 512 * S.ri[i]);
  });
  SB();
  sfor<0, 8>([&](auto r) {
    constexpr int cur = r & 1, nxt = cur ^ 1;
    sfor<0, 18>([&](auto m) {
      if constexpr (r == 0 && S.mq[m] == 0) mfma16z(acc[S.ma[m]], U[cur][S.mq[m]], K[cur][S.mk[m]]);
      else mfma16(acc[S.ma[m]], U[cur][S.mq[m]], K[cur][S.mk[m]]);
      if constexpr (r < 7 && m < S.nr) {
        if constexpr (S.ru[m]) U[nxt][S.ri[m]] = *(LAS v4u*)(ub + 64 * (r + 1) + 512 * S.ri[m]);
        else K[nxt][S.ri[m]] = *(LAS v4u*)(kb + 64 * (r + 1) - 512 * S.ri[m]);
      }
      extra(r, m);
      SB();
    });
  });
  asm volatile("s_nop 7" : "+v"(acc[0]), "+v"(acc[1]), "+v"(acc[2]), "+v"(acc[3]));
}

DEV void silu_mul8(const float (&g)[8], const float (&u)[8], v4u& o) {
  float r[8];
#pragma unroll
  for (int e = 0; e < 8; ++e) r[e] = g[e] * __builtin_amdgcn_rcpf(1.0f + __builtin_amdgcn_exp2f(g[e] * -1.44269504088896340736f)) * u[e];
#pragma unroll
  for (int e = 0; e < 4; ++e) o[e] = min(pkbf(r[2 * e], r[2 * e + 1]), 0xfffffffeu);
}

__global__ __launch_bounds__(256, 1) void k_fused(const u16* __restrict__ x, const u16* __restrict__ lar_, const u16* __restrict__ aim_,
                                                  const u16* __restrict__ cre_, const u16* __restrict__ cim_, const u16* __restrict__ ldt_,
                                                  const u16* __restrict__ dsk_, const u16* __restrict__ wg, const u16* __restrict__ wu,
                                                  const u16* __restrict__ wd, char* __restrict__ ws, u16* __restrict__ out) {
  extern __shared__ __attribute__((aligned(16))) char smx[];
  LAS char* sm = (LAS char*)smx;
  asm volatile("" ::: "a0", "a255");
  const int t = threadIdx.x, lane = t & 63, w = __builtin_amdgcn_readfirstlane(t >> 6), b = blockIdx.x;
  STAMP(0);
  const int xcd = b & 7, bi = b >> 3;
  const auto rs = __builtin_amdgcn_make_buffer_rsrc(ws, 0, WS_USED, 0x00020000);
  const v4u srd = {(u32)(uintptr_t)ws, (u32)((uintptr_t)ws >> 32) & 0xffffu, WS_USED, 0x00020000u};
  const int pr = xcd * 32 + bi;
  const int cw = w >> 1, nh = w & 1, ch = 2 * pr + cw;
  const int mt = 4 * (xcd >> 1) + (bi & 3), j = 8 * (xcd & 1) + (bi >> 2);
  const int e2 = j & 1, dg = j >> 1;
  const int G = lane >> 4, q4 = (lane >> 2) & 3, p4 = lane & 3, i16 = lane & 15;
  const u32 lane16 = lane * 16;
  LAS char* cb = sm + cw * CH_B;

  v4u xa[2];
#pragma unroll
  for (int i = 0; i < 2; ++i) xa[i] = *(const v4u*)(x + (size_t)(8 * b + 2 * w + i) * D + lane * 8);
  const u32 ep = __builtin_amdgcn_raw_buffer_load_b32(rs, WS_EPOCH, 0, 16);
  const int n = 64 * nh + lane;
  const u32 r_lar = lar_[ch * NS + n], r_aim = aim_[ch * NS + n], r_cre = cre_[ch * NS + n], r_cim = cim_[ch * NS + n];
  const u32 r_cre2 = cre_[ch * NS + (n ^ 64)], r_cim2 = cim_[ch * NS + (n ^ 64)];
  const u32 r_ldt = ldt_[ch], r_dsk = dsk_[ch];
  SB();
  {
    const v4u s = {~0u, ~0u, ~0u, ~0u};
#pragma unroll
    for (int i = 0; i < 2; ++i) __builtin_amdgcn_raw_buffer_store_b128(s, rs, t * 16 + i * 4096, WS_HT + pr * 8192, 16);
  }
  SB();
  const int rb = b < 224 ? b : 223;
  const int mat = rb / 112, cg = (rb % 112) % 28, rq = (rb % 112) / 28, dgp = rb & 7, fb = rb >> 3;
  v4u wa[4], wb[2];
  {
    const u16* W = mat ? wu : wg;
#pragma unroll
    for (int i = 0; i < 4; ++i) wa[i] = *(const v4u*)(W + (size_t)(128 * rq + (t >> 3) + 32 * i) * F + 64 * cg + (t & 7) * 8);
#pragma unroll
    for (int i = 0; i < 2; ++i) wb[i] = *(const v4u*)(wd + (size_t)(64 * fb + (t >> 3) + 32 * i) * D + 64 * dgp + (t & 7) * 8);
  }
  u32 xr[8];
#pragma unroll
  for (int i = 0; i < 8; ++i) xr[i] = *(const u32*)(x + (size_t)(8 * t + i) * D + 2 * pr);
  u32 pfv;
  {
    u64 pc; u32 len;
    asm volatile("s_getpc_b64 %0\n.Lkf_here:\n\ts_mov_b32 %1, .Lkf_end-.Lkf_here" : "=s"(pc), "=s"(len));
    const u32 off = min((u32)((bi * 32 + (t & 31)) * 64), len);
    asm volatile("global_load_dword %0, %1, %2" : "=v"(pfv) : "v"(off), "s"(pc));
  }
  SB();
  {
    const v4u z = {0, 0, 0, 0};
    const int tt = t & 127;
    LAS char* c2 = sm + (t >> 7) * CH_B;
    *(LAS v4u*)(c2 + OFF_K + (tt / 33) * KC_B + (255 + tt % 33) * 16) = z;
    if (tt < 4) *(LAS v4u*)(c2 + OFF_K + 3 * KC_B + (255 + 29 + tt) * 16) = z;
    if (tt < 16) *(LAS v4u*)(c2 + OFF_U + (tt >> 3) * UC_B + (tt & 7) * 16) = z;
  }
  const u32 tag = __builtin_amdgcn_readfirstlane(ep) + 1u;
  {
    float v[2][8], s[2], q[2];
#pragma unroll
    for (int i = 0; i < 2; ++i) {
#pragma unroll
      for (int k = 0; k < 4; ++k) { v[i][2 * k] = bflo(xa[i][k]); v[i][2 * k + 1] = bfhi(xa[i][k]); }
      s[i] = ((v[i][0] + v[i][1]) + (v[i][2] + v[i][3])) + ((v[i][4] + v[i][5]) + (v[i][6] + v[i][7]));
    }
#pragma unroll
    for (int i = 0; i < 2; ++i) s[i] = wave_sum(s[i]) * (1.0f / D);
#pragma unroll
    for (int i = 0; i < 2; ++i) {
      q[i] = 0;
#pragma unroll
      for (int k = 0; k < 8; ++k) { const float d = v[i][k] - s[i]; q[i] += d * d; }
    }
#pragma unroll
    for (int i = 0; i < 2; ++i) q[i] = wave_sum(q[i]) * (1.0f / D);
    const float m = (lane & 1) ? s[1] : s[0], vv = (lane & 1) ? q[1] : q[0];
    const u32 pk = pkbf(m, __builtin_sqrtf(rbf(rbf(vv) + 1e-5f)));
    SB();
    WAITV(15);
    if (lane < 16) __builtin_amdgcn_raw_buffer_store_b64(v2u{pk, tag}, rs, (lane >> 1) * 16384 + (lane & 1) * 8, WS_GRAN + (8 * b + 2 * w) * 8, 16);
    SB();
  }
  STAMP(1);
  const float dt = rbf(expf(bf2f(r_ldt)));
  const float ar = -rbf(expf(bf2f(r_lar))) * dt, ai = bf2f(r_aim) * dt;
  float cmax = fmaxf(fmaxf(fabsf(bf2f(r_cre)), fabsf(bf2f(r_cim))), fmaxf(fabsf(bf2f(r_cre2)), fabsf(bf2f(r_cim2))));
  cmax = wave_max(cmax);
  int ex = (int)((__float_as_uint(cmax) >> 23) & 255);
  ex = ex < 8 ? 8 : (ex > 250 ? 250 : ex);
  const float scl = __uint_as_float((u32)(257 - ex) << 23);
  const float yscl = 2.0f * dt * __uint_as_float((u32)(ex - 3) << 23);
  {
    LAS char* tb = cb + n * 4;
#define PUTB(col, v) *(LAS u32*)(tb + (col) * TCOL) = pkh((v).re, (v).im)
    const cf z1 = cexp_p(ar, ai, 1.0f), z16 = cexp_p(ar, ai, 16.0f), z256 = cexp_p(ar, ai, 256.0f);
    cf a = {bf2f(r_cre) * scl, bf2f(r_cim) * scl};
#pragma unroll
    for (int i = 0; i < 16; ++i) {
      *(LAS u32*)(tb + i * TCOL) = pkh(a.re, -a.im);
      if (i < 15) a = cmul(a, z16);
    }
    cf zp[17];
    zp[0] = {1.0f, 0.0f}; zp[1] = z1; zp[16] = z16;
#pragma unroll
    for (int i = 2; i < 16; ++i) zp[i] = (i & 1) ? cmul(zp[i - 1], z1) : cmul(zp[i / 2], zp[i / 2]);
#pragma unroll
    for (int i = 0; i < 16; ++i) PUTB(16 + i, zp[i]);
    cf zb[8];
    zb[1] = z256; zb[2] = cmul(z256, z256); zb[3] = cmul(zb[2], z256); zb[4] = cmul(zb[2], zb[2]);
    zb[5] = cmul(zb[4], z256); zb[6] = cmul(zb[4], zb[2]); zb[7] = cmul(zb[4], zb[3]);
    PUTB(32, zb[1]);
#pragma unroll
    for (int i = 1; i < 9; ++i) { const cf v = cmul(zb[1], zp[2 * i]); PUTB(32 + i, v); }
#pragma unroll
    for (int m = 2; m < 4; ++m) {
      PUTB(41 + 5 * (m - 2), zb[m]);
#pragma unroll
      for (int i = 1; i < 5; ++i) { const cf v = cmul(zb[m], zp[4 * i]); PUTB(41 + 5 * (m - 2) + i, v); }
    }
#pragma unroll
    for (int m = 4; m < 8; ++m) {
      PUTB(51 + 3 * (m - 4), zb[m]);
#pragma unroll
      for (int i = 1; i < 3; ++i) { const cf v = cmul(zb[m], zp[8 * i]); PUTB(51 + 3 * (m - 4) + i, v); }
    }
#undef PUTB
  }
  STAMP(2);
  SB();
#pragma unroll
  for (int i = 0; i < 4; ++i) *(LAS v4u*)(sm + L_RT1 + ((t >> 3) + 32 * i) * 144 + (t & 7) * 16) = wa[i];
#pragma unroll
  for (int i = 0; i < 2; ++i) *(LAS v4u*)(sm + L_RT2 + ((t >> 3) + 32 * i) * 144 + (t & 7) * 16) = wb[i];
  BAR();
  STAMP(3);
  if (b < 224) {
    const int g16 = 4 * cg + w, jj = g16 / 7, pt = g16 % 7, h = G & 1;
    float cs = 0;
#pragma unroll
    for (int it = 0; it < 4; ++it) {
      const int ksl = 2 * it + (G >> 1), ks = 8 * rq + ksl;
      const v4u o = trfrag(sm + L_RT1 + (16 * ksl + 8 * h + q4) * 144 + 32 * w + 8 * p4, 4 * 144);
#pragma unroll
      for (int e = 0; e < 4; ++e) cs = dot2bf(o[e], 0x3f803f80u, cs);
      const int fr = pt < 4 ? pt * 32 + ks : 128 + ks * 3 + (pt - 4);
      __builtin_amdgcn_raw_buffer_store_b128(o, rs, fr * 1024 + (32 * h + 16 * mat + i16) * 16, WS_WT1 + jj * 229376, 16);
    }
    cs = sum32x(sum16x(cs));
    if (lane < 16) __builtin_amdgcn_raw_buffer_store_b32(__float_as_uint(cs), rs, lane * 4, WS_CSUM + jj * 4096 + (rq * 224 + mat * 112 + pt * 16) * 4, 16);
#pragma unroll
    for (int it = 0; it < 2; ++it) {
      const int sl = 2 * it + (G >> 1), s = 4 * fb + sl, jp = s / 7, pp = s % 7;
      const v4u o = trfrag(sm + L_RT2 + (16 * sl + 4 * h + q4) * 144 + 32 * w + 8 * p4, 8 * 144);
      const int pos = pp < 4 ? (pp >> 1) * 8 + (jp & 3) * 2 + (pp & 1) : 16 + (jp & 3) * 3 + (pp - 4);
      const int xrow = (i16 & 3) + 8 * (i16 >> 2) + 4 * (w & 1);
      __builtin_amdgcn_raw_buffer_store_b128(o, rs, (((jp >> 2) * 28 + pos) * 2 + (w >> 1)) * 1024 + (32 * h + xrow) * 16, WS_WT2 + dgp * 229376, 16);
    }
  }
  SB();
  {
    v4u Bf[8], Af[2][8];
    const int c2 = (i16 + ((i16 >> 1) & 1)) >> 1, c4 = (i16 + 1 + ((i16 >> 2) & 1)) >> 2, c8 = (i16 + 3 + ((i16 >> 3) & 1)) >> 3;
    LAS char* ap[4];
    ap[0] = cb + (nh ? 32 + c2 : 16 + i16) * TCOL + G * 16;
    ap[1] = cb + (41 + 5 * nh + c4) * TCOL + G * 16;
    ap[2] = cb + (51 + 3 * nh + c8) * TCOL + G * 16;
    ap[3] = ap[2] + 6 * TCOL;
#pragma unroll
    for (int ks = 0; ks < 8; ++ks) { Bf[ks] = *(LAS v4u*)(cb + i16 * TCOL + ks * 64 + G * 16); Af[0][ks] = *(LAS v4u*)(ap[0] + ks * 64); }
    SB();
    v4f d[4];
    sfor<0, 4>([&](auto mm) {
      sfor<0, 8>([&](auto ks) {
        if constexpr (ks == 0) mfma16z(d[mm], Af[mm & 1][ks], Bf[ks]);
        else mfma16(d[mm], Af[mm & 1][ks], Bf[ks]);
        if constexpr (mm < 3) Af[(mm & 1) ^ 1][ks] = *(LAS v4u*)(ap[mm + 1] + ks * 64);
        SB();
      });
    });
    asm volatile("s_nop 7" : "+v"(d[0]), "+v"(d[1]), "+v"(d[2]), "+v"(d[3]));
    LAS char* kp = cb + OFF_K + 16 + (2038 - 256 * nh - 16 * i16 - 4 * G - 3 * 512) * 2;
#pragma unroll
    for (int mm = 0; mm < 4; ++mm) {
      const u32 p0 = pkh(d[mm][3], d[mm][2]), p1 = pkh(d[mm][1], d[mm][0]);
#pragma unroll
      for (int e = 0; e < 4; ++e) {
        LAS char* k2 = kp + (3 - mm) * 1024 + e * (KC_B + 4);
        if (e & 1) *(LAS v2u*)k2 = v2u{p0, p1};
        else { *(LAS u32*)k2 = p0; *(LAS u32*)(k2 + 4) = p1; }
      }
    }
  }
  STAMP(4);
  {
    v4u g[4];
    for (int spin = 0; spin < SPIN_CAP; ++spin) {
      asm volatile("" ::: "memory");
#pragma unroll
      for (int i = 0; i < 4; ++i) g[i] = __builtin_amdgcn_raw_buffer_load_b128(rs, t * 64 + i * 16, WS_GRAN + xcd * 16384, 16);
      const bool ok = g[0][1] == tag && g[0][3] == tag && g[1][1] == tag && g[1][3] == tag && g[2][1] == tag && g[2][3] == tag && g[3][1] == tag && g[3][3] == tag;
      if (__all(ok)) break;
    }
    WAITV(0);
    asm volatile("" :: "v"(pfv));
    if (lane == 0) {
      if (b < 224) __builtin_amdgcn_raw_buffer_store_b32(tag, rs, 0, WS_WFLAG + (b * 4 + w) * 16, 16);
    }
    STAMP(5);
    float mm[8], rr[8];
#pragma unroll
    for (int i = 0; i < 8; ++i) { const u32 s = g[i >> 1][(i & 1) * 2]; mm[i] = bflo(s); rr[i] = __builtin_amdgcn_rcpf(bfhi(s)); }
#pragma unroll
    for (int c = 0; c < 2; ++c) {
      float u[8];
#pragma unroll
      for (int i = 0; i < 8; i += 2) {
        const u32 d = pkbf((c ? bfhi(xr[i]) : bflo(xr[i])) - mm[i], (c ? bfhi(xr[i + 1]) : bflo(xr[i + 1])) - mm[i + 1]);
        const u32 o = pkbf(bflo(d) * rr[i], bfhi(d) * rr[i + 1]);
        u[i] = bflo(o); u[i + 1] = bfhi(o);
      }
      LAS char* c2 = sm + c * CH_B;
      *(LAS v4u*)(c2 + OFF_U + UC_B + (8 * t + 56) * 2) = v4u{pkh(u[0], u[1]), pkh(u[2], u[3]), pkh(u[4], u[5]), pkh(u[6], u[7])};
      LAS char* p0 = c2 + OFF_U + (8 * t + 57) * 2;
      const u32 e0 = pkh(u[0], u[7]);
      *(LAS u16*)p0 = (u16)e0;
      *(LAS u32*)(p0 + 2) = pkh(u[1], u[2]);
      *(LAS u32*)(p0 + 6) = pkh(u[3], u[4]);
      *(LAS u32*)(p0 + 10) = pkh(u[5], u[6]);
      *(LAS u16*)(p0 + 14) = (u16)(e0 >> 16);
      v4u xs;
#pragma unroll
      for (int i = 0; i < 4; ++i)
        xs[i] = c ? __builtin_amdgcn_perm(xr[2 * i + 1], xr[2 * i], 0x07060302u) : __builtin_amdgcn_perm(xr[2 * i + 1], xr[2 * i], 0x05040100u);
      *(LAS v4u*)(c2 + OFF_X + t * 16) = xs;
    }
  }
  BAR();
  if (b == 0 && t == 0) __builtin_amdgcn_raw_buffer_store_b32(tag, rs, 0, WS_EPOCH, 16);
  STAMP(6);
  v4f acc[4];
  {
    u32 fo;
    {
      int g16, pw;
      if (lane < 8) g16 = 7 * j + w; else g16 = 7 * j + 4 + ((lane - 8) >> 3);
      pw = ((lane & 1) * 112 + ((lane >> 1) & 3) * 28 + (g16 >> 2)) * 4 + (g16 & 3);
      if (lane >= 32) { const int k = lane < 60 ? lane - 32 : 0; pw = ((7 * w + (k >> 2)) * 8 + dg) * 4 + (k & 3); }
      fo = pw * 16;
    }
    const u32 sW1 = WS_WT1 + j * 229376 + w * 32768, sW2 = WS_WT1 + j * 229376 + 131072;
    const u32 sAct = WS_ACT + ((mt * 4) * 112 + j * 7) * 1024;
    u32 fl;
    const v4u sent = {~0u, ~0u, ~0u, ~0u};
    auto extra = [&](auto r, auto m) {
      if constexpr (r == 0 && m >= 2 && m < 9) {
        __builtin_amdgcn_raw_buffer_store_b128(sent, rs, lane16 + (int)(m - 2) * 1024, sAct + w * 112 * 1024, 16);
      }
      if constexpr (r == 2 && m == 8) ld_flag(fl, srd, fo, WS_WFLAG);
      if constexpr (r == 3 && m == 17) {
        wait0_pin(fl);
        for (int spin = 0; spin < SPIN_CAP && !__all(fl == tag); ++spin) { ld_flag(fl, srd, fo, WS_WFLAG); wait0_pin(fl); }
      }
      if constexpr (r >= 4) {
        constexpr int k = (r - 4) * 18 + m;
        if constexpr (k < 32) ld_agpr<A_W + 4 * k, (k & 3) * 1024>(srd, lane16, sW1 + (k >> 2) * 4096);
        else if constexpr (k < 38) dma16(srd, lane16, sW2 + (w * 6 + k - 32) * 1024, L_W2 + (w * 6 + k - 32) * 1024);
        else if constexpr (k == 38) dma16(srd, lane16, WS_CSUM + j * 4096 + w * 1024, L_CSP + w * 1024);
      }
    };
    LAS char* ub = cb + OFF_U + (i16 & 1) * UC_B + ((i16 >> 1) + G) * 16;
    LAS char* kb = cb + OFF_K + (i16 & 3) * KC_B + 16 + (248 - 8 * (i16 >> 2) + G) * 16;
    if (nh == 0) conv_main<0>(ub, kb, acc, extra);
    else conv_main<1>(ub, kb, acc, extra);
  }
  STAMP(7);
  {
    const float dsk = bf2f(r_dsk);
    const float inf = __builtin_inff();
#pragma unroll
    for (int a = 0; a < 4; ++a) {
#pragma unroll
      for (int hh = 0; hh < 2; ++hh) {
        const int l = 256 * (4 * (a >> 1) + ((a & 1) ? 3 - nh : nh)) + 8 * (2 * G + hh) + 2 * (i16 & 3) + 64 * (i16 >> 2);
        const v2h up = *(LAS v2h*)(cb + OFF_U + UC_B + (l + 56) * 2);
        LAS u32* xp = (LAS u32*)(cb + OFF_X + l * 2);
        const u32 xv = *xp;
        const u32 yb = pkbf(acc[a][2 * hh] * yscl, acc[a][2 * hh + 1] * yscl);
        const u32 sk = pkbf((float)up[0] * dsk, (float)up[1] * dsk);
        const u32 zz = pkbf(bflo(yb) + bflo(sk), bfhi(yb) + bfhi(sk));
        float h0 = bflo(xv) + bflo(zz), h1 = bfhi(xv) + bfhi(zz);
        asm("v_min_f32 %0, %1, %2" : "=v"(h0) : "v"(h0), "v"(inf));
        asm("v_min_f32 %0, %1, %2" : "=v"(h1) : "v"(h1), "v"(inf));
        *xp = pkbf(h0, h1);
      }
    }
    WAITL0();
#pragma unroll
    for (int i = 0; i < 2; ++i) {
      const int c = 4 * i + ((lane >> 5) ? 3 - nh : nh);
      const int l = 256 * c + (lane & 31) * 8;
      __builtin_amdgcn_raw_buffer_store_b128(*(LAS v4u*)(cb + OFF_X + l * 2), rs, l * 2, WS_HT + ch * 4096, 16);
    }
  }
  WAITV(0);
  BAR();
  STAMP(8);
  const u32 vtok = (lane >> 4) * 4096 + (((lane & 15) ^ (4 * (lane >> 4))) * 16);
  const u32 stok = WS_HT + mt * 256 + w * 65536;
  auto tok_dma = [&](int c) {
#pragma unroll
    for (int i = 0; i < 4; ++i) dma16(srd, vtok, stok + (64 * c + 4 * i) * 4096, c * 16384 + (4 * w + i) * 1024);
  };
#pragma unroll
  for (int c = 0; c < 8; ++c) tok_dma(c);
  if (t < 224) {
    LAS float* cp = (LAS float*)(sm + L_CSP) + t;
    *(LAS float*)(sm + L_CS + t * 4) = (cp[0] + cp[224]) + (cp[448] + cp[672]);
  }
  LAS char* tp[4][2];
#pragma unroll
  for (int i = 0; i < 4; ++i) {
    tp[i][0] = sm + (8 * (G >> 1) + q4) * 256 + 8 * (p4 & 1) + (((4 * (w ^ i) + 2 * (G & 1) + (p4 >> 1)) ^ (4 * q4)) * 16);
    tp[i][1] = tp[i][0] + 65536;
  }
  v4u own[32];
  float sum = 0, ssq = 0;
  LAS u32* vote = (LAS u32*)(sm + L_VOTE);
  auto own_rd = [&](auto s) { own[s] = trfrag(tp[0][s >> 4] + (s & 15) * 4096, 1024); };
  auto own_acc = [&](auto s, float& sg, float& qg) {
#pragma unroll
    for (int e = 0; e < 4; ++e) { sg = dot2bf(own[s][e], 0x3f803f80u, sg); qg = dot2bf(own[s][e], own[s][e], qg); }
  };
  auto cast_vote = [&](int g, float qg) { const u32 bad = __any(qg != qg); if (lane == 0) vote[g * 4 + w] = bad; };
  auto read_vote = [&](int g) { const v4u v = *(LAS v4u*)(vote + g * 4); return (int)__builtin_amdgcn_readfirstlane(v[0] | v[1] | v[2] | v[3]); };
  auto redo = [&](auto c0, auto c1, int g, float& sg, float& qg) {
    for (int spin = 0; spin < SPIN_CAP; ++spin) {
      BAR();
#pragma unroll
      for (int c = c0; c < c1; ++c) tok_dma(c);
      WAITV(0);
      BAR();
      sg = 0; qg = 0;
      sfor<4 * c0, 4 * c1>([&](auto s) { own_rd(s); });
      sfor<4 * c0, 4 * c1>([&](auto s) { own_acc(s, sg, qg); });
      cast_vote(g, qg);
      BAR();
      if (!read_vote(g)) break;
    }
  };
  {
    float sg = 0, qg = 0;
    WAITV(24);
    BAR();
    sfor<0, 8>([&](auto s) { own_rd(s); });
    sfor<0, 8>([&](auto s) { own_acc(s, sg, qg); });
    cast_vote(0, qg);
    BAR();
    if (read_vote(0)) redo(IC<0>{}, IC<2>{}, 0, sg, qg);
    sum = sg; ssq = qg;
  }
  STAMP(9);
  const u32 sW2 = WS_WT1 + j * 229376 + 131072;
  auto w2_dma = [&](int g) {
#pragma unroll
    for (int i = 0; i < 3; ++i) dma16(srd, lane16, sW2 + g * 12288 + (3 * w + i) * 1024, (g - 2) * 16384 + (3 * w + i) * 1024);
  };
  v4u hres[2];
  {
    v4u ot[2][3], hA[2], hB[2];
    float sg = 0, qg = 0;
    LAS char* hp = sm + (64 * dg + 32 * (w >> 1) + 16 * (G >> 1) + q4) * 256 + 8 * (p4 & 1) +
                   (((4 * (2 * e2 + (w & 1)) + 2 * (G & 1) + (p4 >> 1)) ^ (4 * q4)) * 16);
    auto ot_rd = [&](auto s, auto i) { ot[s & 1][i] = trfrag(tp[i + 1][s >> 4] + (s & 15) * 4096, 1024); };
    sfor<0, 3>([&](auto i) { ot_rd(IC<0>{}, i); });
    SB();
    sfor<0, 32>([&](auto s) {
      if constexpr (s == 2) { WAITV(12); BARX(); }
      if constexpr (s == 10) { WAITV(0); BARX(); w2_dma(2); w2_dma(3); }
      if constexpr (s == 25) { BARX(); w2_dma(7); }
      constexpr bool last_of_group = (s == 7 || s == 19);
      constexpr int o0 = (s >= 2 && s < 8) ? 8 + 2 * (s - 2) : (s >= 10 && s < 16) ? 20 + 2 * (s - 10) : -1;
      constexpr int a0 = (s >= 3 && s < 8) ? 8 + 2 * (s - 3) : (s >= 11 && s < 17) ? 20 + 2 * (s - 11) : -1;
      mfma_aav<A_ACC1, A_W + 4 * s, s == 0>(own[s]);
      if constexpr (s < 31 && !last_of_group) ot_rd(IC<s + 1>{}, IC<0>{});
      if constexpr (o0 >= 0) own_rd(IC<o0>{});
      SB();
      mfma_aav<A_ACC1 + 16, A_W + 4 * s, s == 0>(ot[s & 1][0]);
      if constexpr (s < 31 && !last_of_group) ot_rd(IC<s + 1>{}, IC<1>{});
      if constexpr (a0 >= 0) own_acc(IC<a0>{}, sg, qg);
      SB();
      mfma_aav<A_ACC1 + 32, A_W + 4 * s, s == 0>(ot[s & 1][1]);
      if constexpr (s < 31 && !last_of_group) ot_rd(IC<s + 1>{}, IC<2>{});
      if constexpr (o0 >= 0) own_rd(IC<o0 + 1>{});
      SB();
      mfma_aav<A_ACC1 + 48, A_W + 4 * s, s == 0>(ot[s & 1][2]);
      if constexpr (a0 >= 0) own_acc(IC<a0 + 1>{}, sg, qg);
      if constexpr (s == 8) { hA[0] = trfrag(hp, 1024); hA[1] = trfrag(hp + 2048, 1024); }
      if constexpr (s == 20) { hB[0] = trfrag(hp, 1024); hB[1] = trfrag(hp + 2048, 1024); }
      if constexpr (s == 22) { hres[0] = dg < 5 ? hA[0] : hB[0]; hres[1] = dg < 5 ? hA[1] : hB[1]; }
      SB();
      if constexpr (last_of_group) {
        constexpr int g = s == 7 ? 1 : 2;
        if constexpr (s == 7) { own_acc(IC<18>{}, sg, qg); own_acc(IC<19>{}, sg, qg); }
        cast_vote(g, qg);
        BAR();
        if (read_vote(g)) redo(IC<(s == 7 ? 2 : 5)>{}, IC<(s == 7 ? 5 : 8)>{}, g, sg, qg);
        sum += sg; ssq += qg; sg = 0; qg = 0;
        sfor<0, 3>([&](auto i) { ot_rd(IC<s + 1>{}, i); });
        if constexpr (s == 19) { w2_dma(4); w2_dma(5); w2_dma(6); }
        SB();
      }
      if constexpr (s == 28) {
        const float sa = sum32x(sum), qa = sum32x(ssq);
        const float m = sa * (1.0f / D);
        const float var = fmaxf(qa * (1.0f / D) - m * m, 0.0f);
        const float sd = rbf(__builtin_sqrtf(rbf(rbf(var) + 1e-5f)));
        if (lane < 32) *(LAS v2f*)(sm + L_MR + (w * 32 + lane) * 8) = v2f{rbf(m), __builtin_amdgcn_rcpf(sd)};
        SB();
      }
    });
  }
  WAITV(0);
  BAR();
  STAMP(10);
  const u32 sAct = WS_ACT + ((mt * 4) * 112 + j * 7) * 1024;
  const u32 sWD = WS_WT2 + dg * 229376 + w * 57344;
  const u32 sAin = WS_ACT + ((mt * 4 + 2 * e2) * 112 + 28 * w) * 1024;
  v4u acte[8][2], actl[8][2], actb[12][2];
  auto ld_early = [&](auto i, auto k) { ld_vgpr<(i & 1) * 1024>(acte[i][k], srd, lane16, sAin + (k * 112 + 7 * (i >> 1) + 2 * e2) * 1024); };
  auto ld_late = [&](auto i, auto k) { ld_vgpr<(i & 1) * 1024>(actl[i][k], srd, lane16, sAin + (k * 112 + 7 * (i >> 1) + 2 - 2 * e2) * 1024); };
  auto ld_b = [&](auto i, auto k) { ld_vgpr<(i % 3) * 1024>(actb[i][k], srd, lane16, sAin + (k * 112 + 7 * (i / 3) + 4) * 1024); };
  const int hh = lane >> 5;
  v2f mr[4];
  {
#pragma unroll
    for (int i = 0; i < 4; ++i) mr[i] = *(LAS v2f*)(sm + L_MR + ((w ^ i) * 32 + (lane & 31)) * 8);
    LAS char* cp = sm + L_CS + (w * 16 + 4 * hh) * 4;
    const v4f cg0 = *(LAS v4f*)cp, cg1 = *(LAS v4f*)(cp + 32), cu0 = *(LAS v4f*)(cp + 448), cu1 = *(LAS v4f*)(cp + 480);
    LAS char* wp[3] = {sm + lane16, sm + 65536 + lane16, sm + L_W2 + lane16};
    v4u w2[2][3];
    auto w2_rd = [&](auto s, auto i) {
      constexpr int g = s >> 2;
      constexpr int off = (s & 3) * 3072 + i * 1024;
      if constexpr (g < 2) w2[s & 1][i] = *(LAS v4u*)(wp[2] + g * 12288 + off);
      else if constexpr (g < 6) w2[s & 1][i] = *(LAS v4u*)(wp[0] + (g - 2) * 16384 + off);
      else w2[s & 1][i] = *(LAS v4u*)(wp[1] + (g - 6) * 16384 + off);
    };
    sfor<0, 3>([&](auto i) { w2_rd(IC<0>{}, i); });
    float rl[4], r2[4];
#pragma unroll
    for (int i = 0; i < 4; ++i) { rl[i] = mr[i][1] * -1.44269504088896340736f; r2[i] = mr[i][1] * mr[i][1]; }
    SB();
    v4u o;
    float prev = 0;
    sfor<0, 32>([&](auto s) {
      constexpr int un = s >> 3, e = s & 7;
      mfma_avv<A_ACC2, s == 0>(w2[s & 1][0], own[s]);
      if constexpr (s < 31) w2_rd(IC<s + 1>{}, IC<0>{});
      ld_agpr<A_W + 4 * s, (s & 3) * 1024>(srd, lane16, sWD + ((s < 16 ? e2 : 1 - e2) * 16 + (s & 12)) * 1024);
      const float tg = __builtin_fmaf(-mr[un][0], e < 4 ? cg0[e & 3] : cg1[e & 3], accrd<A_ACC1 + 16 * un + e>());
      const float tu = __builtin_fmaf(-mr[un][0], e < 4 ? cu0[e & 3] : cu1[e & 3], accrd<A_ACC1 + 16 * un + 8 + e>());
      SB();
      mfma_avv<A_ACC2 + 16, s == 0>(w2[s & 1][1], own[s]);
      if constexpr (s < 31) w2_rd(IC<s + 1>{}, IC<1>{});
      if constexpr (s >= 24) ld_early(IC<2 * (s & 3)>{}, IC<(s >= 28)>{});
      const float ex = __builtin_amdgcn_exp2f(tg * rl[un]);
      const float p = tg * tu * r2[un];
      const float dn = 1.0f + ex;
      SB();
      mfma_avv<A_ACC2 + 32, s == 0>(w2[s & 1][2], own[s]);
      if constexpr (s < 31) w2_rd(IC<s + 1>{}, IC<2>{});
      if constexpr (s >= 24) ld_early(IC<2 * (s & 3) + 1>{}, IC<(s < 28)>{});
      const float v = p * __builtin_amdgcn_rcpf(dn);
      if constexpr (e & 1) o[e >> 1] = min(pkbf(prev, v), 0xfffffffeu);
      else prev = v;
      if constexpr (e == 7) __builtin_amdgcn_raw_buffer_store_b128(o, rs, lane16, sAct + ((w ^ un) * 112 + w) * 1024, 16);
      SB();
    });
  }
  STAMP(11);
  asm volatile("s_nop 7\n\ts_nop 3" ::: "memory");
  sfor<0, 3>([&](auto un) {
    LAS char* cp = sm + L_CS + ((4 + un) * 16 + 4 * hh) * 4;
    const v4f cg0 = *(LAS v4f*)cp, cg1 = *(LAS v4f*)(cp + 32), cu0 = *(LAS v4f*)(cp + 448), cu1 = *(LAS v4f*)(cp + 480);
    float g[8], u[8];
    sfor<0, 8>([&](auto e) {
      g[e] = __builtin_fmaf(-mr[0][0], e < 4 ? cg0[e & 3] : cg1[e & 3], accrd<A_ACC2 + 16 * un + e>()) * mr[0][1];
      u[e] = __builtin_fmaf(-mr[0][0], e < 4 ? cu0[e & 3] : cu1[e & 3], accrd<A_ACC2 + 16 * un + 8 + e>()) * mr[0][1];
    });
    v4u o;
    silu_mul8(g, u, o);
    __builtin_amdgcn_raw_buffer_store_b128(o, rs, lane16, sAct + (w * 112 + 4 + un) * 1024, 16);
  });
  SB();
  BARX();
  STAMP(12);
  sfor<0, 8>([&](auto i) { ld_late(i, IC<(i & 1)>{}); });
  sfor<0, 8>([&](auto i) { ld_late(i, IC<(i & 1) ^ 1>{}); });
  auto mx = [&](u32 m, const v4u& v) { return max(max(m, max(v[0], v[1])), max(v[2], v[3])); };
#define PIN8(a) "+v"(a[0][0]), "+v"(a[0][1]), "+v"(a[1][0]), "+v"(a[1][1]), "+v"(a[2][0]), "+v"(a[2][1]), "+v"(a[3][0]), "+v"(a[3][1]), \
                "+v"(a[4][0]), "+v"(a[4][1]), "+v"(a[5][0]), "+v"(a[5][1]), "+v"(a[6][0]), "+v"(a[6][1]), "+v"(a[7][0]), "+v"(a[7][1])
#define PIN4(a) "+v"(a[8][0]), "+v"(a[8][1]), "+v"(a[9][0]), "+v"(a[9][1]), "+v"(a[10][0]), "+v"(a[10][1]), "+v"(a[11][0]), "+v"(a[11][1])
  auto g2step = [&](auto first, auto aw, const v4u& b0, const v4u& b1, auto&& ride) {
    mfma_aav<A_ACC1, aw, first>(b0); ride(IC<0>{}); SB();
    mfma_aav<A_ACC1 + 16, aw, first>(b1); ride(IC<1>{}); SB();
    mfma_aav<A_ACC1 + 32, aw + 4, first>(b0); ride(IC<2>{}); SB();
    mfma_aav<A_ACC1 + 48, aw + 4, first>(b1); ride(IC<3>{}); SB();
  };
  asm volatile("s_waitcnt vmcnt(20)" : PIN8(acte) :: "memory");
  for (int spin = 0; spin < SPIN_CAP; ++spin) {
    u32 m = 0;
    sfor<0, 8>([&](auto i) { m = mx(mx(m, acte[i][0]), acte[i][1]); });
    if (!__any(m == ~0u)) break;
    sfor<0, 8>([&](auto i) { ld_early(i, IC<0>{}); ld_early(i, IC<1>{}); });
    asm volatile("s_waitcnt vmcnt(0)" : PIN8(acte) :: "memory");
  }
  SB();
  STAMP(13);
  sfor<0, 8>([&](auto i) {
    g2step(IC<(i == 0)>{}, IC<A_W + 8 * i>{}, acte[i][0], acte[i][1], [&](auto k) {
      if constexpr (i > 0 && k < 2) ld_agpr<A_W + 8 * (i - 1) + 4 * k, ((2 * (i - 1) + k) & 3) * 1024>(srd, lane16, sWD + 32768 + ((2 * (i - 1)) >> 2) * 4096);
    });
  });
  ld_agpr<A_W + 56, 2048>(srd, lane16, sWD + 32768 + 3 * 4096);
  ld_agpr<A_W + 60, 3072>(srd, lane16, sWD + 32768 + 3 * 4096);
  asm volatile("s_waitcnt vmcnt(16)" : PIN8(actl) :: "memory");
  for (int spin = 0; spin < SPIN_CAP; ++spin) {
    u32 m = 0;
    sfor<0, 8>([&](auto i) { m = mx(mx(m, actl[i][0]), actl[i][1]); });
    if (!__any(m == ~0u)) break;
    sfor<0, 8>([&](auto i) { ld_late(i, IC<0>{}); ld_late(i, IC<1>{}); });
    asm volatile("s_waitcnt vmcnt(0)" : PIN8(actl) :: "memory");
  }
  SB();
  sfor<0, 8>([&](auto i) {
    g2step(IC<0>{}, IC<A_W + 64 + 8 * i>{}, actl[i][0], actl[i][1], [&](auto k) {
      if constexpr (i < 6) ld_b(IC<2 * i + (k >> 1)>{}, IC<(k & 1)>{});
      if constexpr (i >= 1 && i < 5 && k < 2) ld_agpr<A_W + 64 + 8 * (i - 1) + 4 * k, ((2 * (i - 1) + k) & 3) * 1024>(srd, lane16, sWD + 32768 + 16384 + ((2 * (i - 1)) >> 2) * 4096);
    });
  });
  asm volatile("s_waitcnt vmcnt(0)" : PIN8(actb) :: "memory");
  asm volatile("" : PIN4(actb));
  for (int spin = 0; spin < SPIN_CAP; ++spin) {
    u32 m = 0;
    sfor<0, 12>([&](auto i) { m = mx(mx(m, actb[i][0]), actb[i][1]); });
    if (!__any(m == ~0u)) break;
    sfor<0, 12>([&](auto i) { ld_b(i, IC<0>{}); ld_b(i, IC<1>{}); });
    asm volatile("s_waitcnt vmcnt(0)" : PIN8(actb) :: "memory");
    asm volatile("" : PIN4(actb));
  }
  SB();
  sfor<0, 12>([&](auto i) { g2step(IC<0>{}, IC<A_W + 8 * i>{}, actb[i][0], actb[i][1], [&](auto) {}); });
  STAMP(14);
  asm volatile("s_nop 7\n\ts_nop 3" ::: "memory");
  float a[16];
  auto xch = [&](auto W) {
    const u32 ad = W * 4096 + lane16;
    sfor<0, 4>([&](auto k) {
      if constexpr (k != W) {
        sfor<0, 4>([&](auto e) {
          dsw_agpr<A_ACC1 + 16 * k + 4 * e, k * 16384 + e * 1024>(ad);
        });
      }
    });
    sfor<0, 16>([&](auto e) { a[e] = accrd<A_ACC1 + 16 * W + e>(); });
  };
  if (w == 0) xch(IC<0>{}); else if (w == 1) xch(IC<1>{}); else if (w == 2) xch(IC<2>{}); else xch(IC<3>{});
  BAR();
#pragma unroll
  for (int k = 1; k < 4; ++k) {
#pragma unroll
    for (int e = 0; e < 4; ++e) {
      const v4f p = *(LAS v4f*)(sm + w * 16384 + (w ^ k) * 4096 + e * 1024 + lane16);
#pragma unroll
      for (int c = 0; c < 4; ++c) a[4 * e + c] += p[c];
    }
  }
  {
    u32 o[8];
#pragma unroll
    for (int e = 0; e < 8; ++e) {
      const u32 hv = hres[e >> 2][e & 3];
      const u32 f = pkbf(a[2 * e], a[2 * e + 1]);
      o[e] = pkbf(bflo(hv) + bflo(f), bfhi(hv) + bfhi(f));
    }
    u16* op = out + (size_t)(128 * mt + 64 * e2 + 32 * (w & 1) + (lane & 31)) * D + 64 * dg + 32 * (w >> 1) + 16 * hh;
    *(v4u*)op = v4u{o[0], o[1], o[2], o[3]};
    *(v4u*)(op + 8) = v4u{o[4], o[5], o[6], o[7]};
  }
  STAMP(15);
  asm volatile(".Lkf_end:");
}

extern "C" void kernel_launch(void* const* d_in, const int* in_sizes, int n_in, void* d_out, int out_size, void* d_ws, size_t ws_size,
                              hipStream_t stream) {
  (void)hipFuncSetAttribute((const void*)k_fused, hipFuncAttributeMaxDynamicSharedMemorySize, LDS_B);
  k_fused<<<256, 256, LDS_B, stream>>>((const u16*)d_in[0], (const u16*)d_in[1], (const u16*)d_in[2], (const u16*)d_in[3], (const u16*)d_in[4],
                                       (const u16*)d_in[5], (const u16*)d_in[6], (const u16*)d_in[8], (const u16*)d_in[9],
                                       (const u16*)d_in[10], (char*)d_ws, (u16*)d_out);
}
```

```cpp
#include <hip/hip_runtime.h>
#include <hip/hip_bf16.h>
#include <stdint.h>
#include <type_traits>

typedef unsigned short u16;
typedef unsigned int u32;
typedef unsigned long long u64;
typedef short v4s __attribute__((ext_vector_type(4)));
typedef __bf16 v2bf __attribute__((ext_vector_type(2)));
typedef _Float16 v2h __attribute__((ext_vector_type(2)));
typedef float v2f __attribute__((ext_vector_type(2)));
typedef float v4f __attribute__((ext_vector_type(4)));
typedef u32 v2u __attribute__((ext_vector_type(2)));
typedef u32 v4u __attribute__((ext_vector_type(4)));
#define LAS __attribute__((address_space(3)))
#define GAS __attribute__((address_space(1)))
#define DEV __device__ __forceinline__

constexpr int L = 2048, D = 512, NS = 128, F = 1792;

constexpr u32 WS_EPOCH = 0;
constexpr u32 WS_WFLAG = 4096;
constexpr u32 WS_GRAN  = 64 << 10;
constexpr u32 WS_CSUM  = 256 << 10;
constexpr u32 WS_HT    = 1 << 20;
constexpr u32 WS_WT1   = 4 << 20;
constexpr u32 WS_WT2   = 8 << 20;
constexpr u32 WS_ACT   = 12 << 20;
constexpr u32 WS_XG    = 20 << 20;
constexpr u32 WS_USED  = 24 << 20;
constexpr u32 WS_FIX   = 4 << 20;
constexpr size_t WS_ROT = 40 << 20;
constexpr size_t WS_DBG = 32 << 20;

template <int I> using IC = std::integral_constant<int, I>;
template <int B, int E, class Fn> DEV void sfor(Fn&& f) {
  if constexpr (B < E) { f(IC<B>{}); sfor<B + 1, E>(f); }
}

DEV float bf2f(u32 b) { return __uint_as_float(b << 16); }
DEV float bflo(u32 p) { return __uint_as_float(p << 16); }
DEV float bfhi(u32 p) { return __uint_as_float(p & 0xffff0000u); }
DEV u32 pkbf(float a, float b) { v2f f = {a, b}; return __builtin_bit_cast(u32, __builtin_convertvector(f, v2bf)); }
DEV u32 pkh(float a, float b) { v2f f = {a, b}; return __builtin_bit_cast(u32, __builtin_convertvector(f, v2h)); }
DEV float rbf(float a) { return bflo(pkbf(a, a)); }
DEV float dot2bf(u32 a, u32 b, float c) { return __builtin_amdgcn_fdot2_f32_bf16(__builtin_bit_cast(v2bf, a), __builtin_bit_cast(v2bf, b), c, false); }
DEV v2u trread(LAS char* p) { return __builtin_bit_cast(v2u, __builtin_amdgcn_ds_read_tr16_b64_v4i16((LAS v4s*)p)); }
DEV v4u trfrag(LAS char* p, int second) { const v2u a = trread(p), b = trread(p + second); return v4u{a[0], a[1], b[0], b[1]}; }
#define WAITV(n) asm volatile("s_waitcnt vmcnt(%0)" :: "i"(n) : "memory")
#define WAITL0() asm volatile("s_waitcnt lgkmcnt(0)" ::: "memory")
#define BAR() asm volatile("s_waitcnt lgkmcnt(0)\n\ts_barrier" ::: "memory")
#define BARX() asm volatile("s_barrier" ::: "memory")
#define SB() __builtin_amdgcn_sched_barrier(0)
constexpr int SPIN_CAP = 1 << 20;

#define STAMP(slot) do {} while (0)
#define COUNT(slot) do {} while (0)

template <int CTRL> DEV float dpp(float v) { return __builtin_bit_cast(float, __builtin_amdgcn_update_dpp(0, __builtin_bit_cast(int, v), CTRL, 0xf, 0xf, true)); }
DEV float swap16_other(float v, float& o) { auto r = __builtin_amdgcn_permlane16_swap(__float_as_uint(v), __float_as_uint(v), false, false); o = __uint_as_float(r[1]); return __uint_as_float(r[0]); }
DEV float swap32_other(float v, float& o) { auto r = __builtin_amdgcn_permlane32_swap(__float_as_uint(v), __float_as_uint(v), false, false); o = __uint_as_float(r[1]); return __uint_as_float(r[0]); }
DEV float sum16x(float v) { float o; const float a = swap16_other(v, o); return a + o; }
DEV float sum32x(float v) { float o; const float a = swap32_other(v, o); return a + o; }
DEV float wave_sum(float v) {
  v += dpp<0xB1>(v); v += dpp<0x4E>(v); v += dpp<0x141>(v); v += dpp<0x140>(v);
  return sum32x(sum16x(v));
}
DEV float wave_max(float v) {
  v = fmaxf(v, dpp<0xB1>(v)); v = fmaxf(v, dpp<0x4E>(v)); v = fmaxf(v, dpp<0x141>(v)); v = fmaxf(v, dpp<0x140>(v));
  float o; float a = swap16_other(v, o); v = fmaxf(a, o); a = swap32_other(v, o); return fmaxf(a, o);
}

DEV void dma16(v4u srd, u32 voff, u32 soff, u32 lds) {
  asm volatile("s_mov_b32 m0, %3\n\ts_nop 0\n\tbuffer_load_dwordx4 %0, %1, %2 offen sc1 lds" :: "v"(voff), "s"(srd), "s"(soff), "s"(lds) : "memory");
}
template <int A, int IMM> DEV void ld_agpr(v4u srd, u32 voff, u32 soff) {
  asm volatile("buffer_load_dwordx4 a[%c3:%c4], %0, %1, %2 offen offset:%c5 sc1" :: "v"(voff), "s"(srd), "s"(soff), "i"(A), "i"(A + 3), "i"(IMM) : "memory");
}
template <int IMM> DEV void ld_vgpr(v4u& d, v4u srd, u32 voff, u32 soff) {
  asm volatile("buffer_load_dwordx4 %0, %1, %2, %3 offen offset:%c4 sc1" : "=v"(d) : "v"(voff), "s"(srd), "s"(soff), "i"(IMM) : "memory");
}
template <int ACC, int A, bool FIRST> DEV void mfma_aav(const v4u& b) {
  if constexpr (FIRST) asm volatile("v_mfma_f32_32x32x16_bf16 a[%c1:%c2], a[%c3:%c4], %0, 0" :: "v"(b), "i"(ACC), "i"(ACC + 15), "i"(A), "i"(A + 3));
  else asm volatile("v_mfma_f32_32x32x16_bf16 a[%c1:%c2], a[%c3:%c4], %0, a[%c1:%c2]" :: "v"(b), "i"(ACC), "i"(ACC + 15), "i"(A), "i"(A + 3));
}
template <int ACC, bool FIRST> DEV void mfma_avv(const v4u& a, const v4u& b) {
  if constexpr (FIRST) asm volatile("v_mfma_f32_32x32x16_bf16 a[%c2:%c3], %0, %1, 0" :: "v"(a), "v"(b), "i"(ACC), "i"(ACC + 15));
  else asm volatile("v_mfma_f32_32x32x16_bf16 a[%c2:%c3], %0, %1, a[%c2:%c3]" :: "v"(a), "v"(b), "i"(ACC), "i"(ACC + 15));
}
template <int A, int OFF> DEV void dsw_agpr(u32 ad) {
  asm volatile("ds_write_b128 %0, a[%c1:%c2] offset:%c3" :: "v"(ad), "i"(A), "i"(A + 3), "i"(OFF) : "memory");
}
template <int OFF> DEV void dsw_halves(u32 ad, u32 v) {
  asm volatile("ds_write_b16 %0, %1 offset:%c2\n\tds_write_b16_d16_hi %0, %1 offset:%c3" :: "v"(ad), "v"(v), "i"(OFF), "i"(OFF + 2) : "memory");
}
DEV void ld_flag(u32& d, v4u srd, u32 voff, u32 soff) {
  asm volatile("buffer_load_dword %0, %1, %2, %3 offen sc1" : "=v"(d) : "v"(voff), "s"(srd), "s"(soff) : "memory");
}
DEV void wait0_pin(u32& d) { asm volatile("s_waitcnt vmcnt(0)" : "+v"(d) :: "memory"); }
DEV void pin2(float& a, float& b) { asm volatile("" : "+v"(a), "+v"(b)); }
template <int A> DEV float accrd() { float v; asm volatile("v_accvgpr_read_b32 %0, a%c1" : "=v"(v) : "i"(A)); return v; }
DEV void mfma16(v4f& c, const v4u& a, const v4u& b) { asm volatile("v_mfma_f32_16x16x32_f16 %0, %1, %2, %0" : "+v"(c) : "v"(a), "v"(b)); }
DEV void mfma16z(v4f& c, const v4u& a, const v4u& b) { asm volatile("v_mfma_f32_16x16x32_f16 %0, %1, %2, 0" : "=v"(c) : "v"(a), "v"(b)); }
constexpr int A_W = 0, A_ACC1 = 128, A_ACC2 = 192;

constexpr int TCOL = 528, NCOL = 63, TAB_B = NCOL * TCOL;
constexpr int KC_B = 4640, UC_B = 4224;
constexpr int OFF_K = TAB_B, OFF_U = OFF_K + 4 * KC_B, OFF_X = OFF_U + 2 * UC_B, CH_B = OFF_X + 4096;
constexpr int L_RT1 = 2 * CH_B, L_RT2 = L_RT1 + 128 * 144, L_MISC = L_RT2 + 64 * 144;
constexpr int L_W2 = 131072;
constexpr int L_CSP = L_MISC, L_CS = L_CSP + 4096, L_MR = L_CS + 1024, L_VOTE = L_MR + 1024, LDS_B = L_VOTE + 64;
static_assert(L_W2 >= L_RT1 && L_W2 + 24576 <= L_MISC && LDS_B <= 163840 && L_MISC % 16 == 0, "lds map");

typedef v2f cf;
DEV void cmul2(cf& r0, cf a0, cf b0, cf& r1, cf a1, cf b1) {
  asm("v_pk_mul_f32 %0, %2, %3 op_sel_hi:[0,1]\n\t"
      "v_pk_mul_f32 %1, %4, %5 op_sel_hi:[0,1]\n\t"
      "v_pk_fma_f32 %0, %2, %3, %0 op_sel:[1,1,0] op_sel_hi:[1,0,1] neg_lo:[1,0,0]\n\t"
      "v_pk_fma_f32 %1, %4, %5, %1 op_sel:[1,1,0] op_sel_hi:[1,0,1] neg_lo:[1,0,0]"
      : "=&v"(r0), "=&v"(r1) : "v"(a0), "v"(b0), "v"(a1), "v"(b1));
}
DEV cf cexp_p(float ar, float ai, float p) {
  const float e = __builtin_amdgcn_exp2f(ar * p * 1.44269504088896340736f);
  const float xx = ai * p;
  const float chi = 0.15915493667125702f, clo = 6.4206383e-09f;
  const float th = xx * chi;
  float tl = __builtin_fmaf(xx, chi, -th);
  tl = __builtin_fmaf(xx, clo, tl);
  const float fr = (th - __builtin_rintf(th)) + tl;
  return cf{e * __builtin_amdgcn_cosf(fr), e * __builtin_amdgcn_sinf(fr)};
}
template <int CTRL> DEV u32 dppu(u32 v) { return (u32)__builtin_amdgcn_update_dpp(0, (int)v, CTRL, 0xf, 0xf, true); }
DEV u32 wave_umax(u32 v) {
  v = max(v, dppu<0xB1>(v)); v = max(v, dppu<0x4E>(v)); v = max(v, dppu<0x141>(v)); v = max(v, dppu<0x140>(v));
  auto r = __builtin_amdgcn_permlane16_swap(v, v, false, false); v = max(r[0], r[1]);
  auto q = __builtin_amdgcn_permlane32_swap(v, v, false, false); return max(q[0], q[1]);
}

struct ConvSched { int nm, ma[18], mq[18], mk[18]; int nr, ru[16], ri[16]; };
constexpr ConvSched mk_sched(int c0, int c1, int c2, int c3) {
  ConvSched s{};
  const int c[4] = {c0, c1, c2, c3};
  bool hu[8] = {}, hk[8] = {};
  for (int q = 0; q <= c3; ++q)
    for (int a = 3; a >= 0; --a)
      if (q <= c[a]) {
        const int k = c[a] - q;
        s.ma[s.nm] = a; s.mq[s.nm] = q; s.mk[s.nm] = k; ++s.nm;
        if (!hu[q]) { hu[q] = true; s.ru[s.nr] = 1; s.ri[s.nr] = q; ++s.nr; }
        if (!hk[k]) { hk[k] = true; s.ru[s.nr] = 0; s.ri[s.nr] = k; ++s.nr; }
      }
  return s;
}
template <int V> struct CS { static constexpr ConvSched s = V == 0 ? mk_sched(0, 3, 4, 7) : mk_sched(1, 2, 5, 6); };

template <int V, class Ex> DEV void conv_main(LAS char* ub, LAS char* kb, v4f (&acc)[4], Ex&& extra) {
  constexpr ConvSched S = CS<V>::s;
  v4u U[2][8], K[2][8];
  sfor<0, S.nr>([&](auto i) {
    if constexpr (S.ru[i]) U[0][S.ri[i]] = *(LAS v4u*)(ub + 512 * S.ri[i]);
    else K[0][S.ri[i]] = *(LAS v4u*)(kb - 512 * S.ri[i]);
  });
  SB();
  sfor<0, 8>([&](auto r) {
    constexpr int cur = r & 1, nxt = cur ^ 1;
    sfor<0, 18>([&](auto m) {
      if constexpr (r == 0 && S.mq[m] == 0) mfma16z(acc[S.ma[m]], U[cur][S.mq[m]], K[cur][S.mk[m]]);
      else mfma16(acc[S.ma[m]], U[cur][S.mq[m]], K[cur][S.mk[m]]);
      if constexpr (r < 7 && m < S.nr) {
        if constexpr (S.ru[m]) U[nxt][S.ri[m]] = *(LAS v4u*)(ub + 64 * (r + 1) + 512 * S.ri[m]);
        else K[nxt][S.ri[m]] = *(LAS v4u*)(kb + 64 * (r + 1) - 512 * S.ri[m]);
      }
      extra(r, m);
      SB();
    });
  });
  asm volatile("s_nop 7" : "+v"(acc[0]), "+v"(acc[1]), "+v"(acc[2]), "+v"(acc[3]));
}

DEV void silu_mul8(const float (&g)[8], const float (&u)[8], v4u& o) {
  float r[8];
#pragma unroll
  for (int e = 0; e < 8; ++e) r[e] = g[e] * __builtin_amdgcn_rcpf(1.0f + __builtin_amdgcn_exp2f(g[e] * -1.44269504088896340736f)) * u[e];
#pragma unroll
  for (int e = 0; e < 4; ++e) o[e] = min(pkbf(r[2 * e], r[2 * e + 1]), 0xfffffffeu);
}

__global__ __launch_bounds__(256, 1) void k_fused(const u16* __restrict__ x, const u16* __restrict__ lar_, const u16* __restrict__ aim_,
                                                  const u16* __restrict__ cre_, const u16* __restrict__ cim_, const u16* __restrict__ ldt_,
                                                  const u16* __restrict__ dsk_, const u16* __restrict__ wg, const u16* __restrict__ wu,
                                                  const u16* __restrict__ wd, char* __restrict__ ws, u16* __restrict__ out) {
  extern __shared__ __attribute__((aligned(16))) char smx[];
  LAS char* sm = (LAS char*)smx;
  asm volatile("" ::: "a0", "a255");
  const int t = threadIdx.x, lane = t & 63, w = __builtin_amdgcn_readfirstlane(t >> 6), b = blockIdx.x;
  STAMP(0);
  const int xcd = b & 7, bi = b >> 3;
  const auto rs0 = __builtin_amdgcn_make_buffer_rsrc(ws, 0, WS_FIX, 0x00020000);
  const v4u srd0 = {(u32)(uintptr_t)ws, (u32)((uintptr_t)ws >> 32) & 0xffffu, WS_FIX, 0x00020000u};
  const int pr = xcd * 32 + bi;
  const int cw = w >> 1, nh = w & 1, ch = 2 * pr + cw;
  const int mt = 4 * (xcd >> 1) + (bi & 3), j = 8 * (xcd & 1) + (bi >> 2);
  const int e2 = j & 1, dg = j >> 1;
  const int G = lane >> 4, q4 = (lane >> 2) & 3, p4 = lane & 3, i16 = lane & 15;
  const u32 lane16 = lane * 16;
  LAS char* cb = sm + cw * CH_B;

  v4u xa[2];
#pragma unroll
  for (int i = 0; i < 2; ++i) xa[i] = *(const v4u*)(x + (size_t)(8 * b + 2 * w + i) * D + lane * 8);
  u32 xq[4][2];
#pragma unroll
  for (int i = 0; i < 4; ++i)
#pragma unroll
    for (int k = 0; k < 2; ++k) xq[i][k] = *(const u32*)(x + (size_t)(8 * b + 2 * (lane & 3) + k) * D + 2 * (64 * w + 16 * i + (lane >> 2)));
  const u32 ep = __builtin_amdgcn_raw_buffer_load_b32(rs0, WS_EPOCH, 0, 16);
  const int n = 64 * nh + lane;
  const u32 r_lar = lar_[ch * NS + n], r_aim = aim_[ch * NS + n], r_cre = cre_[ch * NS + n], r_cim = cim_[ch * NS + n];
  const u32 r_cre2 = cre_[ch * NS + (n ^ 64)], r_cim2 = cim_[ch * NS + (n ^ 64)];
  const u32 r_ldt = ldt_[ch], r_dsk = dsk_[ch];
  SB();
  {
    const v4u s = {~0u, ~0u, ~0u, ~0u};
#pragma unroll
    for (int i = 0; i < 2; ++i) __builtin_amdgcn_raw_buffer_store_b128(s, rs0, t * 16 + i * 4096, WS_HT + pr * 8192, 16);
  }
  SB();
  const int rb = b < 224 ? b : 223;
  const int mat = rb / 112, cg = (rb % 112) % 28, rq = (rb % 112) / 28, dgp = rb & 7, fb = rb >> 3;
  v4u wa[4], wb[2];
  {
    const u16* W = mat ? wu : wg;
#pragma unroll
    for (int i = 0; i < 4; ++i) wa[i] = *(const v4u*)(W + (size_t)(128 * rq + (t >> 3) + 32 * i) * F + 64 * cg + (t & 7) * 8);
#pragma unroll
    for (int i = 0; i < 2; ++i) wb[i] = *(const v4u*)(wd + (size_t)(64 * fb + (t >> 3) + 32 * i) * D + 64 * dgp + (t & 7) * 8);
  }
  u32 pfv;
  {
    u64 pc; u32 len;
    asm volatile("s_getpc_b64 %0\n.Lkf_here:\n\ts_mov_b32 %1, .Lkf_end-.Lkf_here" : "=s"(pc), "=s"(len));
    const u32 off = min((u32)((bi * 32 + (t & 31)) * 64), len);
    asm volatile("global_load_dword %0, %1, %2" : "=v"(pfv) : "v"(off), "s"(pc));
  }
  SB();
  {
    const v4u z = {0, 0, 0, 0};
    const int tt = t & 127;
    LAS char* c2 = sm + (t >> 7) * CH_B;
    *(LAS v4u*)(c2 + OFF_K + (tt / 33) * KC_B + (255 + tt % 33) * 16) = z;
    if (tt < 4) *(LAS v4u*)(c2 + OFF_K + 3 * KC_B + (255 + 29 + tt) * 16) = z;
    if (tt < 16) *(LAS v4u*)(c2 + OFF_U + (tt >> 3) * UC_B + (tt & 7) * 16) = z;
  }
  const u32 tag = __builtin_amdgcn_readfirstlane(ep) + 1u;
  char* const wr = ws + WS_ROT + (size_t)(tag & 7u) * WS_USED;
  const auto rs = __builtin_amdgcn_make_buffer_rsrc(wr, 0, WS_USED, 0x00020000);
  const v4u srd = {(u32)(uintptr_t)wr, (u32)((uintptr_t)wr >> 32) & 0xffffu, WS_USED, 0x00020000u};
#pragma unroll
  for (int i = 0; i < 4; ++i)
    __builtin_amdgcn_raw_buffer_store_b128(v4u{xq[i][0], tag, xq[i][1], tag}, rs, (16 * i + (lane >> 2)) * 16384 + (lane & 3) * 16, WS_XG + w * (64 * 16384) + b * 64, 16);
  SB();
  {
    float v[2][8], s[2], q[2];
#pragma unroll
    for (int i = 0; i < 2; ++i) {
#pragma unroll
      for (int k = 0; k < 4; ++k) { v[i][2 * k] = bflo(xa[i][k]); v[i][2 * k + 1] = bfhi(xa[i][k]); }
      s[i] = ((v[i][0] + v[i][1]) + (v[i][2] + v[i][3])) + ((v[i][4] + v[i][5]) + (v[i][6] + v[i][7]));
    }
#pragma unroll
    for (int i = 0; i < 2; ++i) s[i] = wave_sum(s[i]) * (1.0f / D);
#pragma unroll
    for (int i = 0; i < 2; ++i) {
      q[i] = 0;
#pragma unroll
      for (int k = 0; k < 8; ++k) { const float d = v[i][k] - s[i]; q[i] += d * d; }
    }
#pragma unroll
    for (int i = 0; i < 2; ++i) q[i] = wave_sum(q[i]) * (1.0f / D);
    const float m = (lane & 1) ? s[1] : s[0], vv = (lane & 1) ? q[1] : q[0];
    const u32 pk = pkbf(m, __builtin_sqrtf(rbf(rbf(vv) + 1e-5f)));
    SB();
    WAITV(11);
    if (lane < 16) __builtin_amdgcn_raw_buffer_store_b64(v2u{pk, tag}, rs, (lane >> 1) * 16384 + (lane & 1) * 8, WS_GRAN + (8 * b + 2 * w) * 8, 16);
    SB();
  }
  STAMP(1);
  const float dt = rbf(expf(bf2f(r_ldt)));
  const float ar = -rbf(expf(bf2f(r_lar))) * dt, ai = bf2f(r_aim) * dt;
  int ex = (int)(wave_umax(max(max(r_cre & 0x7fffu, r_cim & 0x7fffu), max(r_cre2 & 0x7fffu, r_cim2 & 0x7fffu))) >> 7);
  ex = ex < 8 ? 8 : (ex > 250 ? 250 : ex);
  const float scl = __uint_as_float((u32)(257 - ex) << 23);
  const float yscl = 2.0f * dt * __uint_as_float((u32)(ex - 3) << 23);
  {
    LAS char* tb = cb + n * 4;
#define PUT(col, v) *(LAS u32*)(tb + (col) * TCOL) = pkh((v)[0], (v)[1])
    const v4u sent = {~0u, ~0u, ~0u, ~0u};
#define SENT(i) do { SB(); __builtin_amdgcn_raw_buffer_store_b128(sent, rs, lane16 + ((i) & 3) * 1024, WS_ACT + ((mt * 4 + w) * 112 + j * 7 + ((i) & 4)) * 1024, 16); SB(); } while (0)
    cf zp[17], zb[8], a[16], pa, pb;
    zp[1] = cexp_p(ar, ai, 1.0f); zp[16] = cexp_p(ar, ai, 16.0f); zb[1] = cexp_p(ar, ai, 256.0f);
    const cf wc = {zp[16][0], -zp[16][1]};
    a[0] = cf{bf2f(r_cre) * scl, -bf2f(r_cim) * scl};
    *(LAS u32*)(tb + 16 * TCOL) = 0x00003c00u;
    PUT(17, zp[1]); PUT(32, zb[1]); PUT(0, a[0]);
    SENT(0);
    cmul2(zp[2], zp[1], zp[1], zb[2], zb[1], zb[1]);
    cmul2(zp[3], zp[2], zp[1], zp[4], zp[2], zp[2]);
    cmul2(zb[3], zb[2], zb[1], zb[4], zb[2], zb[2]);
    cmul2(a[1], a[0], wc, zp[8], zp[4], zp[4]);
    PUT(18, zp[2]); PUT(19, zp[3]); PUT(20, zp[4]); PUT(41, zb[2]); PUT(46, zb[3]); PUT(51, zb[4]);
    SENT(1);
    cmul2(a[2], a[1], wc, zp[5], zp[4], zp[1]);
    cmul2(a[3], a[2], wc, zp[6], zp[4], zp[2]);
    cmul2(a[4], a[3], wc, zp[7], zp[4], zp[3]);
    PUT(1, a[1]); PUT(2, a[2]); PUT(3, a[3]); PUT(4, a[4]); PUT(21, zp[5]); PUT(22, zp[6]); PUT(23, zp[7]); PUT(24, zp[8]);
    SENT(2);
    sfor<9, 16>([&](auto i) { cmul2(a[i - 4], a[i - 5], wc, zp[i], zp[8], zp[i - 8]); PUT(i - 4, a[i - 4]); PUT(16 + i, zp[i]); });
    SENT(3);
    cmul2(a[12], a[11], wc, zb[5], zb[4], zb[1]);
    cmul2(a[13], a[12], wc, zb[6], zb[4], zb[2]);
    cmul2(a[14], a[13], wc, zb[7], zb[4], zb[3]);
    cmul2(a[15], a[14], wc, pa, zb[1], zp[16]);
    PUT(12, a[12]); PUT(13, a[13]); PUT(14, a[14]); PUT(15, a[15]); PUT(54, zb[5]); PUT(57, zb[6]); PUT(60, zb[7]); PUT(40, pa);
    SENT(4);
    sfor<0, 3>([&](auto i) { cmul2(pa, zb[1], zp[4 * i + 2], pb, zb[1], zp[4 * i + 4]); PUT(33 + 2 * i, pa); PUT(34 + 2 * i, pb); });
    cmul2(pa, zb[1], zp[14], pb, zb[1], zp[14]); PUT(39, pa);
    SENT(5);
    sfor<1, 5>([&](auto i) { cmul2(pa, zb[2], zp[4 * i], pb, zb[3], zp[4 * i]); PUT(41 + i, pa); PUT(46 + i, pb); });
    SENT(6);
    sfor<4, 8>([&](auto m) { cmul2(pa, zb[m], zp[8], pb, zb[m], zp[16]); PUT(52 + 3 * (m - 4), pa); PUT(53 + 3 * (m - 4), pb); });
#undef PUT
#undef SENT
  }
  STAMP(2);
  SB();
#pragma unroll
  for (int i = 0; i < 4; ++i) *(LAS v4u*)(sm + L_RT1 + ((t >> 3) + 32 * i) * 144 + (t & 7) * 16) = wa[i];
#pragma unroll
  for (int i = 0; i < 2; ++i) *(LAS v4u*)(sm + L_RT2 + ((t >> 3) + 32 * i) * 144 + (t & 7) * 16) = wb[i];
  BAR();
  STAMP(3);
  if (b < 224) {
    const int g16 = 4 * cg + w, jj = g16 / 7, pt = g16 % 7, h = G & 1;
    float cs = 0;
#pragma unroll
    for (int it = 0; it < 4; ++it) {
      const int ksl = 2 * it + (G >> 1), ks = 8 * rq + ksl;
      const v4u o = trfrag(sm + L_RT1 + (16 * ksl + 8 * h + q4) * 144 + 32 * w + 8 * p4, 4 * 144);
#pragma unroll
      for (int e = 0; e < 4; ++e) cs = dot2bf(o[e], 0x3f803f80u, cs);
      const int fr = pt < 4 ? pt * 32 + ks : 128 + ks * 3 + (pt - 4);
      __builtin_amdgcn_raw_buffer_store_b128(o, rs, fr * 1024 + (32 * h + 16 * mat + i16) * 16, WS_WT1 + jj * 229376, 16);
    }
    cs = sum32x(sum16x(cs));
    if (lane < 16) __builtin_amdgcn_raw_buffer_store_b32(__float_as_uint(cs), rs, lane * 4, WS_CSUM + jj * 4096 + (rq * 224 + mat * 112 + pt * 16) * 4, 16);
#pragma unroll
    for (int it = 0; it < 2; ++it) {
      const int sl = 2 * it + (G >> 1), s = 4 * fb + sl, jp = s / 7, pp = s % 7;
      const v4u o = trfrag(sm + L_RT2 + (16 * sl + 4 * h + q4) * 144 + 32 * w + 8 * p4, 8 * 144);
      const int pos = pp < 4 ? (pp >> 1) * 8 + (jp & 3) * 2 + (pp & 1) : 16 + (jp & 3) * 3 + (pp - 4);
      const int xrow = (i16 & 3) + 8 * (i16 >> 2) + 4 * (w & 1);
      __builtin_amdgcn_raw_buffer_store_b128(o, rs, (((jp >> 2) * 28 + pos) * 2 + (w >> 1)) * 1024 + (32 * h + xrow) * 16, WS_WT2 + dgp * 229376, 16);
    }
  }
  SB();
  STAMP(18);
  {
    v4u Bf[8], Af[2][8];
    const int c2 = (i16 + ((i16 >> 1) & 1)) >> 1, c4 = (i16 + 1 + ((i16 >> 2) & 1)) >> 2, c8 = (i16 + 3 + ((i16 >> 3) & 1)) >> 3;
    LAS char* ap[4];
    ap[0] = cb + (nh ? 32 + c2 : 16 + i16) * TCOL + G * 16;
    ap[1] = cb + (41 + 5 * nh + c4) * TCOL + G * 16;
    ap[2] = cb + (51 + 3 * nh + c8) * TCOL + G * 16;
    ap[3] = ap[2] + 6 * TCOL;
#pragma unroll
    for (int ks = 0; ks < 8; ++ks) { Bf[ks] = *(LAS v4u*)(cb + i16 * TCOL + ks * 64 + G * 16); Af[0][ks] = *(LAS v4u*)(ap[0] + ks * 64); }
    SB();
    v4f d[4];
    sfor<0, 4>([&](auto mm) {
      sfor<0, 8>([&](auto ks) {
        if constexpr (ks == 0) mfma16z(d[mm], Af[mm & 1][ks], Bf[ks]);
        else mfma16(d[mm], Af[mm & 1][ks], Bf[ks]);
        if constexpr (mm < 3) Af[(mm & 1) ^ 1][ks] = *(LAS v4u*)(ap[mm + 1] + ks * 64);
        SB();
      });
    });
    asm volatile("s_nop 7" : "+v"(d[0]), "+v"(d[1]), "+v"(d[2]), "+v"(d[3]));
    LAS char* kp = cb + OFF_K + 16 + (2038 - 256 * nh - 16 * i16 - 4 * G - 3 * 512) * 2;
#pragma unroll
    for (int mm = 0; mm < 4; ++mm) {
      const u32 p0 = pkh(d[mm][3], d[mm][2]), p1 = pkh(d[mm][1], d[mm][0]);
#pragma unroll
      for (int e = 0; e < 4; ++e) {
        LAS char* k2 = kp + (3 - mm) * 1024 + e * (KC_B + 4);
        if (e & 1) *(LAS v2u*)k2 = v2u{p0, p1};
        else { *(LAS u32*)k2 = p0; *(LAS u32*)(k2 + 4) = p1; }
      }
    }
  }
  WAITV(0);
  asm volatile("" :: "v"(pfv));
  if (lane == 0 && b < 224) __builtin_amdgcn_raw_buffer_store_b32(tag, rs, 0, WS_WFLAG + (b * 4 + w) * 16, 16);
  STAMP(4);
  {
    v4u g[4], gx[4];
    for (int spin = 0; spin < SPIN_CAP; ++spin) {
      asm volatile("" ::: "memory");
#pragma unroll
      for (int i = 0; i < 4; ++i) gx[i] = __builtin_amdgcn_raw_buffer_load_b128(rs, t * 16 + i * 4096, WS_XG + pr * 16384, 16);
#pragma unroll
      for (int i = 0; i < 4; ++i) g[i] = __builtin_amdgcn_raw_buffer_load_b128(rs, t * 16 + i * 4096, WS_GRAN + xcd * 16384, 16);
      u32 df = 0;
#pragma unroll
      for (int i = 0; i < 4; ++i) df |= ((g[i][1] ^ tag) | (g[i][3] ^ tag)) | ((gx[i][1] ^ tag) | (gx[i][3] ^ tag));
      if (__all(df == 0)) break;
      COUNT(0);
    }
    STAMP(5);
    LAS char* cc[2] = {sm + t * 4, sm + CH_B + t * 4};
    asm volatile("" : "+v"(cc[1]));
    const u32 ub0 = t * 4, ub1 = CH_B + t * 4;
    sfor<0, 4>([&](auto i) {
      const float m0 = bflo(g[i][0]), m1 = bflo(g[i][2]);
      const float r0 = __builtin_amdgcn_rcpf(bfhi(g[i][0])), r1 = __builtin_amdgcn_rcpf(bfhi(g[i][2]));
#pragma unroll
      for (int c = 0; c < 2; ++c) {
        const u32 d = pkbf((c ? bfhi(gx[i][0]) : bflo(gx[i][0])) - m0, (c ? bfhi(gx[i][2]) : bflo(gx[i][2])) - m1);
        const u32 o = pkbf(bflo(d) * r0, bfhi(d) * r1);
        const u32 uh = pkh(bflo(o), bfhi(o));
        LAS char* c2 = cc[c] + i * 1024;
        *(LAS u32*)(c2 + OFF_U + UC_B + 112) = uh;
        dsw_halves<OFF_U + 114 + i * 1024>(c ? ub1 : ub0, uh);
        *(LAS u32*)(c2 + OFF_X) = __builtin_amdgcn_perm(gx[i][2], gx[i][0], c ? 0x07060302u : 0x05040100u);
      }
    });
  }
  BAR();
  if (b == 0 && t == 0) __builtin_amdgcn_raw_buffer_store_b32(tag, rs0, 0, WS_EPOCH, 16);
  STAMP(6);
  v4f acc[4];
  {
    u32 fo;
    {
      int g16, pw;
      if (lane < 8) g16 = 7 * j + w; else g16 = 7 * j + 4 + ((lane - 8) >> 3);
      pw = ((lane & 1) * 112 + ((lane >> 1) & 3) * 28 + (g16 >> 2)) * 4 + (g16 & 3);
      if (lane >= 32) { const int k = lane < 60 ? lane - 32 : 0; pw = ((7 * w + (k >> 2)) * 8 + dg) * 4 + (k & 3); }
      fo = pw * 16;
    }
    const u32 sW1 = WS_WT1 + j * 229376 + w * 32768, sW2 = WS_WT1 + j * 229376 + 131072;
    u32 fl;
    auto extra = [&](auto r, auto m) {
      if constexpr (r == 2 && m == 8) ld_flag(fl, srd, fo, WS_WFLAG);
      if constexpr (r == 3 && m == 17) {
        STAMP(16);
        wait0_pin(fl);
        for (int spin = 0; spin < SPIN_CAP && !__all(fl == tag); ++spin) { ld_flag(fl, srd, fo, WS_WFLAG); wait0_pin(fl); COUNT(1); }
        STAMP(17);
      }
      if constexpr (r >= 4) {
        constexpr int k = (r - 4) * 18 + m;
        if constexpr (k < 32) ld_agpr<A_W + 4 * k, (k & 3) * 1024>(srd, lane16, sW1 + (k >> 2) * 4096);
        else if constexpr (k < 38) dma16(srd, lane16, sW2 + (w * 6 + k - 32) * 1024, L_W2 + (w * 6 + k - 32) * 1024);
        else if constexpr (k == 38) dma16(srd, lane16, WS_CSUM + j * 4096 + w * 1024, L_CSP + w * 1024);
      }
    };
    LAS char* ub = cb + OFF_U + (i16 & 1) * UC_B + ((i16 >> 1) + G) * 16;
    LAS char* kb = cb + OFF_K + (i16 & 3) * KC_B + 16 + (248 - 8 * (i16 >> 2) + G) * 16;
    if (nh == 0) conv_main<0>(ub, kb, acc, extra);
    else conv_main<1>(ub, kb, acc, extra);
  }
  STAMP(7);
  {
    const float dsk = bf2f(r_dsk);
#pragma unroll
    for (int a = 0; a < 4; ++a) {
#pragma unroll
      for (int hh = 0; hh < 2; ++hh) {
        const int l = 256 * (4 * (a >> 1) + ((a & 1) ? 3 - nh : nh)) + 8 * (2 * G + hh) + 2 * (i16 & 3) + 64 * (i16 >> 2);
        const v2h up = *(LAS v2h*)(cb + OFF_U + UC_B + (l + 56) * 2);
        LAS u32* xp = (LAS u32*)(cb + OFF_X + l * 2);
        const u32 xv = *xp;
        const u32 yb = pkbf(acc[a][2 * hh] * yscl, acc[a][2 * hh + 1] * yscl);
        const u32 sk = pkbf((float)up[0] * dsk, (float)up[1] * dsk);
        const u32 zz = pkbf(bflo(yb) + bflo(sk), bfhi(yb) + bfhi(sk));
        *xp = min(pkbf(bflo(xv) + bflo(zz), bfhi(xv) + bfhi(zz)), 0xfffffffeu);
      }
    }
    WAITL0();
#pragma unroll
    for (int i = 0; i < 2; ++i) {
      const int c = 4 * i + ((lane >> 5) ? 3 - nh : nh);
      const int l = 256 * c + (lane & 31) * 8;
      __builtin_amdgcn_raw_buffer_store_b128(*(LAS v4u*)(cb + OFF_X + l * 2), rs0, l * 2, WS_HT + ch * 4096, 16);
    }
  }
  WAITV(0);
  BAR();
  STAMP(8);
  const u32 vtok = (lane >> 4) * 4096 + (((lane & 15) ^ (4 * (lane >> 4))) * 16);
  const u32 stok = WS_HT + mt * 256 + w * 65536;
  auto tok_dma = [&](int c) {
#pragma unroll
    for (int i = 0; i < 4; ++i) dma16(srd0, vtok, stok + (64 * c + 4 * i) * 4096, c * 16384 + (4 * w + i) * 1024);
  };
#pragma unroll
  for (int c = 0; c < 8; ++c) tok_dma(c);
  LAS char* tp[4][2];
#pragma unroll
  for (int i = 0; i < 4; ++i) {
    tp[i][0] = sm + (8 * (G >> 1) + q4) * 256 + 8 * (p4 & 1) + (((4 * (w ^ i) + 2 * (G & 1) + (p4 >> 1)) ^ (4 * q4)) * 16);
    tp[i][1] = tp[i][0] + 65536;
    asm volatile("" : "+v"(tp[i][1]));
  }
  LAS char* vp[2];
  vp[0] = sm + w * 4096 + lane16; vp[1] = vp[0] + 65536;
  asm volatile("" : "+v"(vp[1]));
  v4u own[32], vq[4];
  u32 vm = 0;
  float sA = 0, sB = 0, qA = 0, qB = 0;
  auto mx = [&](u32 m, const v4u& v) { return max(max(m, max(v[0], v[1])), max(v[2], v[3])); };
  auto own_rd = [&](auto s) { own[s] = trfrag(tp[0][s >> 4] + (s & 15) * 4096, 1024); };
  auto own_acc = [&](auto s) {
#pragma unroll
    for (int e = 0; e < 4; e += 2) {
      sA = dot2bf(own[s][e], 0x3f803f80u, sA); qA = dot2bf(own[s][e], own[s][e], qA);
      sB = dot2bf(own[s][e + 1], 0x3f803f80u, sB); qB = dot2bf(own[s][e + 1], own[s][e + 1], qB);
    }
    pin2(sA, qA); pin2(sB, qB);
  };
  auto v_rd = [&](auto c, auto i) { vq[i] = *(LAS v4u*)(vp[c >> 2] + (c & 3) * 16384 + i * 1024); };
  auto fix = [&](auto c0, auto c1) {
    for (int spin = 0; spin < SPIN_CAP && __any(vm == ~0u); ++spin) {
      COUNT(2 + (c0 >> 1));
#pragma unroll
      for (int c = c0; c < c1; ++c) tok_dma(c);
      WAITV(0);
      vm = 0;
      sfor<c0, c1>([&](auto c) {
        sfor<0, 4>([&](auto i) { v_rd(c, i); });
        sfor<0, 4>([&](auto i) { vm = mx(vm, vq[i]); });
      });
    }
    vm = 0;
  };
  WAITV(28);
  sfor<0, 4>([&](auto i) { v_rd(IC<0>{}, i); });
  sfor<0, 4>([&](auto i) { vm = mx(vm, vq[i]); });
  SB();
  WAITV(24);
  sfor<0, 4>([&](auto i) { v_rd(IC<1>{}, i); });
  sfor<0, 4>([&](auto i) { vm = mx(vm, vq[i]); });
  fix(IC<0>{}, IC<2>{});
  BAR();
  STAMP(9);
  const u32 sW2 = WS_WT1 + j * 229376 + 131072;
  auto w2_dma = [&](int g) {
#pragma unroll
    for (int i = 0; i < 3; ++i) dma16(srd, lane16, sW2 + g * 12288 + (3 * w + i) * 1024, (g - 2) * 16384 + (3 * w + i) * 1024);
  };
  v4u hres[2];
  {
    v4u ot[2][3];
    LAS char* hp = sm + (64 * dg + 32 * (w >> 1) + 16 * (G >> 1) + q4) * 256 + 8 * (p4 & 1) +
                   (((4 * (2 * e2 + (w & 1)) + 2 * (G & 1) + (p4 >> 1)) ^ (4 * q4)) * 16);
    auto ot_rd = [&](auto s, auto i) { ot[s & 1][i] = trfrag(tp[i + 1][s >> 4] + (s & 15) * 4096, 1024); };
    own_rd(IC<0>{});
    sfor<0, 3>([&](auto i) { ot_rd(IC<0>{}, i); });
    if (t < 224) {
      LAS float* cp = (LAS float*)(sm + L_CSP) + t;
      *(LAS float*)(sm + L_CS + t * 4) = (cp[0] + cp[224]) + (cp[448] + cp[672]);
    }
    SB();
    sfor<0, 32>([&](auto s) {
      constexpr int vc = s == 3 ? 2 : s == 5 ? 3 : s == 8 ? 4 : s == 9 ? 5 : s == 11 ? 6 : s == 13 ? 7 : -1;
      constexpr bool vmx = s == 4 || s == 6 || s == 9 || s == 10 || s == 12 || s == 14;
      constexpr int o0 = s < 15 ? s + 1 : s < 23 ? 16 + 2 * (s - 15) : -1, o1 = (s >= 15 && s < 23) ? o0 + 1 : -1;
      if constexpr (s == 5) sfor<0, 6>([&](auto k) { own_acc(k); });
      if constexpr (s == 13) sfor<6, 14>([&](auto k) { own_acc(k); });
      if constexpr (s == 20) sfor<14, 24>([&](auto k) { own_acc(k); });
      if constexpr (s == 24) sfor<24, 32>([&](auto k) { own_acc(k); });
      if constexpr (vc >= 0) WAITV(4 * (7 - vc) + (s > 6 ? 6 : 0));
      mfma_aav<A_ACC1, A_W + 4 * s, s == 0>(own[s]);
      if constexpr (s < 31) ot_rd(IC<s + 1>{}, IC<0>{});
      if constexpr (vmx) { vm = mx(vm, vq[0]); vm = mx(vm, vq[1]); }
      SB();
      mfma_aav<A_ACC1 + 16, A_W + 4 * s, s == 0>(ot[s & 1][0]);
      if constexpr (s < 31) ot_rd(IC<s + 1>{}, IC<1>{});
      if constexpr (vmx) { vm = mx(vm, vq[2]); vm = mx(vm, vq[3]); }
      if constexpr (o1 >= 0) own_rd(IC<o1>{});
      SB();
      mfma_aav<A_ACC1 + 32, A_W + 4 * s, s == 0>(ot[s & 1][1]);
      if constexpr (s < 31) ot_rd(IC<s + 1>{}, IC<2>{});
      if constexpr (vc >= 0) { v_rd(IC<vc>{}, IC<0>{}); v_rd(IC<vc>{}, IC<1>{}); }
      SB();
      mfma_aav<A_ACC1 + 48, A_W + 4 * s, s == 0>(ot[s & 1][2]);
      if constexpr (o0 >= 0) own_rd(IC<o0>{});
      if constexpr (vc >= 0) { v_rd(IC<vc>{}, IC<2>{}); v_rd(IC<vc>{}, IC<3>{}); }
      if constexpr (s == 1) { if (dg < 2) { hres[0] = trfrag(hp, 1024); hres[1] = trfrag(hp + 2048, 1024); } }
      if constexpr (s == 7) { if ((dg >> 1) == 1) { hres[0] = trfrag(hp, 1024); hres[1] = trfrag(hp + 2048, 1024); } }
      if constexpr (s == 16) { if (dg >= 4) { hres[0] = trfrag(hp, 1024); hres[1] = trfrag(hp + 2048, 1024); } }
      SB();
      if constexpr (s == 6) { fix(IC<2>{}, IC<4>{}); BAR(); STAMP(19); w2_dma(2); w2_dma(3); SB(); }
      if constexpr (s == 14) { fix(IC<4>{}, IC<8>{}); BAR(); STAMP(20); w2_dma(4); w2_dma(5); SB(); }
      if constexpr (s == 22) { BAR(); w2_dma(6); w2_dma(7); SB(); }
      if constexpr (s == 26) {
        const float sa = sum32x(sA + sB), qa = sum32x(qA + qB);
        const float m = sa * (1.0f / D);
        const float var = fmaxf(qa * (1.0f / D) - m * m, 0.0f);
        const float sd = rbf(__builtin_sqrtf(rbf(rbf(var) + 1e-5f)));
        if (lane < 32) *(LAS v2f*)(sm + L_MR + (w * 32 + lane) * 8) = v2f{rbf(m), __builtin_amdgcn_rcpf(sd)};
        SB();
      }
    });
  }
  WAITV(6);
  BAR();
  STAMP(10);
  const u32 sAct = WS_ACT + ((mt * 4) * 112 + j * 7) * 1024;
  const u32 sWD = WS_WT2 + dg * 229376 + w * 57344;
  const u32 sAin = WS_ACT + ((mt * 4 + 2 * e2) * 112 + 28 * w) * 1024;
  v4u acte[8][2], actl[8][2], actb[12][2];
  auto ld_early = [&](auto i, auto k) { ld_vgpr<(i & 1) * 1024>(acte[i][k], srd, lane16, sAin + (k * 112 + 7 * (i >> 1) + 2 * e2) * 1024); };
  auto ld_late = [&](auto i, auto k) { ld_vgpr<(i & 1) * 1024>(actl[i][k], srd, lane16, sAin + (k * 112 + 7 * (i >> 1) + 2 - 2 * e2) * 1024); };
  auto ld_b = [&](auto i, auto k) { ld_vgpr<(i % 3) * 1024>(actb[i][k], srd, lane16, sAin + (k * 112 + 7 * (i / 3) + 4) * 1024); };
  const int hh = lane >> 5;
  v2f mr[4];
  {
#pragma unroll
    for (int i = 0; i < 4; ++i) mr[i] = *(LAS v2f*)(sm + L_MR + ((w ^ i) * 32 + (lane & 31)) * 8);
    LAS char* cp = sm + L_CS + (w * 16 + 4 * hh) * 4;
    const v4f cg0 = *(LAS v4f*)cp, cg1 = *(LAS v4f*)(cp + 32), cu0 = *(LAS v4f*)(cp + 448), cu1 = *(LAS v4f*)(cp + 480);
    LAS char* wp[3] = {sm + lane16, sm + 65536 + lane16, sm + L_W2 + lane16};
    asm volatile("" : "+v"(wp[1]), "+v"(wp[2]));
    v4u w2[2][3];
    auto w2_rd = [&](auto s, auto i) {
      constexpr int g = s >> 2;
      constexpr int off = (s & 3) * 3072 + i * 1024;
      if constexpr (g < 2) w2[s & 1][i] = *(LAS v4u*)(wp[2] + g * 12288 + off);
      else if constexpr (g < 6) w2[s & 1][i] = *(LAS v4u*)(wp[0] + (g - 2) * 16384 + off);
      else w2[s & 1][i] = *(LAS v4u*)(wp[1] + (g - 6) * 16384 + off);
    };
    sfor<0, 3>([&](auto i) { w2_rd(IC<0>{}, i); });
    float rl[4], r2[4];
#pragma unroll
    for (int i = 0; i < 4; ++i) { rl[i] = mr[i][1] * -1.44269504088896340736f; r2[i] = mr[i][1] * mr[i][1]; }
    SB();
    v4u o;
    float prev = 0;
    sfor<0, 32>([&](auto s) {
      constexpr int un = s >> 3, e = s & 7;
      if constexpr (s == 20) { WAITV(22); BARX(); }
      mfma_avv<A_ACC2, s == 0>(w2[s & 1][0], own[s]);
      if constexpr (s < 31) w2_rd(IC<s + 1>{}, IC<0>{});
      ld_agpr<A_W + 4 * s, (s & 3) * 1024>(srd, lane16, sWD + ((s < 16 ? e2 : 1 - e2) * 16 + (s & 12)) * 1024);
      const float tg = __builtin_fmaf(-mr[un][0], e < 4 ? cg0[e & 3] : cg1[e & 3], accrd<A_ACC1 + 16 * un + e>());
      const float tu = __builtin_fmaf(-mr[un][0], e < 4 ? cu0[e & 3] : cu1[e & 3], accrd<A_ACC1 + 16 * un + 8 + e>());
      SB();
      mfma_avv<A_ACC2 + 16, s == 0>(w2[s & 1][1], own[s]);
      if constexpr (s < 31) w2_rd(IC<s + 1>{}, IC<1>{});
      if constexpr (s >= 24) ld_early(IC<2 * (s & 3)>{}, IC<(s >= 28)>{});
      const float ex = __builtin_amdgcn_exp2f(tg * rl[un]);
      const float p = tg * tu * r2[un];
      const float dn = 1.0f + ex;
      SB();
      mfma_avv<A_ACC2 + 32, s == 0>(w2[s & 1][2], own[s]);
      if constexpr (s < 31) w2_rd(IC<s + 1>{}, IC<2>{});
      if constexpr (s >= 24) ld_early(IC<2 * (s & 3) + 1>{}, IC<(s < 28)>{});
      const float v = p * __builtin_amdgcn_rcpf(dn);
      if constexpr (e & 1) o[e >> 1] = min(pkbf(prev, v), 0xfffffffeu);
      else prev = v;
      if constexpr (e == 7) __builtin_amdgcn_raw_buffer_store_b128(o, rs, lane16, sAct + ((w ^ un) * 112 + w) * 1024, 16);
      SB();
    });
  }
  STAMP(11);
  asm volatile("s_nop 7\n\ts_nop 3" ::: "memory");
  sfor<0, 3>([&](auto un) {
    LAS char* cp = sm + L_CS + ((4 + un) * 16 + 4 * hh) * 4;
    const v4f cg0 = *(LAS v4f*)cp, cg1 = *(LAS v4f*)(cp + 32), cu0 = *(LAS v4f*)(cp + 448), cu1 = *(LAS v4f*)(cp + 480);
    float g[8], u[8];
    sfor<0, 8>([&](auto e) {
      g[e] = __builtin_fmaf(-mr[0][0], e < 4 ? cg0[e & 3] : cg1[e & 3], accrd<A_ACC2 + 16 * un + e>()) * mr[0][1];
      u[e] = __builtin_fmaf(-mr[0][0], e < 4 ? cu0[e & 3] : cu1[e & 3], accrd<A_ACC2 + 16 * un + 8 + e>()) * mr[0][1];
    });
    v4u o;
    silu_mul8(g, u, o);
    __builtin_amdgcn_raw_buffer_store_b128(o, rs, lane16, sAct + (w * 112 + 4 + un) * 1024, 16);
  });
  SB();
  BARX();
  STAMP(12);
  sfor<0, 8>([&](auto i) { ld_late(i, IC<(i & 1)>{}); });
  sfor<0, 8>([&](auto i) { ld_late(i, IC<(i & 1) ^ 1>{}); });
#define PIN8(a) "+v"(a[0][0]), "+v"(a[0][1]), "+v"(a[1][0]), "+v"(a[1][1]), "+v"(a[2][0]), "+v"(a[2][1]), "+v"(a[3][0]), "+v"(a[3][1]), \
                "+v"(a[4][0]), "+v"(a[4][1]), "+v"(a[5][0]), "+v"(a[5][1]), "+v"(a[6][0]), "+v"(a[6][1]), "+v"(a[7][0]), "+v"(a[7][1])
#define PIN4(a) "+v"(a[8][0]), "+v"(a[8][1]), "+v"(a[9][0]), "+v"(a[9][1]), "+v"(a[10][0]), "+v"(a[10][1]), "+v"(a[11][0]), "+v"(a[11][1])
  auto g2step = [&](auto first, auto aw, const v4u& b0, const v4u& b1, auto&& ride) {
    mfma_aav<A_ACC1, aw, first>(b0); ride(IC<0>{}); SB();
    mfma_aav<A_ACC1 + 16, aw, first>(b1); ride(IC<1>{}); SB();
    mfma_aav<A_ACC1 + 32, aw + 4, first>(b0); ride(IC<2>{}); SB();
    mfma_aav<A_ACC1 + 48, aw + 4, first>(b1); ride(IC<3>{}); SB();
  };
  asm volatile("s_waitcnt vmcnt(20)" : PIN8(acte) :: "memory");
  for (int spin = 0; spin < SPIN_CAP; ++spin) {
    u32 m = 0;
    sfor<0, 8>([&](auto i) { m = mx(mx(m, acte[i][0]), acte[i][1]); });
    if (!__any(m == ~0u)) break;
    COUNT(5);
    sfor<0, 8>([&](auto i) { ld_early(i, IC<0>{}); ld_early(i, IC<1>{}); });
    asm volatile("s_waitcnt vmcnt(0)" : PIN8(acte) :: "memory");
  }
  SB();
  STAMP(13);
  sfor<0, 8>([&](auto i) {
    g2step(IC<(i == 0)>{}, IC<A_W + 8 * i>{}, acte[i][0], acte[i][1], [&](auto k) {
      if constexpr (i > 0 && k < 2) ld_agpr<A_W + 8 * (i - 1) + 4 * k, ((2 * (i - 1) + k) & 3) * 1024>(srd, lane16, sWD + 32768 + ((2 * (i - 1)) >> 2) * 4096);
    });
  });
  ld_agpr<A_W + 56, 2048>(srd, lane16, sWD + 32768 + 3 * 4096);
  ld_agpr<A_W + 60, 3072>(srd, lane16, sWD + 32768 + 3 * 4096);
  STAMP(21);
  asm volatile("s_waitcnt vmcnt(16)" : PIN8(actl) :: "memory");
  for (int spin = 0; spin < SPIN_CAP; ++spin) {
    u32 m = 0;
    sfor<0, 8>([&](auto i) { m = mx(mx(m, actl[i][0]), actl[i][1]); });
    if (!__any(m == ~0u)) break;
    COUNT(6);
    sfor<0, 8>([&](auto i) { ld_late(i, IC<0>{}); ld_late(i, IC<1>{}); });
    asm volatile("s_waitcnt vmcnt(0)" : PIN8(actl) :: "memory");
  }
  SB();
  sfor<0, 8>([&](auto i) {
    g2step(IC<0>{}, IC<A_W + 64 + 8 * i>{}, actl[i][0], actl[i][1], [&](auto k) {
      if constexpr (i < 6) ld_b(IC<2 * i + (k >> 1)>{}, IC<(k & 1)>{});
      if constexpr (i >= 1 && i < 5 && k < 2) ld_agpr<A_W + 64 + 8 * (i - 1) + 4 * k, ((2 * (i - 1) + k) & 3) * 1024>(srd, lane16, sWD + 32768 + 16384 + ((2 * (i - 1)) >> 2) * 4096);
    });
  });
  STAMP(22);
  asm volatile("s_waitcnt vmcnt(0)" : PIN8(actb) :: "memory");
  asm volatile("" : PIN4(actb));
  for (int spin = 0; spin < SPIN_CAP; ++spin) {
    u32 m = 0;
    sfor<0, 12>([&](auto i) { m = mx(mx(m, actb[i][0]), actb[i][1]); });
    if (!__any(m == ~0u)) break;
    COUNT(7);
    sfor<0, 12>([&](auto i) { ld_b(i, IC<0>{}); ld_b(i, IC<1>{}); });
    asm volatile("s_waitcnt vmcnt(0)" : PIN8(actb) :: "memory");
    asm volatile("" : PIN4(actb));
  }
  SB();
  sfor<0, 12>([&](auto i) { g2step(IC<0>{}, IC<A_W + 8 * i>{}, actb[i][0], actb[i][1], [&](auto) {}); });
  STAMP(14);
  asm volatile("s_nop 7\n\ts_nop 3" ::: "memory");
  float a[16];
  auto xch = [&](auto W) {
    const u32 ad = W * 4096 + lane16;
    sfor<0, 4>([&](auto k) {
      if constexpr (k != W) {
        sfor<0, 4>([&](auto e) {
          dsw_agpr<A_ACC1 + 16 * k + 4 * e, k * 16384 + e * 1024>(ad);
        });
      }
    });
    sfor<0, 16>([&](auto e) { a[e] = accrd<A_ACC1 + 16 * W + e>(); });
  };
  if (w == 0) xch(IC<0>{}); else if (w == 1) xch(IC<1>{}); else if (w == 2) xch(IC<2>{}); else xch(IC<3>{});
  BAR();
#pragma unroll
  for (int k = 1; k < 4; ++k) {
#pragma unroll
    for (int e = 0; e < 4; ++e) {
      const v4f p = *(LAS v4f*)(sm + w * 16384 + (w ^ k) * 4096 + e * 1024 + lane16);
#pragma unroll
      for (int c = 0; c < 4; ++c) a[4 * e + c] += p[c];
    }
  }
  {
    u32 o[8];
#pragma unroll
    for (int e = 0; e < 8; ++e) {
      const u32 hv = hres[e >> 2][e & 3];
      const u32 f = pkbf(a[2 * e], a[2 * e + 1]);
      o[e] = pkbf(bflo(hv) + bflo(f), bfhi(hv) + bfhi(f));
    }
    u16* op = out + (size_t)(128 * mt + 64 * e2 + 32 * (w & 1) + (lane & 31)) * D + 64 * dg + 32 * (w >> 1) + 16 * hh;
    *(v4u*)op = v4u{o[0], o[1], o[2], o[3]};
    *(v4u*)(op + 8) = v4u{o[4], o[5], o[6], o[7]};
  }
  STAMP(15);
  asm volatile(".Lkf_end:");
}

extern "C" void kernel_launch(void* const* d_in, const int* in_sizes, int n_in, void* d_out, int out_size, void* d_ws, size_t ws_size,
                              hipStream_t stream) {
  (void)hipFuncSetAttribute((const void*)k_fused, hipFuncAttributeMaxDynamicSharedMemorySize, LDS_B);
  k_fused<<<256, 256, LDS_B, stream>>>((const u16*)d_in[0], (const u16*)d_in[1], (const u16*)d_in[2], (const u16*)d_in[3], (const u16*)d_in[4],
                                       (const u16*)d_in[5], (const u16*)d_in[6], (const u16*)d_in[8], (const u16*)d_in[9],
                                       (const u16*)d_in[10], (char*)d_ws, (u16*)d_out);
}
```

```cpp
#include <hip/hip_runtime.h>
#include <hip/hip_bf16.h>
#include <stdint.h>
#include <type_traits>

typedef unsigned short u16;
typedef unsigned int u32;
typedef unsigned long long u64;
typedef short v4s __attribute__((ext_vector_type(4)));
typedef __bf16 v2bf __attribute__((ext_vector_type(2)));
typedef _Float16 v2h __attribute__((ext_vector_type(2)));
typedef float v2f __attribute__((ext_vector_type(2)));
typedef float v4f __attribute__((ext_vector_type(4)));
typedef u32 v2u __attribute__((ext_vector_type(2)));
typedef u32 v4u __attribute__((ext_vector_type(4)));
#define LAS __attribute__((address_space(3)))
#define GAS __attribute__((address_space(1)))
#define DEV __device__ __forceinline__

constexpr int L = 2048, D = 512, NS = 128, F = 1792;

constexpr u32 WS_EPOCH = 0;
constexpr u32 WS_WFLAG = 4096;
constexpr u32 WS_GRAN  = 64 << 10;
constexpr u32 WS_CSUM  = 256 << 10;
constexpr u32 WS_HT    = 1 << 20;
constexpr u32 WS_WT1   = 4 << 20;
constexpr u32 WS_WT2   = 8 << 20;
constexpr u32 WS_ACT   = 12 << 20;
constexpr u32 WS_XG    = 20 << 20;
constexpr u32 WS_USED  = 24 << 20;
constexpr u32 WS_FIX   = 4 << 20;
constexpr size_t WS_ROT = 40 << 20;
constexpr size_t WS_DBG = 32 << 20;

template <int I> using IC = std::integral_constant<int, I>;
template <int B, int E, class Fn> DEV void sfor(Fn&& f) {
  if constexpr (B < E) { f(IC<B>{}); sfor<B + 1, E>(f); }
}

DEV float bf2f(u32 b) { return __uint_as_float(b << 16); }
DEV float bflo(u32 p) { return __uint_as_float(p << 16); }
DEV float bfhi(u32 p) { return __uint_as_float(p & 0xffff0000u); }
DEV u32 pkbf(float a, float b) { v2f f = {a, b}; return __builtin_bit_cast(u32, __builtin_convertvector(f, v2bf)); }
DEV u32 pkh(float a, float b) { v2f f = {a, b}; return __builtin_bit_cast(u32, __builtin_convertvector(f, v2h)); }
DEV float rbf(float a) { return bflo(pkbf(a, a)); }
DEV float dot2bf(u32 a, u32 b, float c) { return __builtin_amdgcn_fdot2_f32_bf16(__builtin_bit_cast(v2bf, a), __builtin_bit_cast(v2bf, b), c, false); }
DEV v2u trread(LAS char* p) { return __builtin_bit_cast(v2u, __builtin_amdgcn_ds_read_tr16_b64_v4i16((LAS v4s*)p)); }
DEV v4u trfrag(LAS char* p, int second) { const v2u a = trread(p), b = trread(p + second); return v4u{a[0], a[1], b[0], b[1]}; }
#define WAITV(n) asm volatile("s_waitcnt vmcnt(%0)" :: "i"(n) : "memory")
#define WAITL0() asm volatile("s_waitcnt lgkmcnt(0)" ::: "memory")
#define BAR() asm volatile("s_waitcnt lgkmcnt(0)\n\ts_barrier" ::: "memory")
#define BARX() asm volatile("s_barrier" ::: "memory")
#define SB() __builtin_amdgcn_sched_barrier(0)
constexpr int SPIN_CAP = 1 << 20;

#define STAMP(slot) do {} while (0)
#define COUNT(slot) do {} while (0)

template <int CTRL> DEV float dpp(float v) { return __builtin_bit_cast(float, __builtin_amdgcn_update_dpp(0, __builtin_bit_cast(int, v), CTRL, 0xf, 0xf, true)); }
DEV float swap16_other(float v, float& o) { auto r = __builtin_amdgcn_permlane16_swap(__float_as_uint(v), __float_as_uint(v), false, false); o = __uint_as_float(r[1]); return __uint_as_float(r[0]); }
DEV float swap32_other(float v, float& o) { auto r = __builtin_amdgcn_permlane32_swap(__float_as_uint(v), __float_as_uint(v), false, false); o = __uint_as_float(r[1]); return __uint_as_float(r[0]); }
DEV float sum16x(float v) { float o; const float a = swap16_other(v, o); return a + o; }
DEV float sum32x(float v) { float o; const float a = swap32_other(v, o); return a + o; }
DEV float wave_sum(float v) {
  v += dpp<0xB1>(v); v += dpp<0x4E>(v); v += dpp<0x141>(v); v += dpp<0x140>(v);
  return sum32x(sum16x(v));
}
DEV float wave_max(float v) {
  v = fmaxf(v, dpp<0xB1>(v)); v = fmaxf(v, dpp<0x4E>(v)); v = fmaxf(v, dpp<0x141>(v)); v = fmaxf(v, dpp<0x140>(v));
  float o; float a = swap16_other(v, o); v = fmaxf(a, o); a = swap32_other(v, o); return fmaxf(a, o);
}

DEV void dma16(v4u srd, u32 voff, u32 soff, u32 lds) {
  asm volatile("s_mov_b32 m0, %3\n\ts_nop 0\n\tbuffer_load_dwordx4 %0, %1, %2 offen sc1 lds" :: "v"(voff), "s"(srd), "s"(soff), "s"(lds) : "memory");
}
template <int A, int IMM> DEV void ld_agpr(v4u srd, u32 voff, u32 soff) {
  asm volatile("buffer_load_dwordx4 a[%c3:%c4], %0, %1, %2 offen offset:%c5 sc1" :: "v"(voff), "s"(srd), "s"(soff), "i"(A), "i"(A + 3), "i"(IMM) : "memory");
}
template <int IMM> DEV void ld_vgpr(v4u& d, v4u srd, u32 voff, u32 soff) {
  asm volatile("buffer_load_dwordx4 %0, %1, %2, %3 offen offset:%c4 sc1" : "=v"(d) : "v"(voff), "s"(srd), "s"(soff), "i"(IMM) : "memory");
}
template <int ACC, int A, bool FIRST> DEV void mfma_aav(const v4u& b) {
  if constexpr (FIRST) asm volatile("v_mfma_f32_32x32x16_bf16 a[%c1:%c2], a[%c3:%c4], %0, 0" :: "v"(b), "i"(ACC), "i"(ACC + 15), "i"(A), "i"(A + 3));
  else asm volatile("v_mfma_f32_32x32x16_bf16 a[%c1:%c2], a[%c3:%c4], %0, a[%c1:%c2]" :: "v"(b), "i"(ACC), "i"(ACC + 15), "i"(A), "i"(A + 3));
}
template <int ACC, bool FIRST> DEV void mfma_avv(const v4u& a, const v4u& b) {
  if constexpr (FIRST) asm volatile("v_mfma_f32_32x32x16_bf16 a[%c2:%c3], %0, %1, 0" :: "v"(a), "v"(b), "i"(ACC), "i"(ACC + 15));
  else asm volatile("v_mfma_f32_32x32x16_bf16 a[%c2:%c3], %0, %1, a[%c2:%c3]" :: "v"(a), "v"(b), "i"(ACC), "i"(ACC + 15));
}
template <int A, int OFF> DEV void dsw_agpr(u32 ad) {
  asm volatile("ds_write_b128 %0, a[%c1:%c2] offset:%c3" :: "v"(ad), "i"(A), "i"(A + 3), "i"(OFF) : "memory");
}
template <int OFF> DEV void dsw_halves(u32 ad, u32 v) {
  asm volatile("ds_write_b16 %0, %1 offset:%c2\n\tds_write_b16_d16_hi %0, %1 offset:%c3" :: "v"(ad), "v"(v), "i"(OFF), "i"(OFF + 2) : "memory");
}
DEV void ld_flag(u32& d, v4u srd, u32 voff, u32 soff) {
  asm volatile("buffer_load_dword %0, %1, %2, %3 offen sc1" : "=v"(d) : "v"(voff), "s"(srd), "s"(soff) : "memory");
}
DEV void wait0_pin(u32& d) { asm volatile("s_waitcnt vmcnt(0)" : "+v"(d) :: "memory"); }
DEV void pin2(float& a, float& b) { asm volatile("" : "+v"(a), "+v"(b)); }
template <int A> DEV float accrd() { float v; asm volatile("v_accvgpr_read_b32 %0, a%c1" : "=v"(v) : "i"(A)); return v; }
DEV void mfma16(v4f& c, const v4u& a, const v4u& b) { asm volatile("v_mfma_f32_16x16x32_f16 %0, %1, %2, %0" : "+v"(c) : "v"(a), "v"(b)); }
DEV void mfma16z(v4f& c, const v4u& a, const v4u& b) { asm volatile("v_mfma_f32_16x16x32_f16 %0, %1, %2, 0" : "=v"(c) : "v"(a), "v"(b)); }
constexpr int A_W = 0, A_ACC1 = 128, A_ACC2 = 192;

constexpr int TCOL = 528, NCOL = 63, TAB_B = NCOL * TCOL;
constexpr int KC_B = 4640, UC_B = 4224;
constexpr int OFF_K = TAB_B, OFF_U = OFF_K + 4 * KC_B, OFF_X = OFF_U + 2 * UC_B, CH_B = OFF_X + 4096;
constexpr int L_RT1 = 2 * CH_B, L_RT2 = L_RT1 + 128 * 144, L_MISC = L_RT2 + 64 * 144;
constexpr int L_W2 = 131072;
constexpr int L_CSP = L_MISC, L_CS = L_CSP + 4096, L_MR = L_CS + 1024, L_VOTE = L_MR + 1024, LDS_B = L_VOTE + 64;
static_assert(L_W2 >= L_RT1 && L_W2 + 24576 <= L_MISC && LDS_B <= 163840 && L_MISC % 16 == 0, "lds map");

typedef v2f cf;
DEV void cmul2(cf& r0, cf a0, cf b0, cf& r1, cf a1, cf b1) {
  asm("v_pk_mul_f32 %0, %2, %3 op_sel_hi:[0,1]\n\t"
      "v_pk_mul_f32 %1, %4, %5 op_sel_hi:[0,1]\n\t"
      "v_pk_fma_f32 %0, %2, %3, %0 op_sel:[1,1,0] op_sel_hi:[1,0,1] neg_lo:[1,0,0]\n\t"
      "v_pk_fma_f32 %1, %4, %5, %1 op_sel:[1,1,0] op_sel_hi:[1,0,1] neg_lo:[1,0,0]"
      : "=&v"(r0), "=&v"(r1) : "v"(a0), "v"(b0), "v"(a1), "v"(b1));
}
DEV cf cexp_p(float ar, float ai, float p) {
  const float e = __builtin_amdgcn_exp2f(ar * p * 1.44269504088896340736f);
  const float xx = ai * p;
  const float chi = 0.15915493667125702f, clo = 6.4206383e-09f;
  const float th = xx * chi;
  float tl = __builtin_fmaf(xx, chi, -th);
  tl = __builtin_fmaf(xx, clo, tl);
  const float fr = (th - __builtin_rintf(th)) + tl;
  return cf{e * __builtin_amdgcn_cosf(fr), e * __builtin_amdgcn_sinf(fr)};
}
template <int CTRL> DEV u32 dppu(u32 v) { return (u32)__builtin_amdgcn_update_dpp(0, (int)v, CTRL, 0xf, 0xf, true); }
DEV u32 wave_umax(u32 v) {
  v = max(v, dppu<0xB1>(v)); v = max(v, dppu<0x4E>(v)); v = max(v, dppu<0x141>(v)); v = max(v, dppu<0x140>(v));
  auto r = __builtin_amdgcn_permlane16_swap(v, v, false, false); v = max(r[0], r[1]);
  auto q = __builtin_amdgcn_permlane32_swap(v, v, false, false); return max(q[0], q[1]);
}

struct ConvSched { int nm, ma[18], mq[18], mk[18]; int nr, ru[16], ri[16]; };
constexpr ConvSched mk_sched(int c0, int c1, int c2, int c3) {
  ConvSched s{};
  const int c[4] = {c0, c1, c2, c3};
  bool hu[8] = {}, hk[8] = {};
  for (int q = 0; q <= c3; ++q)
    for (int a = 3; a >= 0; --a)
      if (q <= c[a]) {
        const int k = c[a] - q;
        s.ma[s.nm] = a; s.mq[s.nm] = q; s.mk[s.nm] = k; ++s.nm;
        if (!hu[q]) { hu[q] = true; s.ru[s.nr] = 1; s.ri[s.nr] = q; ++s.nr; }
        if (!hk[k]) { hk[k] = true; s.ru[s.nr] = 0; s.ri[s.nr] = k; ++s.nr; }
      }
  return s;
}
template <int V> struct CS { static constexpr ConvSched s = V == 0 ? mk_sched(0, 3, 4, 7) : mk_sched(1, 2, 5, 6); };

template <int V, class Ex> DEV void conv_main(LAS char* ub, LAS char* kb, v4f (&acc)[4], Ex&& extra) {
  constexpr ConvSched S = CS<V>::s;
  v4u U[2][8], K[2][8];
  sfor<0, S.nr>([&](auto i) {
    if constexpr (S.ru[i]) U[0][S.ri[i]] = *(LAS v4u*)(ub + 512 * S.ri[i]);
    else K[0][S.ri[i]] = *(LAS v4u*)(kb - 512 * S.ri[i]);
  });
  SB();
  sfor<0, 8>([&](auto r) {
    constexpr int cur = r & 1, nxt = cur ^ 1;
    sfor<0, 18>([&](auto m) {
      if constexpr (r == 0 && S.mq[m] == 0) mfma16z(acc[S.ma[m]], U[cur][S.mq[m]], K[cur][S.mk[m]]);
      else mfma16(acc[S.ma[m]], U[cur][S.mq[m]], K[cur][S.mk[m]]);
      if constexpr (r < 7 && m < S.nr) {
        if constexpr (S.ru[m]) U[nxt][S.ri[m]] = *(LAS v4u*)(ub + 64 * (r + 1) + 512 * S.ri[m]);
        else K[nxt][S.ri[m]] = *(LAS v4u*)(kb + 64 * (r + 1) - 512 * S.ri[m]);
      }
      extra(r, m);
      SB();
    });
  });
  asm volatile("s_nop 7" : "+v"(acc[0]), "+v"(acc[1]), "+v"(acc[2]), "+v"(acc[3]));
}

DEV void silu_mul8(const float (&g)[8], const float (&u)[8], v4u& o) {
  float r[8];
#pragma unroll
  for (int e = 0; e < 8; ++e) r[e] = g[e] * __builtin_amdgcn_rcpf(1.0f + __builtin_amdgcn_exp2f(g[e] * -1.44269504088896340736f)) * u[e];
#pragma unroll
  for (int e = 0; e < 4; ++e) o[e] = min(pkbf(r[2 * e], r[2 * e + 1]), 0xfffffffeu);
}

__global__ __launch_bounds__(256, 1) void k_fused(const u16* __restrict__ x, const u16* __restrict__ lar_, const u16* __restrict__ aim_,
                                                  const u16* __restrict__ cre_, const u16* __restrict__ cim_, const u16* __restrict__ ldt_,
                                                  const u16* __restrict__ dsk_, const u16* __restrict__ wg, const u16* __restrict__ wu,
                                                  const u16* __restrict__ wd, char* __restrict__ ws, u16* __restrict__ out) {
  extern __shared__ __attribute__((aligned(16))) char smx[];
  LAS char* sm = (LAS char*)smx;
  asm volatile("" ::: "a0", "a255");
  const int t = threadIdx.x, lane = t & 63, w = __builtin_amdgcn_readfirstlane(t >> 6), b = blockIdx.x;
  STAMP(0);
  const int xcd = b & 7, bi = b >> 3;
  const auto rs0 = __builtin_amdgcn_make_buffer_rsrc(ws, 0, WS_FIX, 0x00020000);
  const v4u srd0 = {(u32)(uintptr_t)ws, (u32)((uintptr_t)ws >> 32) & 0xffffu, WS_FIX, 0x00020000u};
  const int pr = xcd * 32 + bi;
  const int cw = w >> 1, nh = w & 1, ch = 2 * pr + cw;
  const int mt = 4 * (xcd >> 1) + (bi & 3), j = 8 * (xcd & 1) + (bi >> 2);
  const int e2 = j & 1, dg = j >> 1;
  const int G = lane >> 4, q4 = (lane >> 2) & 3, p4 = lane & 3, i16 = lane & 15;
  const u32 lane16 = lane * 16;
  LAS char* cb = sm + cw * CH_B;

  v4u xa[2];
#pragma unroll
  for (int i = 0; i < 2; ++i) xa[i] = *(const v4u*)(x + (size_t)(8 * b + 2 * w + i) * D + lane * 8);
  u32 xq[4][2];
#pragma unroll
  for (int i = 0; i < 4; ++i)
#pragma unroll
    for (int k = 0; k < 2; ++k) xq[i][k] = *(const u32*)(x + (size_t)(8 * b + 2 * (lane & 3) + k) * D + 2 * (64 * w + 16 * i + (lane >> 2)));
  const u32 ep = __builtin_amdgcn_raw_buffer_load_b32(rs0, WS_EPOCH, 0, 16);
  const int n = 64 * nh + lane;
  const u32 r_lar = lar_[ch * NS + n], r_aim = aim_[ch * NS + n], r_cre = cre_[ch * NS + n], r_cim = cim_[ch * NS + n];
  const u32 r_cre2 = cre_[ch * NS + (n ^ 64)], r_cim2 = cim_[ch * NS + (n ^ 64)];
  const u32 r_ldt = ldt_[ch], r_dsk = dsk_[ch];
  SB();
  {
    const v4u s = {~0u, ~0u, ~0u, ~0u};
#pragma unroll
    for (int i = 0; i < 2; ++i) __builtin_amdgcn_raw_buffer_store_b128(s, rs0, t * 16 + i * 4096, WS_HT + pr * 8192, 16);
  }
  SB();
  const int rb = b < 224 ? b : 223;
  const int mat = rb / 112, cg = (rb % 112) % 28, rq = (rb % 112) / 28, dgp = rb & 7, fb = rb >> 3;
  v4u wa[4], wb[2];
  {
    const u16* W = mat ? wu : wg;
#pragma unroll
    for (int i = 0; i < 4; ++i) wa[i] = *(const v4u*)(W + (size_t)(128 * rq + (t >> 3) + 32 * i) * F + 64 * cg + (t & 7) * 8);
#pragma unroll
    for (int i = 0; i < 2; ++i) wb[i] = *(const v4u*)(wd + (size_t)(64 * fb + (t >> 3) + 32 * i) * D + 64 * dgp + (t & 7) * 8);
  }
  u32 pfv;
  {
    u64 pc; u32 len;
    asm volatile("s_getpc_b64 %0\n.Lkf_here:\n\ts_mov_b32 %1, .Lkf_end-.Lkf_here" : "=s"(pc), "=s"(len));
    const u32 off = min((u32)((bi * 32 + (t & 31)) * 64), len);
    asm volatile("global_load_dword %0, %1, %2" : "=v"(pfv) : "v"(off), "s"(pc));
  }
  SB();
  {
    const v4u z = {0, 0, 0, 0};
    const int tt = t & 127;
    LAS char* c2 = sm + (t >> 7) * CH_B;
    *(LAS v4u*)(c2 + OFF_K + (tt / 33) * KC_B + (255 + tt % 33) * 16) = z;
    if (tt < 4) *(LAS v4u*)(c2 + OFF_K + 3 * KC_B + (255 + 29 + tt) * 16) = z;
    if (tt < 16) *(LAS v4u*)(c2 + OFF_U + (tt >> 3) * UC_B + (tt & 7) * 16) = z;
  }
  const u32 tag = __builtin_amdgcn_readfirstlane(ep) + 1u;
  char* const wr = ws + WS_ROT + (size_t)(tag & 7u) * WS_USED;
  const auto rs = __builtin_amdgcn_make_buffer_rsrc(wr, 0, WS_USED, 0x00020000);
  const v4u srd = {(u32)(uintptr_t)wr, (u32)((uintptr_t)wr >> 32) & 0xffffu, WS_USED, 0x00020000u};
  u32 pfw;
  {
    const char* pa = lane < 48 ? wr + (size_t)lane * (WS_USED / 48) : (const char*)out + (size_t)(lane - 48) * (L * D * 2 / 16);
    asm volatile("global_load_dword %0, %1, off" : "=v"(pfw) : "v"(pa));
  }
#pragma unroll
  for (int i = 0; i < 4; ++i)
    __builtin_amdgcn_raw_buffer_store_b128(v4u{xq[i][0], tag, xq[i][1], tag}, rs, (16 * i + (lane >> 2)) * 16384 + (lane & 3) * 16, WS_XG + w * (64 * 16384) + b * 64, 16);
  SB();
  {
    float v[2][8], s[2], q[2];
#pragma unroll
    for (int i = 0; i < 2; ++i) {
#pragma unroll
      for (int k = 0; k < 4; ++k) { v[i][2 * k] = bflo(xa[i][k]); v[i][2 * k + 1] = bfhi(xa[i][k]); }
      s[i] = ((v[i][0] + v[i][1]) + (v[i][2] + v[i][3])) + ((v[i][4] + v[i][5]) + (v[i][6] + v[i][7]));
    }
#pragma unroll
    for (int i = 0; i < 2; ++i) s[i] = wave_sum(s[i]) * (1.0f / D);
#pragma unroll
    for (int i = 0; i < 2; ++i) {
      q[i] = 0;
#pragma unroll
      for (int k = 0; k < 8; ++k) { const float d = v[i][k] - s[i]; q[i] += d * d; }
    }
#pragma unroll
    for (int i = 0; i < 2; ++i) q[i] = wave_sum(q[i]) * (1.0f / D);
    const float m = (lane & 1) ? s[1] : s[0], vv = (lane & 1) ? q[1] : q[0];
    const u32 pk = pkbf(m, __builtin_sqrtf(rbf(rbf(vv) + 1e-5f)));
    SB();
    WAITV(12);
    if (lane < 16) __builtin_amdgcn_raw_buffer_store_b64(v2u{pk, tag}, rs, (lane >> 1) * 16384 + (lane & 1) * 8, WS_GRAN + (8 * b + 2 * w) * 8, 16);
    SB();
  }
  STAMP(1);
  const float dt = rbf(expf(bf2f(r_ldt)));
  const float ar = -rbf(expf(bf2f(r_lar))) * dt, ai = bf2f(r_aim) * dt;
  int ex = (int)(wave_umax(max(max(r_cre & 0x7fffu, r_cim & 0x7fffu), max(r_cre2 & 0x7fffu, r_cim2 & 0x7fffu))) >> 7);
  ex = ex < 8 ? 8 : (ex > 250 ? 250 : ex);
  const float scl = __uint_as_float((u32)(257 - ex) << 23);
  const float yscl = 2.0f * dt * __uint_as_float((u32)(ex - 3) << 23);
  {
    LAS char* tb = cb + n * 4;
#define PUT(col, v) *(LAS u32*)(tb + (col) * TCOL) = pkh((v)[0], (v)[1])
    const v4u sent = {~0u, ~0u, ~0u, ~0u};
#define SENT(i) do { SB(); __builtin_amdgcn_raw_buffer_store_b128(sent, rs, lane16 + ((i) & 3) * 1024, WS_ACT + ((mt * 4 + w) * 112 + j * 7 + ((i) & 4)) * 1024, 16); SB(); } while (0)
    cf zp[17], zb[8], a[16], pa, pb;
    zp[1] = cexp_p(ar, ai, 1.0f); zp[16] = cexp_p(ar, ai, 16.0f); zb[1] = cexp_p(ar, ai, 256.0f);
    const cf wc = {zp[16][0], -zp[16][1]};
    a[0] = cf{bf2f(r_cre) * scl, -bf2f(r_cim) * scl};
    *(LAS u32*)(tb + 16 * TCOL) = 0x00003c00u;
    PUT(17, zp[1]); PUT(32, zb[1]); PUT(0, a[0]);
    SENT(0);
    cmul2(zp[2], zp[1], zp[1], zb[2], zb[1], zb[1]);
    cmul2(zp[3], zp[2], zp[1], zp[4], zp[2], zp[2]);
    cmul2(zb[3], zb[2], zb[1], zb[4], zb[2], zb[2]);
    cmul2(a[1], a[0], wc, zp[8], zp[4], zp[4]);
    PUT(18, zp[2]); PUT(19, zp[3]); PUT(20, zp[4]); PUT(41, zb[2]); PUT(46, zb[3]); PUT(51, zb[4]);
    SENT(1);
    cmul2(a[2], a[1], wc, zp[5], zp[4], zp[1]);
    cmul2(a[3], a[2], wc, zp[6], zp[4], zp[2]);
    cmul2(a[4], a[3], wc, zp[7], zp[4], zp[3]);
    PUT(1, a[1]); PUT(2, a[2]); PUT(3, a[3]); PUT(4, a[4]); PUT(21, zp[5]); PUT(22, zp[6]); PUT(23, zp[7]); PUT(24, zp[8]);
    SENT(2);
    sfor<9, 16>([&](auto i) { cmul2(a[i - 4], a[i - 5], wc, zp[i], zp[8], zp[i - 8]); PUT(i - 4, a[i - 4]); PUT(16 + i, zp[i]); });
    SENT(3);
    cmul2(a[12], a[11], wc, zb[5], zb[4], zb[1]);
    cmul2(a[13], a[12], wc, zb[6], zb[4], zb[2]);
    cmul2(a[14], a[13], wc, zb[7], zb[4], zb[3]);
    cmul2(a[15], a[14], wc, pa, zb[1], zp[16]);
    PUT(12, a[12]); PUT(13, a[13]); PUT(14, a[14]); PUT(15, a[15]); PUT(54, zb[5]); PUT(57, zb[6]); PUT(60, zb[7]); PUT(40, pa);
    SENT(4);
    sfor<0, 3>([&](auto i) { cmul2(pa, zb[1], zp[4 * i + 2], pb, zb[1], zp[4 * i + 4]); PUT(33 + 2 * i, pa); PUT(34 + 2 * i, pb); });
    cmul2(pa, zb[1], zp[14], pb, zb[1], zp[14]); PUT(39, pa);
    SENT(5);
    sfor<1, 5>([&](auto i) { cmul2(pa, zb[2], zp[4 * i], pb, zb[3], zp[4 * i]); PUT(41 + i, pa); PUT(46 + i, pb); });
    SENT(6);
    sfor<4, 8>([&](auto m) { cmul2(pa, zb[m], zp[8], pb, zb[m], zp[16]); PUT(52 + 3 * (m - 4), pa); PUT(53 + 3 * (m - 4), pb); });
#undef PUT
#undef SENT
  }
  STAMP(2);
  SB();
#pragma unroll
  for (int i = 0; i < 4; ++i) *(LAS v4u*)(sm + L_RT1 + ((t >> 3) + 32 * i) * 144 + (t & 7) * 16) = wa[i];
#pragma unroll
  for (int i = 0; i < 2; ++i) *(LAS v4u*)(sm + L_RT2 + ((t >> 3) + 32 * i) * 144 + (t & 7) * 16) = wb[i];
  BAR();
  STAMP(3);
  if (b < 224) {
    const int g16 = 4 * cg + w, jj = g16 / 7, pt = g16 % 7, h = G & 1;
    float cs = 0;
#pragma unroll
    for (int it = 0; it < 4; ++it) {
      const int ksl = 2 * it + (G >> 1), ks = 8 * rq + ksl;
      const v4u o = trfrag(sm + L_RT1 + (16 * ksl + 8 * h + q4) * 144 + 32 * w + 8 * p4, 4 * 144);
#pragma unroll
      for (int e = 0; e < 4; ++e) cs = dot2bf(o[e], 0x3f803f80u, cs);
      const int fr = pt < 4 ? pt * 32 + ks : 128 + ks * 3 + (pt - 4);
      __builtin_amdgcn_raw_buffer_store_b128(o, rs, fr * 1024 + (32 * h + 16 * mat + i16) * 16, WS_WT1 + jj * 229376, 16);
    }
    cs = sum32x(sum16x(cs));
    if (lane < 16) __builtin_amdgcn_raw_buffer_store_b32(__float_as_uint(cs), rs, lane * 4, WS_CSUM + jj * 4096 + (rq * 224 + mat * 112 + pt * 16) * 4, 16);
#pragma unroll
    for (int it = 0; it < 2; ++it) {
      const int sl = 2 * it + (G >> 1), s = 4 * fb + sl, jp = s / 7, pp = s % 7;
      const v4u o = trfrag(sm + L_RT2 + (16 * sl + 4 * h + q4) * 144 + 32 * w + 8 * p4, 8 * 144);
      const int pos = pp < 4 ? (pp >> 1) * 8 + (jp & 3) * 2 + (pp & 1) : 16 + (jp & 3) * 3 + (pp - 4);
      const int xrow = (i16 & 3) + 8 * (i16 >> 2) + 4 * (w & 1);
      __builtin_amdgcn_raw_buffer_store_b128(o, rs, (((jp >> 2) * 28 + pos) * 2 + (w >> 1)) * 1024 + (32 * h + xrow) * 16, WS_WT2 + dgp * 229376, 16);
    }
  }
  SB();
  STAMP(18);
  {
    v4u Bf[8], Af[2][8];
    const int c2 = (i16 + ((i16 >> 1) & 1)) >> 1, c4 = (i16 + 1 + ((i16 >> 2) & 1)) >> 2, c8 = (i16 + 3 + ((i16 >> 3) & 1)) >> 3;
    LAS char* ap[4];
    ap[0] = cb + (nh ? 32 + c2 : 16 + i16) * TCOL + G * 16;
    ap[1] = cb + (41 + 5 * nh + c4) * TCOL + G * 16;
    ap[2] = cb + (51 + 3 * nh + c8) * TCOL + G * 16;
    ap[3] = ap[2] + 6 * TCOL;
#pragma unroll
    for (int ks = 0; ks < 8; ++ks) { Bf[ks] = *(LAS v4u*)(cb + i16 * TCOL + ks * 64 + G * 16); Af[0][ks] = *(LAS v4u*)(ap[0] + ks * 64); }
    SB();
    v4f d[4];
    sfor<0, 4>([&](auto mm) {
      sfor<0, 8>([&](auto ks) {
        if constexpr (ks == 0) mfma16z(d[mm], Af[mm & 1][ks], Bf[ks]);
        else mfma16(d[mm], Af[mm & 1][ks], Bf[ks]);
        if constexpr (mm < 3) Af[(mm & 1) ^ 1][ks] = *(LAS v4u*)(ap[mm + 1] + ks * 64);
        SB();
      });
    });
    asm volatile("s_nop 7" : "+v"(d[0]), "+v"(d[1]), "+v"(d[2]), "+v"(d[3]));
    LAS char* kp = cb + OFF_K + 16 + (2038 - 256 * nh - 16 * i16 - 4 * G - 3 * 512) * 2;
#pragma unroll
    for (int mm = 0; mm < 4; ++mm) {
      const u32 p0 = pkh(d[mm][3], d[mm][2]), p1 = pkh(d[mm][1], d[mm][0]);
#pragma unroll
      for (int e = 0; e < 4; ++e) {
        LAS char* k2 = kp + (3 - mm) * 1024 + e * (KC_B + 4);
        if (e & 1) *(LAS v2u*)k2 = v2u{p0, p1};
        else { *(LAS u32*)k2 = p0; *(LAS u32*)(k2 + 4) = p1; }
      }
    }
  }
  WAITV(0);
  asm volatile("" :: "v"(pfv), "v"(pfw));
  if (lane == 0 && b < 224) __builtin_amdgcn_raw_buffer_store_b32(tag, rs, 0, WS_WFLAG + (b * 4 + w) * 16, 16);
  STAMP(4);
  {
    v4u g[4], gx[4];
    for (int spin = 0; spin < SPIN_CAP; ++spin) {
      asm volatile("" ::: "memory");
#pragma unroll
      for (int i = 0; i < 4; ++i) gx[i] = __builtin_amdgcn_raw_buffer_load_b128(rs, t * 16 + i * 4096, WS_XG + pr * 16384, 16);
#pragma unroll
      for (int i = 0; i < 4; ++i) g[i] = __builtin_amdgcn_raw_buffer_load_b128(rs, t * 16 + i * 4096, WS_GRAN + xcd * 16384, 16);
      u32 df = 0;
#pragma unroll
      for (int i = 0; i < 4; ++i) df |= ((g[i][1] ^ tag) | (g[i][3] ^ tag)) | ((gx[i][1] ^ tag) | (gx[i][3] ^ tag));
      if (__all(df == 0)) break;
      COUNT(0);
    }
    STAMP(5);
    LAS char* cc[2] = {sm + t * 4, sm + CH_B + t * 4};
    asm volatile("" : "+v"(cc[1]));
    const u32 ub0 = t * 4, ub1 = CH_B + t * 4;
    sfor<0, 4>([&](auto i) {
      const float m0 = bflo(g[i][0]), m1 = bflo(g[i][2]);
      const float r0 = __builtin_amdgcn_rcpf(bfhi(g[i][0])), r1 = __builtin_amdgcn_rcpf(bfhi(g[i][2]));
#pragma unroll
      for (int c = 0; c < 2; ++c) {
        const u32 d = pkbf((c ? bfhi(gx[i][0]) : bflo(gx[i][0])) - m0, (c ? bfhi(gx[i][2]) : bflo(gx[i][2])) - m1);
        const u32 o = pkbf(bflo(d) * r0, bfhi(d) * r1);
        const u32 uh = pkh(bflo(o), bfhi(o));
        LAS char* c2 = cc[c] + i * 1024;
        *(LAS u32*)(c2 + OFF_U + UC_B + 112) = uh;
        dsw_halves<OFF_U + 114 + i * 1024>(c ? ub1 : ub0, uh);
        *(LAS u32*)(c2 + OFF_X) = __builtin_amdgcn_perm(gx[i][2], gx[i][0], c ? 0x07060302u : 0x05040100u);
      }
    });
  }
  BAR();
  if (b == 0 && t == 0) __builtin_amdgcn_raw_buffer_store_b32(tag, rs0, 0, WS_EPOCH, 16);
  STAMP(6);
  v4f acc[4];
  {
    u32 fo;
    {
      int g16, pw;
      if (lane < 8) g16 = 7 * j + w; else g16 = 7 * j + 4 + ((lane - 8) >> 3);
      pw = ((lane & 1) * 112 + ((lane >> 1) & 3) * 28 + (g16 >> 2)) * 4 + (g16 & 3);
      if (lane >= 32) { const int k = lane < 60 ? lane - 32 : 0; pw = ((7 * w + (k >> 2)) * 8 + dg) * 4 + (k & 3); }
      fo = pw * 16;
    }
    const u32 sW1 = WS_WT1 + j * 229376 + w * 32768, sW2 = WS_WT1 + j * 229376 + 131072;
    u32 fl;
    auto extra = [&](auto r, auto m) {
      if constexpr (r == 2 && m == 8) ld_flag(fl, srd, fo, WS_WFLAG);
      if constexpr (r == 3 && m == 17) {
        STAMP(16);
        wait0_pin(fl);
        for (int spin = 0; spin < SPIN_CAP && !__all(fl == tag); ++spin) { ld_flag(fl, srd, fo, WS_WFLAG); wait0_pin(fl); COUNT(1); }
        STAMP(17);
      }
      if constexpr (r >= 4) {
        constexpr int k = (r - 4) * 18 + m;
        if constexpr (k < 32) ld_agpr<A_W + 4 * k, (k & 3) * 1024>(srd, lane16, sW1 + (k >> 2) * 4096);
        else if constexpr (k < 38) dma16(srd, lane16, sW2 + (w * 6 + k - 32) * 1024, L_W2 + (w * 6 + k - 32) * 1024);
        else if constexpr (k == 38) dma16(srd, lane16, WS_CSUM + j * 4096 + w * 1024, L_CSP + w * 1024);
      }
    };
    LAS char* ub = cb + OFF_U + (i16 & 1) * UC_B + ((i16 >> 1) + G) * 16;
    LAS char* kb = cb + OFF_K + (i16 & 3) * KC_B + 16 + (248 - 8 * (i16 >> 2) + G) * 16;
    if (nh == 0) conv_main<0>(ub, kb, acc, extra);
    else conv_main<1>(ub, kb, acc, extra);
  }
  STAMP(7);
  {
    const float dsk = bf2f(r_dsk);
#pragma unroll
    for (int a = 0; a < 4; ++a) {
#pragma unroll
      for (int hh = 0; hh < 2; ++hh) {
        const int l = 256 * (4 * (a >> 1) + ((a & 1) ? 3 - nh : nh)) + 8 * (2 * G + hh) + 2 * (i16 & 3) + 64 * (i16 >> 2);
        const v2h up = *(LAS v2h*)(cb + OFF_U + UC_B + (l + 56) * 2);
        LAS u32* xp = (LAS u32*)(cb + OFF_X + l * 2);
        const u32 xv = *xp;
        const u32 yb = pkbf(acc[a][2 * hh] * yscl, acc[a][2 * hh + 1] * yscl);
        const u32 sk = pkbf((float)up[0] * dsk, (float)up[1] * dsk);
        const u32 zz = pkbf(bflo(yb) + bflo(sk), bfhi(yb) + bfhi(sk));
        *xp = min(pkbf(bflo(xv) + bflo(zz), bfhi(xv) + bfhi(zz)), 0xfffffffeu);
      }
    }
    WAITL0();
#pragma unroll
    for (int i = 0; i < 2; ++i) {
      const int c = 4 * i + ((lane >> 5) ? 3 - nh : nh);
      const int l = 256 * c + (lane & 31) * 8;
      __builtin_amdgcn_raw_buffer_store_b128(*(LAS v4u*)(cb + OFF_X + l * 2), rs0, l * 2, WS_HT + ch * 4096, 16);
    }
  }
  WAITV(0);
  BAR();
  STAMP(8);
  const u32 vtok = (lane >> 4) * 4096 + (((lane & 15) ^ (4 * (lane >> 4))) * 16);
  const u32 stok = WS_HT + mt * 256 + w * 65536;
  auto tok_dma = [&](int c) {
#pragma unroll
    for (int i = 0; i < 4; ++i) dma16(srd0, vtok, stok + (64 * c + 4 * i) * 4096, c * 16384 + (4 * w + i) * 1024);
  };
#pragma unroll
  for (int c = 0; c < 8; ++c) tok_dma(c);
  LAS char* tp[4][2];
#pragma unroll
  for (int i = 0; i < 4; ++i) {
    tp[i][0] = sm + (8 * (G >> 1) + q4) * 256 + 8 * (p4 & 1) + (((4 * (w ^ i) + 2 * (G & 1) + (p4 >> 1)) ^ (4 * q4)) * 16);
    tp[i][1] = tp[i][0] + 65536;
    asm volatile("" : "+v"(tp[i][1]));
  }
  LAS char* vp[2];
  vp[0] = sm + w * 4096 + lane16; vp[1] = vp[0] + 65536;
  asm volatile("" : "+v"(vp[1]));
  v4u own[32], vq[4];
  u32 vm = 0;
  float sA = 0, sB = 0, qA = 0, qB = 0;
  auto mx = [&](u32 m, const v4u& v) { return max(max(m, max(v[0], v[1])), max(v[2], v[3])); };
  auto own_rd = [&](auto s) { own[s] = trfrag(tp[0][s >> 4] + (s & 15) * 4096, 1024); };
  auto own_acc = [&](auto s) {
#pragma unroll
    for (int e = 0; e < 4; e += 2) {
      sA = dot2bf(own[s][e], 0x3f803f80u, sA); qA = dot2bf(own[s][e], own[s][e], qA);
      sB = dot2bf(own[s][e + 1], 0x3f803f80u, sB); qB = dot2bf(own[s][e + 1], own[s][e + 1], qB);
    }
    pin2(sA, qA); pin2(sB, qB);
  };
  auto v_rd = [&](auto c, auto i) { vq[i] = *(LAS v4u*)(vp[c >> 2] + (c & 3) * 16384 + i * 1024); };
  auto fix = [&](auto c0, auto c1) {
    for (int spin = 0; spin < SPIN_CAP && __any(vm == ~0u); ++spin) {
      COUNT(2 + (c0 >> 1));
#pragma unroll
      for (int c = c0; c < c1; ++c) tok_dma(c);
      WAITV(0);
      vm = 0;
      sfor<c0, c1>([&](auto c) {
        sfor<0, 4>([&](auto i) { v_rd(c, i); });
        sfor<0, 4>([&](auto i) { vm = mx(vm, vq[i]); });
      });
    }
    vm = 0;
  };
  WAITV(28);
  sfor<0, 4>([&](auto i) { v_rd(IC<0>{}, i); });
  sfor<0, 4>([&](auto i) { vm = mx(vm, vq[i]); });
  SB();
  WAITV(24);
  sfor<0, 4>([&](auto i) { v_rd(IC<1>{}, i); });
  sfor<0, 4>([&](auto i) { vm = mx(vm, vq[i]); });
  fix(IC<0>{}, IC<2>{});
  BAR();
  STAMP(9);
  const u32 sW2 = WS_WT1 + j * 229376 + 131072;
  auto w2_dma = [&](int g) {
#pragma unroll
    for (int i = 0; i < 3; ++i) dma16(srd, lane16, sW2 + g * 12288 + (3 * w + i) * 1024, (g - 2) * 16384 + (3 * w + i) * 1024);
  };
  v4u hres[2];
  {
    v4u ot[2][3];
    LAS char* hp = sm + (64 * dg + 32 * (w >> 1) + 16 * (G >> 1) + q4) * 256 + 8 * (p4 & 1) +
                   (((4 * (2 * e2 + (w & 1)) + 2 * (G & 1) + (p4 >> 1)) ^ (4 * q4)) * 16);
    auto ot_rd = [&](auto s, auto i) { ot[s & 1][i] = trfrag(tp[i + 1][s >> 4] + (s & 15) * 4096, 1024); };
    own_rd(IC<0>{});
    sfor<0, 3>([&](auto i) { ot_rd(IC<0>{}, i); });
    if (t < 224) {
      LAS float* cp = (LAS float*)(sm + L_CSP) + t;
      *(LAS float*)(sm + L_CS + t * 4) = (cp[0] + cp[224]) + (cp[448] + cp[672]);
    }
    SB();
    sfor<0, 32>([&](auto s) {
      constexpr int vc = s == 3 ? 2 : s == 5 ? 3 : s == 8 ? 4 : s == 9 ? 5 : s == 11 ? 6 : s == 13 ? 7 : -1;
      constexpr bool vmx = s == 4 || s == 6 || s == 9 || s == 10 || s == 12 || s == 14;
      constexpr int o0 = s < 15 ? s + 1 : s < 23 ? 16 + 2 * (s - 15) : -1, o1 = (s >= 15 && s < 23) ? o0 + 1 : -1;
      if constexpr (s == 5) sfor<0, 6>([&](auto k) { own_acc(k); });
      if constexpr (s == 13) sfor<6, 14>([&](auto k) { own_acc(k); });
      if constexpr (s == 20) sfor<14, 24>([&](auto k) { own_acc(k); });
      if constexpr (s == 24) sfor<24, 32>([&](auto k) { own_acc(k); });
      if constexpr (vc >= 0) WAITV(4 * (7 - vc) + (s > 6 ? 6 : 0));
      mfma_aav<A_ACC1, A_W + 4 * s, s == 0>(own[s]);
      if constexpr (s < 31) ot_rd(IC<s + 1>{}, IC<0>{});
      if constexpr (vmx) { vm = mx(vm, vq[0]); vm = mx(vm, vq[1]); }
      SB();
      mfma_aav<A_ACC1 + 16, A_W + 4 * s, s == 0>(ot[s & 1][0]);
      if constexpr (s < 31) ot_rd(IC<s + 1>{}, IC<1>{});
      if constexpr (vmx) { vm = mx(vm, vq[2]); vm = mx(vm, vq[3]); }
      if constexpr (o1 >= 0) own_rd(IC<o1>{});
      SB();
      mfma_aav<A_ACC1 + 32, A_W + 4 * s, s == 0>(ot[s & 1][1]);
      if constexpr (s < 31) ot_rd(IC<s + 1>{}, IC<2>{});
      if constexpr (vc >= 0) { v_rd(IC<vc>{}, IC<0>{}); v_rd(IC<vc>{}, IC<1>{}); }
      SB();
      mfma_aav<A_ACC1 + 48, A_W + 4 * s, s == 0>(ot[s & 1][2]);
      if constexpr (o0 >= 0) own_rd(IC<o0>{});
      if constexpr (vc >= 0) { v_rd(IC<vc>{}, IC<2>{}); v_rd(IC<vc>{}, IC<3>{}); }
      if constexpr (s == 1) { if (dg < 2) { hres[0] = trfrag(hp, 1024); hres[1] = trfrag(hp + 2048, 1024); } }
      if constexpr (s == 7) { if ((dg >> 1) == 1) { hres[0] = trfrag(hp, 1024); hres[1] = trfrag(hp + 2048, 1024); } }
      if constexpr (s == 16) { if (dg >= 4) { hres[0] = trfrag(hp, 1024); hres[1] = trfrag(hp + 2048, 1024); } }
      SB();
      if constexpr (s == 6) { fix(IC<2>{}, IC<4>{}); BAR(); STAMP(19); w2_dma(2); w2_dma(3); SB(); }
      if constexpr (s == 14) { fix(IC<4>{}, IC<8>{}); BAR(); STAMP(20); w2_dma(4); w2_dma(5); SB(); }
      if constexpr (s == 22) { BAR(); w2_dma(6); w2_dma(7); SB(); }
      if constexpr (s == 26) {
        const float sa = sum32x(sA + sB), qa = sum32x(qA + qB);
        const float m = sa * (1.0f / D);
        const float var = fmaxf(qa * (1.0f / D) - m * m, 0.0f);
        const float sd = rbf(__builtin_sqrtf(rbf(rbf(var) + 1e-5f)));
        if (lane < 32) *(LAS v2f*)(sm + L_MR + (w * 32 + lane) * 8) = v2f{rbf(m), __builtin_amdgcn_rcpf(sd)};
        SB();
      }
    });
  }
  WAITV(6);
  BAR();
  STAMP(10);
  const u32 sAct = WS_ACT + ((mt * 4) * 112 + j * 7) * 1024;
  const u32 sWD = WS_WT2 + dg * 229376 + w * 57344;
  const u32 sAin = WS_ACT + ((mt * 4 + 2 * e2) * 112 + 28 * w) * 1024;
  v4u acte[8][2], actl[8][2], actb[12][2];
  auto ld_early = [&](auto i, auto k) { ld_vgpr<(i & 1) * 1024>(acte[i][k], srd, lane16, sAin + (k * 112 + 7 * (i >> 1) + 2 * e2) * 1024); };
  auto ld_late = [&](auto i, auto k) { ld_vgpr<(i & 1) * 1024>(actl[i][k], srd, lane16, sAin + (k * 112 + 7 * (i >> 1) + 2 - 2 * e2) * 1024); };
  auto ld_b = [&](auto i, auto k) { ld_vgpr<(i % 3) * 1024>(actb[i][k], srd, lane16, sAin + (k * 112 + 7 * (i / 3) + 4) * 1024); };
  const int hh = lane >> 5;
  v2f mr[4];
  {
#pragma unroll
    for (int i = 0; i < 4; ++i) mr[i] = *(LAS v2f*)(sm + L_MR + ((w ^ i) * 32 + (lane & 31)) * 8);
    LAS char* cp = sm + L_CS + (w * 16 + 4 * hh) * 4;
    const v4f cg0 = *(LAS v4f*)cp, cg1 = *(LAS v4f*)(cp + 32), cu0 = *(LAS v4f*)(cp + 448), cu1 = *(LAS v4f*)(cp + 480);
    LAS char* wp[3] = {sm + lane16, sm + 65536 + lane16, sm + L_W2 + lane16};
    asm volatile("" : "+v"(wp[1]), "+v"(wp[2]));
    v4u w2[2][3];
    auto w2_rd = [&](auto s, auto i) {
      constexpr int g = s >> 2;
      constexpr int off = (s & 3) * 3072 + i * 1024;
      if constexpr (g < 2) w2[s & 1][i] = *(LAS v4u*)(wp[2] + g * 12288 + off);
      else if constexpr (g < 6) w2[s & 1][i] = *(LAS v4u*)(wp[0] + (g - 2) * 16384 + off);
      else w2[s & 1][i] = *(LAS v4u*)(wp[1] + (g - 6) * 16384 + off);
    };
    sfor<0, 3>([&](auto i) { w2_rd(IC<0>{}, i); });
    float rl[4], r2[4];
#pragma unroll
    for (int i = 0; i < 4; ++i) { rl[i] = mr[i][1] * -1.44269504088896340736f; r2[i] = mr[i][1] * mr[i][1]; }
    SB();
    v4u o;
    float prev = 0;
    sfor<0, 32>([&](auto s) {
      constexpr int un = s >> 3, e = s & 7;
      if constexpr (s == 20) { WAITV(22); BARX(); }
      mfma_avv<A_ACC2, s == 0>(w2[s & 1][0], own[s]);
      if constexpr (s < 31) w2_rd(IC<s + 1>{}, IC<0>{});
      ld_agpr<A_W + 4 * s, (s & 3) * 1024>(srd, lane16, sWD + ((s < 16 ? e2 : 1 - e2) * 16 + (s & 12)) * 1024);
      const float tg = __builtin_fmaf(-mr[un][0], e < 4 ? cg0[e & 3] : cg1[e & 3], accrd<A_ACC1 + 16 * un + e>());
      const float tu = __builtin_fmaf(-mr[un][0], e < 4 ? cu0[e & 3] : cu1[e & 3], accrd<A_ACC1 + 16 * un + 8 + e>());
      SB();
      mfma_avv<A_ACC2 + 16, s == 0>(w2[s & 1][1], own[s]);
      if constexpr (s < 31) w2_rd(IC<s + 1>{}, IC<1>{});
      if constexpr (s >= 24) ld_early(IC<2 * (s & 3)>{}, IC<(s >= 28)>{});
      const float ex = __builtin_amdgcn_exp2f(tg * rl[un]);
      const float p = tg * tu * r2[un];
      const float dn = 1.0f + ex;
      SB();
      mfma_avv<A_ACC2 + 32, s == 0>(w2[s & 1][2], own[s]);
      if constexpr (s < 31) w2_rd(IC<s + 1>{}, IC<2>{});
      if constexpr (s >= 24) ld_early(IC<2 * (s & 3) + 1>{}, IC<(s < 28)>{});
      const float v = p * __builtin_amdgcn_rcpf(dn);
      if constexpr (e & 1) o[e >> 1] = min(pkbf(prev, v), 0xfffffffeu);
      else prev = v;
      if constexpr (e == 7) __builtin_amdgcn_raw_buffer_store_b128(o, rs, lane16, sAct + ((w ^ un) * 112 + w) * 1024, 16);
      SB();
    });
  }
  STAMP(11);
  asm volatile("s_nop 7\n\ts_nop 3" ::: "memory");
  sfor<0, 3>([&](auto un) {
    LAS char* cp = sm + L_CS + ((4 + un) * 16 + 4 * hh) * 4;
    const v4f cg0 = *(LAS v4f*)cp, cg1 = *(LAS v4f*)(cp + 32), cu0 = *(LAS v4f*)(cp + 448), cu1 = *(LAS v4f*)(cp + 480);
    float g[8], u[8];
    sfor<0, 8>([&](auto e) {
      g[e] = __builtin_fmaf(-mr[0][0], e < 4 ? cg0[e & 3] : cg1[e & 3], accrd<A_ACC2 + 16 * un + e>()) * mr[0][1];
      u[e] = __builtin_fmaf(-mr[0][0], e < 4 ? cu0[e & 3] : cu1[e & 3], accrd<A_ACC2 + 16 * un + 8 + e>()) * mr[0][1];
    });
    v4u o;
    silu_mul8(g, u, o);
    __builtin_amdgcn_raw_buffer_store_b128(o, rs, lane16, sAct + (w * 112 + 4 + un) * 1024, 16);
  });
  SB();
  BARX();
  STAMP(12);
  sfor<0, 8>([&](auto i) { ld_late(i, IC<(i & 1)>{}); });
  sfor<0, 8>([&](auto i) { ld_late(i, IC<(i & 1) ^ 1>{}); });
#define PIN8(a) "+v"(a[0][0]), "+v"(a[0][1]), "+v"(a[1][0]), "+v"(a[1][1]), "+v"(a[2][0]), "+v"(a[2][1]), "+v"(a[3][0]), "+v"(a[3][1]), \
                "+v"(a[4][0]), "+v"(a[4][1]), "+v"(a[5][0]), "+v"(a[5][1]), "+v"(a[6][0]), "+v"(a[6][1]), "+v"(a[7][0]), "+v"(a[7][1])
#define PIN4(a) "+v"(a[8][0]), "+v"(a[8][1]), "+v"(a[9][0]), "+v"(a[9][1]), "+v"(a[10][0]), "+v"(a[10][1]), "+v"(a[11][0]), "+v"(a[11][1])
  auto g2step = [&](auto first, auto aw, const v4u& b0, const v4u& b1, auto&& ride) {
    mfma_aav<A_ACC1, aw, first>(b0); ride(IC<0>{}); SB();
    mfma_aav<A_ACC1 + 16, aw, first>(b1); ride(IC<1>{}); SB();
    mfma_aav<A_ACC1 + 32, aw + 4, first>(b0); ride(IC<2>{}); SB();
    mfma_aav<A_ACC1 + 48, aw + 4, first>(b1); ride(IC<3>{}); SB();
  };
  asm volatile("s_waitcnt vmcnt(20)" : PIN8(acte) :: "memory");
  for (int spin = 0; spin < SPIN_CAP; ++spin) {
    u32 m = 0;
    sfor<0, 8>([&](auto i) { m = mx(mx(m, acte[i][0]), acte[i][1]); });
    if (!__any(m == ~0u)) break;
    COUNT(5);
    sfor<0, 8>([&](auto i) { ld_early(i, IC<0>{}); ld_early(i, IC<1>{}); });
    asm volatile("s_waitcnt vmcnt(0)" : PIN8(acte) :: "memory");
  }
  SB();
  STAMP(13);
  sfor<0, 8>([&](auto i) {
    g2step(IC<(i == 0)>{}, IC<A_W + 8 * i>{}, acte[i][0], acte[i][1], [&](auto k) {
      if constexpr (i > 0 && k < 2) ld_agpr<A_W + 8 * (i - 1) + 4 * k, ((2 * (i - 1) + k) & 3) * 1024>(srd, lane16, sWD + 32768 + ((2 * (i - 1)) >> 2) * 4096);
    });
  });
  ld_agpr<A_W + 56, 2048>(srd, lane16, sWD + 32768 + 3 * 4096);
  ld_agpr<A_W + 60, 3072>(srd, lane16, sWD + 32768 + 3 * 4096);
  STAMP(21);
  asm volatile("s_waitcnt vmcnt(16)" : PIN8(actl) :: "memory");
  for (int spin = 0; spin < SPIN_CAP; ++spin) {
    u32 m = 0;
    sfor<0, 8>([&](auto i) { m = mx(mx(m, actl[i][0]), actl[i][1]); });
    if (!__any(m == ~0u)) break;
    COUNT(6);
    sfor<0, 8>([&](auto i) { ld_late(i, IC<0>{}); ld_late(i, IC<1>{}); });
    asm volatile("s_waitcnt vmcnt(0)" : PIN8(actl) :: "memory");
  }
  SB();
  sfor<0, 8>([&](auto i) {
    g2step(IC<0>{}, IC<A_W + 64 + 8 * i>{}, actl[i][0], actl[i][1], [&](auto k) {
      if constexpr (i < 6) ld_b(IC<2 * i + (k >> 1)>{}, IC<(k & 1)>{});
      if constexpr (i >= 1 && i < 5 && k < 2) ld_agpr<A_W + 64 + 8 * (i - 1) + 4 * k, ((2 * (i - 1) + k) & 3) * 1024>(srd, lane16, sWD + 32768 + 16384 + ((2 * (i - 1)) >> 2) * 4096);
    });
  });
  STAMP(22);
  asm volatile("s_waitcnt vmcnt(0)" : PIN8(actb) :: "memory");
  asm volatile("" : PIN4(actb));
  for (int spin = 0; spin < SPIN_CAP; ++spin) {
    u32 m = 0;
    sfor<0, 12>([&](auto i) { m = mx(mx(m, actb[i][0]), actb[i][1]); });
    if (!__any(m == ~0u)) break;
    COUNT(7);
    sfor<0, 12>([&](auto i) { ld_b(i, IC<0>{}); ld_b(i, IC<1>{}); });
    asm volatile("s_waitcnt vmcnt(0)" : PIN8(actb) :: "memory");
    asm volatile("" : PIN4(actb));
  }
  SB();
  sfor<0, 12>([&](auto i) { g2step(IC<0>{}, IC<A_W + 8 * i>{}, actb[i][0], actb[i][1], [&](auto) {}); });
  STAMP(14);
  asm volatile("s_nop 7\n\ts_nop 3" ::: "memory");
  float a[16];
  auto xch = [&](auto W) {
    const u32 ad = W * 4096 + lane16;
    sfor<0, 4>([&](auto k) {
      if constexpr (k != W) {
        sfor<0, 4>([&](auto e) {
          dsw_agpr<A_ACC1 + 16 * k + 4 * e, k * 16384 + e * 1024>(ad);
        });
      }
    });
    sfor<0, 16>([&](auto e) { a[e] = accrd<A_ACC1 + 16 * W + e>(); });
  };
  if (w == 0) xch(IC<0>{}); else if (w == 1) xch(IC<1>{}); else if (w == 2) xch(IC<2>{}); else xch(IC<3>{});
  BAR();
#pragma unroll
  for (int k = 1; k < 4; ++k) {
#pragma unroll
    for (int e = 0; e < 4; ++e) {
      const v4f p = *(LAS v4f*)(sm + w * 16384 + (w ^ k) * 4096 + e * 1024 + lane16);
#pragma unroll
      for (int c = 0; c < 4; ++c) a[4 * e + c] += p[c];
    }
  }
  {
    u32 o[8];
#pragma unroll
    for (int e = 0; e < 8; ++e) {
      const u32 hv = hres[e >> 2][e & 3];
      const u32 f = pkbf(a[2 * e], a[2 * e + 1]);
      o[e] = pkbf(bflo(hv) + bflo(f), bfhi(hv) + bfhi(f));
    }
    u16* op = out + (size_t)(128 * mt + 64 * e2 + 32 * (w & 1) + (lane & 31)) * D + 64 * dg + 32 * (w >> 1) + 16 * hh;
    *(v4u*)op = v4u{o[0], o[1], o[2], o[3]};
    *(v4u*)(op + 8) = v4u{o[4], o[5], o[6], o[7]};
  }
  STAMP(15);
  asm volatile(".Lkf_end:");
}

extern "C" void kernel_launch(void* const* d_in, const int* in_sizes, int n_in, void* d_out, int out_size, void* d_ws, size_t ws_size,
                              hipStream_t stream) {
  (void)hipFuncSetAttribute((const void*)k_fused, hipFuncAttributeMaxDynamicSharedMemorySize, LDS_B);
  k_fused<<<256, 256, LDS_B, stream>>>((const u16*)d_in[0], (const u16*)d_in[1], (const u16*)d_in[2], (const u16*)d_in[3], (const u16*)d_in[4],
                                       (const u16*)d_in[5], (const u16*)d_in[6], (const u16*)d_in[8], (const u16*)d_in[9],
                                       (const u16*)d_in[10], (char*)d_ws, (u16*)d_out);
}
```

```cpp
#include <hip/hip_runtime.h>
#include <hip/hip_bf16.h>
#include <stdint.h>
#include <type_traits>

typedef unsigned short u16;
typedef unsigned int u32;
typedef unsigned long long u64;
typedef short v4s __attribute__((ext_vector_type(4)));
typedef __bf16 v2bf __attribute__((ext_vector_type(2)));
typedef _Float16 v2h __attribute__((ext_vector_type(2)));
typedef float v2f __attribute__((ext_vector_type(2)));
typedef float v4f __attribute__((ext_vector_type(4)));
typedef u32 v2u __attribute__((ext_vector_type(2)));
typedef u32 v4u __attribute__((ext_vector_type(4)));
#define LAS __attribute__((address_space(3)))
#define GAS __attribute__((address_space(1)))
#define DEV __device__ __forceinline__

constexpr int L = 2048, D = 512, NS = 128, F = 1792;

constexpr u32 WS_EPOCH = 0;
constexpr u32 WS_WFLAG = 4096;
constexpr u32 WS_GRAN  = 64 << 10;
constexpr u32 WS_CSUM  = 256 << 10;
constexpr u32 WS_HT    = 1 << 20;
constexpr u32 WS_WT1   = 4 << 20;
constexpr u32 WS_WT2   = 8 << 20;
constexpr u32 WS_ACT   = 12 << 20;
constexpr u32 WS_XG    = 20 << 20;
constexpr u32 WS_USED  = 24 << 20;
constexpr u32 WS_FIX   = 4 << 20;
constexpr size_t WS_ROT = 40 << 20;
constexpr size_t WS_DBG = 32 << 20;

template <int I> using IC = std::integral_constant<int, I>;
template <int B, int E, class Fn> DEV void sfor(Fn&& f) {
  if constexpr (B < E) { f(IC<B>{}); sfor<B + 1, E>(f); }
}

DEV float bf2f(u32 b) { return __uint_as_float(b << 16); }
DEV float bflo(u32 p) { return __uint_as_float(p << 16); }
DEV float bfhi(u32 p) { return __uint_as_float(p & 0xffff0000u); }
DEV u32 pkbf(float a, float b) { v2f f = {a, b}; return __builtin_bit_cast(u32, __builtin_convertvector(f, v2bf)); }
DEV u32 pkh(float a, float b) { v2f f = {a, b}; return __builtin_bit_cast(u32, __builtin_convertvector(f, v2h)); }
DEV float rbf(float a) { return bflo(pkbf(a, a)); }
DEV float dot2bf(u32 a, u32 b, float c) { return __builtin_amdgcn_fdot2_f32_bf16(__builtin_bit_cast(v2bf, a), __builtin_bit_cast(v2bf, b), c, false); }
DEV v2u trread(LAS char* p) { return __builtin_bit_cast(v2u, __builtin_amdgcn_ds_read_tr16_b64_v4i16((LAS v4s*)p)); }
DEV v4u trfrag(LAS char* p, int second) { const v2u a = trread(p), b = trread(p + second); return v4u{a[0], a[1], b[0], b[1]}; }
#define WAITV(n) asm volatile("s_waitcnt vmcnt(%0)" :: "i"(n) : "memory")
#define WAITL0() asm volatile("s_waitcnt lgkmcnt(0)" ::: "memory")
#define BAR() asm volatile("s_waitcnt lgkmcnt(0)\n\ts_barrier" ::: "memory")
#define BARX() asm volatile("s_barrier" ::: "memory")
#define SB() __builtin_amdgcn_sched_barrier(0)
constexpr int SPIN_CAP = 1 << 20;

#define STAMP(slot) do {} while (0)
#define COUNT(slot) do {} while (0)

template <int CTRL> DEV float dpp(float v) { return __builtin_bit_cast(float, __builtin_amdgcn_update_dpp(0, __builtin_bit_cast(int, v), CTRL, 0xf, 0xf, true)); }
DEV float swap16_other(float v, float& o) { auto r = __builtin_amdgcn_permlane16_swap(__float_as_uint(v), __float_as_uint(v), false, false); o = __uint_as_float(r[1]); return __uint_as_float(r[0]); }
DEV float swap32_other(float v, float& o) { auto r = __builtin_amdgcn_permlane32_swap(__float_as_uint(v), __float_as_uint(v), false, false); o = __uint_as_float(r[1]); return __uint_as_float(r[0]); }
DEV float sum16x(float v) { float o; const float a = swap16_other(v, o); return a + o; }
DEV float sum32x(float v) { float o; const float a = swap32_other(v, o); return a + o; }
DEV float wave_sum(float v) {
  v += dpp<0xB1>(v); v += dpp<0x4E>(v); v += dpp<0x141>(v); v += dpp<0x140>(v);
  return sum32x(sum16x(v));
}
DEV float wave_max(float v) {
  v = fmaxf(v, dpp<0xB1>(v)); v = fmaxf(v, dpp<0x4E>(v)); v = fmaxf(v, dpp<0x141>(v)); v = fmaxf(v, dpp<0x140>(v));
  float o; float a = swap16_other(v, o); v = fmaxf(a, o); a = swap32_other(v, o); return fmaxf(a, o);
}

DEV void dma16(v4u srd, u32 voff, u32 soff, u32 lds) {
  asm volatile("s_mov_b32 m0, %3\n\ts_nop 0\n\tbuffer_load_dwordx4 %0, %1, %2 offen sc1 lds" :: "v"(voff), "s"(srd), "s"(soff), "s"(lds) : "memory");
}
template <int A, int IMM> DEV void ld_agpr(v4u srd, u32 voff, u32 soff) {
  asm volatile("buffer_load_dwordx4 a[%c3:%c4], %0, %1, %2 offen offset:%c5 sc1" :: "v"(voff), "s"(srd), "s"(soff), "i"(A), "i"(A + 3), "i"(IMM) : "memory");
}
template <int IMM> DEV void ld_vgpr(v4u& d, v4u srd, u32 voff, u32 soff) {
  asm volatile("buffer_load_dwordx4 %0, %1, %2, %3 offen offset:%c4 sc1" : "=v"(d) : "v"(voff), "s"(srd), "s"(soff), "i"(IMM) : "memory");
}
template <int ACC, int A, bool FIRST> DEV void mfma_aav(const v4u& b) {
  if constexpr (FIRST) asm volatile("v_mfma_f32_32x32x16_bf16 a[%c1:%c2], a[%c3:%c4], %0, 0" :: "v"(b), "i"(ACC), "i"(ACC + 15), "i"(A), "i"(A + 3));
  else asm volatile("v_mfma_f32_32x32x16_bf16 a[%c1:%c2], a[%c3:%c4], %0, a[%c1:%c2]" :: "v"(b), "i"(ACC), "i"(ACC + 15), "i"(A), "i"(A + 3));
}
template <int ACC, bool FIRST> DEV void mfma_avv(const v4u& a, const v4u& b) {
  if constexpr (FIRST) asm volatile("v_mfma_f32_32x32x16_bf16 a[%c2:%c3], %0, %1, 0" :: "v"(a), "v"(b), "i"(ACC), "i"(ACC + 15));
  else asm volatile("v_mfma_f32_32x32x16_bf16 a[%c2:%c3], %0, %1, a[%c2:%c3]" :: "v"(a), "v"(b), "i"(ACC), "i"(ACC + 15));
}
template <int A, int OFF> DEV void dsw_agpr(u32 ad) {
  asm volatile("ds_write_b128 %0, a[%c1:%c2] offset:%c3" :: "v"(ad), "i"(A), "i"(A + 3), "i"(OFF) : "memory");
}
template <int OFF> DEV void dsw_halves(u32 ad, u32 v) {
  asm volatile("ds_write_b16 %0, %1 offset:%c2\n\tds_write_b16_d16_hi %0, %1 offset:%c3" :: "v"(ad), "v"(v), "i"(OFF), "i"(OFF + 2) : "memory");
}
DEV void ld_flag(u32& d, v4u srd, u32 voff, u32 soff) {
  asm volatile("buffer_load_dword %0, %1, %2, %3 offen sc1" : "=v"(d) : "v"(voff), "s"(srd), "s"(soff) : "memory");
}
DEV void wait0_pin(u32& d) { asm volatile("s_waitcnt vmcnt(0)" : "+v"(d) :: "memory"); }
DEV void pin2(float& a, float& b) { asm volatile("" : "+v"(a), "+v"(b)); }
template <int A> DEV float accrd() { float v; asm volatile("v_accvgpr_read_b32 %0, a%c1" : "=v"(v) : "i"(A)); return v; }
DEV float x_mul(float a, float b) { float d; asm volatile("v_mul_f32 %0, %1, %2" : "=v"(d) : "v"(a), "v"(b)); return d; }
DEV float x_fma(float a, float b, float c) { float d; asm volatile("v_fma_f32 %0, %1, %2, %3" : "=v"(d) : "v"(a), "v"(b), "v"(c)); return d; }
DEV float x_fnma(float a, float b, float c) { float d; asm volatile("v_fma_f32 %0, -%1, %2, %3" : "=v"(d) : "v"(a), "v"(b), "v"(c)); return d; }
DEV float x_exp(float a) { float d; asm volatile("v_exp_f32 %0, %1" : "=v"(d) : "v"(a)); return d; }
DEV float x_rcp(float a) { float d; asm volatile("v_rcp_f32 %0, %1" : "=v"(d) : "v"(a)); return d; }
DEV u32 x_pk(float a, float b) { u32 d; asm volatile("v_cvt_pk_bf16_f32 %0, %1, %2" : "=v"(d) : "v"(a), "v"(b)); return d; }
DEV u32 x_cap(u32 a) { u32 d; asm volatile("v_min_u32 %0, -2, %1" : "=v"(d) : "v"(a)); return d; }
DEV void x_max(u32& m, const v4u& v) { asm volatile("v_max3_u32 %0, %0, %1, %2\n\tv_max3_u32 %0, %0, %3, %4" : "+v"(m) : "v"(v[0]), "v"(v[1]), "v"(v[2]), "v"(v[3])); }
template <int OFF> DEV void dsr128(v4u& d, u32 ad) { asm volatile("ds_read_b128 %0, %1 offset:%c2" : "=v"(d) : "v"(ad), "i"(OFF) : "memory"); }
template <int N> DEV void waitl3(v4u (&a)[3]) { asm volatile("s_waitcnt lgkmcnt(%c3)" : "+v"(a[0]), "+v"(a[1]), "+v"(a[2]) : "i"(N) : "memory"); }
template <int N> DEV void waitv8(v4u (&a)[8]) {
  asm volatile("s_waitcnt vmcnt(%c8)" : "+v"(a[0]), "+v"(a[1]), "+v"(a[2]), "+v"(a[3]), "+v"(a[4]), "+v"(a[5]), "+v"(a[6]), "+v"(a[7]) : "i"(N) : "memory");
}
struct Sw { float dn, p, v; };
#define SW_A "v_rcp_f32 %[rc], %[dn]\n\tv_accvgpr_read_b32 %[ag], a%c[q]\n\tv_accvgpr_read_b32 %[au], a%c[q8]\n\t"
#define SW_B "v_fma_f32 %[ag], -%[m], %[cg], %[ag]\n\tv_fma_f32 %[au], -%[m], %[cu], %[au]\n\tv_mul_f32 %[rc], %[p], %[rc]\n\tv_mul_f32 %[dn], %[ag], %[rl]\n\t"
#define SW_C0 "v_exp_f32 %[dn], %[dn]\n\tv_mul_f32 %[p], %[ag], %[au]\n\tv_mov_b32 %[v], %[rc]\n\tv_fma_f32 %[dn], %[dn], %[s2], %[s2]"
#define SW_C1 "v_exp_f32 %[dn], %[dn]\n\tv_mul_f32 %[p], %[ag], %[au]\n\tv_cvt_pk_bf16_f32 %[o], %[v], %[rc]\n\tv_fma_f32 %[dn], %[dn], %[s2], %[s2]\n\tv_min_u32 %[o], -2, %[o]"
template <bool FIRST, int OFF> DEV void p2_a(const v4u& c0, v4u (&n)[3], const v4u& b, u32 ad) {
#define P2A(Z) asm volatile("s_waitcnt lgkmcnt(3)\n\tv_mfma_f32_32x32x16_bf16 a[192:207], %[w0], %[b], " Z "\n\t" \
    "ds_read_b128 %[n0], %[ad] offset:%c[of]\n\tds_read_b128 %[n1], %[ad] offset:%c[of]+1024\n\tds_read_b128 %[n2], %[ad] offset:%c[of]+2048" \
    : [n0] "=&v"(n[0]), [n1] "=&v"(n[1]), [n2] "=&v"(n[2]) : [w0] "v"(c0), [b] "v"(b), [ad] "v"(ad), [of] "i"(OFF) : "memory")
  if constexpr (FIRST) P2A("0"); else P2A("a[192:207]");
}
template <bool FIRST, bool ODD, int Q>
DEV void p2_b(const v4u& c1, const v4u& c2, const v4u& b, Sw& st, u32& o, float m, float rl, float s2, float cg, float cu) {
  float ag, au, t0;
#define P2B(Z1, Z2, RC, C) asm volatile("v_accvgpr_read_b32 %[ag], a%c[q]\n\tv_accvgpr_read_b32 %[au], a%c[q8]\n\t" \
    "v_mfma_f32_32x32x16_bf16 a[208:223], %[w1], %[b], " Z1 "\n\tv_rcp_f32 " RC ", %[dn]\n\t" \
    "v_fma_f32 %[ag], -%[m], %[cg], %[ag]\n\tv_fma_f32 %[au], -%[m], %[cu], %[au]\n\tv_mul_f32 %[dn], %[ag], %[rl]\n\tv_mul_f32 " RC ", %[p], " RC "\n\t" \
    "v_mfma_f32_32x32x16_bf16 a[224:239], %[w2], %[b], " Z2 "\n\tv_exp_f32 %[dn], %[dn]\n\tv_mul_f32 %[p], %[ag], %[au]\n\t" C \
    : [dn] "+v"(st.dn), [p] "+v"(st.p), [v] "+v"(st.v), [o] "=&v"(o), [t0] "=&v"(t0), [ag] "=&v"(ag), [au] "=&v"(au) \
    : [w1] "v"(c1), [w2] "v"(c2), [b] "v"(b), [m] "v"(m), [rl] "v"(rl), [s2] "v"(s2), [cg] "v"(cg), [cu] "v"(cu), [q] "i"(Q), [q8] "i"(Q + 8))
#define P2_EVEN "v_fma_f32 %[dn], %[dn], %[s2], %[s2]"
#define P2_ODD "v_cvt_pk_bf16_f32 %[o], %[v], %[t0]\n\tv_fma_f32 %[dn], %[dn], %[s2], %[s2]\n\tv_min_u32 %[o], -2, %[o]"
  if constexpr (FIRST) P2B("0", "0", "%[v]", P2_EVEN);
  else if constexpr (ODD) P2B("a[208:223]", "a[224:239]", "%[t0]", P2_ODD);
  else P2B("a[208:223]", "a[224:239]", "%[v]", P2_EVEN);
}
template <int A, int B> DEV void accmov4() {
  asm volatile("v_accvgpr_mov_b32 a%c0, a%c1\n\tv_accvgpr_mov_b32 a%c2, a%c3\n\tv_accvgpr_mov_b32 a%c4, a%c5\n\tv_accvgpr_mov_b32 a%c6, a%c7"
               :: "i"(A), "i"(B), "i"(A + 1), "i"(B + 1), "i"(A + 2), "i"(B + 2), "i"(A + 3), "i"(B + 3));
}
DEV void set_m0(u32 lds) { asm volatile("s_mov_b32 m0, %0" :: "s"(lds) : "memory"); }
DEV void dma16n(v4u srd, u32 voff, u32 soff) { asm volatile("buffer_load_dwordx4 %0, %1, %2 offen sc1 lds" :: "v"(voff), "s"(srd), "s"(soff) : "memory"); }
#define G2_TXT(Z0, Z1, C) \
  "v_mfma_f32_32x32x16_bf16 a[%c[d0]:%c[d0]+15], a[%c[wa]:%c[wa]+3], %[b], " Z0 "\n\t" SW_A SW_B \
  "v_mfma_f32_32x32x16_bf16 a[%c[d0]+32:%c[d0]+47], a[%c[wa]+4:%c[wa]+7], %[b], " Z1 "\n\t" C
#define G2_OPS : [dn] "+v"(st.dn), [p] "+v"(st.p), [v] "+v"(st.v), [o] "=&v"(o), [rc] "=&v"(rc), [ag] "=&v"(ag), [au] "=&v"(au) \
  : [b] "v"(b), [m] "v"(m), [rl] "v"(rl), [s2] "v"(s2), [cg] "v"(cg), [cu] "v"(cu), [q] "i"(Q), [q8] "i"(Q + 8), [d0] "i"(D0), [wa] "i"(WA)
template <bool FIRST, bool ODD, int Q, int D0, int WA>
DEV void g2_pair(const v4u& b, Sw& st, u32& o, float m, float rl, float s2, float cg, float cu) {
  float rc, ag, au;
  if constexpr (FIRST && ODD) asm volatile(G2_TXT("0", "0", SW_C1) G2_OPS);
  else if constexpr (FIRST) asm volatile(G2_TXT("0", "0", SW_C0) G2_OPS);
  else if constexpr (ODD) asm volatile(G2_TXT("a[%c[d0]:%c[d0]+15]", "a[%c[d0]+32:%c[d0]+47]", SW_C1) G2_OPS);
  else asm volatile(G2_TXT("a[%c[d0]:%c[d0]+15]", "a[%c[d0]+32:%c[d0]+47]", SW_C0) G2_OPS);
}
template <int A, int OFF> DEV void dsr_agpr(u32 ad) { asm volatile("ds_read_b128 a[%c1:%c2], %0 offset:%c3" :: "v"(ad), "i"(A), "i"(A + 3), "i"(OFF) : "memory"); }
DEV void mfma16(v4f& c, const v4u& a, const v4u& b) { asm volatile("v_mfma_f32_16x16x32_f16 %0, %1, %2, %0" : "+v"(c) : "v"(a), "v"(b)); }
DEV void mfma16z(v4f& c, const v4u& a, const v4u& b) { asm volatile("v_mfma_f32_16x16x32_f16 %0, %1, %2, 0" : "=v"(c) : "v"(a), "v"(b)); }
constexpr int A_W = 0, A_ACC1 = 128, A_ACC2 = 192;

constexpr int TCOL = 528, NCOL = 63, TAB_B = NCOL * TCOL;
constexpr int KC_B = 4640, UC_B = 4224;
constexpr int OFF_K = TAB_B, OFF_U = OFF_K + 4 * KC_B, OFF_X = OFF_U + 2 * UC_B, CH_B = OFF_X + 4096;
constexpr int L_RT1 = 2 * CH_B, L_RT2 = L_RT1 + 128 * 144, L_MISC = L_RT2 + 64 * 144;
constexpr int L_W2 = 131072;
constexpr int L_CSP = L_MISC, L_CS = L_CSP + 4096, L_MR = L_CS + 1024, LDS_B = L_MR + 2048;
static_assert(L_W2 >= L_RT1 && L_W2 + 24576 <= L_MISC && LDS_B <= 163840 && L_MISC % 16 == 0, "lds map");

typedef v2f cf;
DEV void cmul2(cf& r0, cf a0, cf b0, cf& r1, cf a1, cf b1) {
  asm("v_pk_mul_f32 %0, %2, %3 op_sel_hi:[0,1]\n\t"
      "v_pk_mul_f32 %1, %4, %5 op_sel_hi:[0,1]\n\t"
      "v_pk_fma_f32 %0, %2, %3, %0 op_sel:[1,1,0] op_sel_hi:[1,0,1] neg_lo:[1,0,0]\n\t"
      "v_pk_fma_f32 %1, %4, %5, %1 op_sel:[1,1,0] op_sel_hi:[1,0,1] neg_lo:[1,0,0]"
      : "=&v"(r0), "=&v"(r1) : "v"(a0), "v"(b0), "v"(a1), "v"(b1));
}
DEV cf cexp_p(float ar, float ai, float p) {
  const float e = __builtin_amdgcn_exp2f(ar * p * 1.44269504088896340736f);
  const float xx = ai * p;
  const float chi = 0.15915493667125702f, clo = 6.4206383e-09f;
  const float th = xx * chi;
  float tl = __builtin_fmaf(xx, chi, -th);
  tl = __builtin_fmaf(xx, clo, tl);
  const float fr = (th - __builtin_rintf(th)) + tl;
  return cf{e * __builtin_amdgcn_cosf(fr), e * __builtin_amdgcn_sinf(fr)};
}
template <int CTRL> DEV u32 dppu(u32 v) { return (u32)__builtin_amdgcn_update_dpp(0, (int)v, CTRL, 0xf, 0xf, true); }
DEV u32 wave_umax(u32 v) {
  v = max(v, dppu<0xB1>(v)); v = max(v, dppu<0x4E>(v)); v = max(v, dppu<0x141>(v)); v = max(v, dppu<0x140>(v));
  auto r = __builtin_amdgcn_permlane16_swap(v, v, false, false); v = max(r[0], r[1]);
  auto q = __builtin_amdgcn_permlane32_swap(v, v, false, false); return max(q[0], q[1]);
}

struct ConvSched { int nm, ma[18], mq[18], mk[18]; int nr, ru[16], ri[16]; };
constexpr ConvSched mk_sched(int c0, int c1, int c2, int c3) {
  ConvSched s{};
  const int c[4] = {c0, c1, c2, c3};
  bool hu[8] = {}, hk[8] = {};
  for (int q = 0; q <= c3; ++q)
    for (int a = 3; a >= 0; --a)
      if (q <= c[a]) {
        const int k = c[a] - q;
        s.ma[s.nm] = a; s.mq[s.nm] = q; s.mk[s.nm] = k; ++s.nm;
        if (!hu[q]) { hu[q] = true; s.ru[s.nr] = 1; s.ri[s.nr] = q; ++s.nr; }
        if (!hk[k]) { hk[k] = true; s.ru[s.nr] = 0; s.ri[s.nr] = k; ++s.nr; }
      }
  return s;
}
template <int V> struct CS { static constexpr ConvSched s = V == 0 ? mk_sched(0, 3, 4, 7) : mk_sched(1, 2, 5, 6); };

template <int V, class Ex> DEV void conv_main(LAS char* ub, LAS char* kb, v4f (&acc)[4], Ex&& extra) {
  constexpr ConvSched S = CS<V>::s;
  v4u U[2][8], K[2][8];
  sfor<0, S.nr>([&](auto i) {
    if constexpr (S.ru[i]) U[0][S.ri[i]] = *(LAS v4u*)(ub + 512 * S.ri[i]);
    else K[0][S.ri[i]] = *(LAS v4u*)(kb - 512 * S.ri[i]);
  });
  SB();
  sfor<0, 8>([&](auto r) {
    constexpr int cur = r & 1, nxt = cur ^ 1;
    sfor<0, 18>([&](auto m) {
      if constexpr (r == 0 && S.mq[m] == 0) mfma16z(acc[S.ma[m]], U[cur][S.mq[m]], K[cur][S.mk[m]]);
      else mfma16(acc[S.ma[m]], U[cur][S.mq[m]], K[cur][S.mk[m]]);
      if constexpr (r < 7 && m < S.nr) {
        if constexpr (S.ru[m]) U[nxt][S.ri[m]] = *(LAS v4u*)(ub + 64 * (r + 1) + 512 * S.ri[m]);
        else K[nxt][S.ri[m]] = *(LAS v4u*)(kb + 64 * (r + 1) - 512 * S.ri[m]);
      }
      extra(r, m);
      SB();
    });
  });
  asm volatile("s_nop 7" : "+v"(acc[0]), "+v"(acc[1]), "+v"(acc[2]), "+v"(acc[3]));
}

__global__ __launch_bounds__(256, 1) void k_fused(const u16* __restrict__ x, const u16* __restrict__ lar_, const u16* __restrict__ aim_,
                                                  const u16* __restrict__ cre_, const u16* __restrict__ cim_, const u16* __restrict__ ldt_,
                                                  const u16* __restrict__ dsk_, const u16* __restrict__ wg, const u16* __restrict__ wu,
                                                  const u16* __restrict__ wd, char* __restrict__ ws, u16* __restrict__ out) {
  extern __shared__ __attribute__((aligned(16))) char smx[];
  LAS char* sm = (LAS char*)smx;
  asm volatile("" ::: "a0", "a255");
  const int t = threadIdx.x, lane = t & 63, w = __builtin_amdgcn_readfirstlane(t >> 6), b = blockIdx.x;
  STAMP(0);
  const int xcd = b & 7, bi = b >> 3;
  const auto rs0 = __builtin_amdgcn_make_buffer_rsrc(ws, 0, WS_FIX, 0x00020000);
  const v4u srd0 = {(u32)(uintptr_t)ws, (u32)((uintptr_t)ws >> 32) & 0xffffu, WS_FIX, 0x00020000u};
  const int pr = xcd * 32 + bi;
  const int cw = w >> 1, nh = w & 1, ch = 2 * pr + cw;
  const int mt = 4 * (xcd >> 1) + (bi & 3), j = 8 * (xcd & 1) + (bi >> 2);
  const int e2 = j & 1, dg = j >> 1;
  const int G = lane >> 4, q4 = (lane >> 2) & 3, p4 = lane & 3, i16 = lane & 15;
  const u32 lane16 = lane * 16;
  LAS char* cb = sm + cw * CH_B;

  v4u xa[2];
#pragma unroll
  for (int i = 0; i < 2; ++i) xa[i] = *(const v4u*)(x + (size_t)(8 * b + 2 * w + i) * D + lane * 8);
  u32 xq[4][2];
#pragma unroll
  for (int i = 0; i < 4; ++i)
#pragma unroll
    for (int k = 0; k < 2; ++k) xq[i][k] = *(const u32*)(x + (size_t)(8 * b + 2 * (lane & 3) + k) * D + 2 * (64 * w + 16 * i + (lane >> 2)));
  const u32 ep = __builtin_amdgcn_raw_buffer_load_b32(rs0, WS_EPOCH, 0, 16);
  const int n = 64 * nh + lane;
  const u32 r_lar = lar_[ch * NS + n], r_aim = aim_[ch * NS + n], r_cre = cre_[ch * NS + n], r_cim = cim_[ch * NS + n];
  const u32 r_cre2 = cre_[ch * NS + (n ^ 64)], r_cim2 = cim_[ch * NS + (n ^ 64)];
  const u32 r_ldt = ldt_[ch], r_dsk = dsk_[ch];
  SB();
  {
    const v4u s = {~0u, ~0u, ~0u, ~0u};
#pragma unroll
    for (int i = 0; i < 2; ++i) __builtin_amdgcn_raw_buffer_store_b128(s, rs0, t * 16 + i * 4096, WS_HT + pr * 8192, 16);
  }
  SB();
  const int rb = b < 224 ? b : 223;
  const int mat = rb / 112, cg = (rb % 112) % 28, rq = (rb % 112) / 28, dgp = rb & 7, fb = rb >> 3;
  v4u wa[4], wb[2];
  {
    const u16* W = mat ? wu : wg;
#pragma unroll
    for (int i = 0; i < 4; ++i) wa[i] = *(const v4u*)(W + (size_t)(128 * rq + (t >> 3) + 32 * i) * F + 64 * cg + (t & 7) * 8);
#pragma unroll
    for (int i = 0; i < 2; ++i) wb[i] = *(const v4u*)(wd + (size_t)(64 * fb + (t >> 3) + 32 * i) * D + 64 * dgp + (t & 7) * 8);
  }
  u32 pfv;
  {
    u64 pc; u32 len;
    asm volatile("s_getpc_b64 %0\n.Lkf_here:\n\ts_mov_b32 %1, .Lkf_end-.Lkf_here" : "=s"(pc), "=s"(len));
    const u32 off = min((u32)((bi * 32 + (t & 31)) * 64), len);
    asm volatile("global_load_dword %0, %1, %2" : "=v"(pfv) : "v"(off), "s"(pc));
  }
  SB();
  {
    const v4u z = {0, 0, 0, 0};
    const int tt = t & 127;
    LAS char* c2 = sm + (t >> 7) * CH_B;
    *(LAS v4u*)(c2 + OFF_K + (tt / 33) * KC_B + (255 + tt % 33) * 16) = z;
    if (tt < 4) *(LAS v4u*)(c2 + OFF_K + 3 * KC_B + (255 + 29 + tt) * 16) = z;
    if (tt < 16) *(LAS v4u*)(c2 + OFF_U + (tt >> 3) * UC_B + (tt & 7) * 16) = z;
  }
  const u32 tag = __builtin_amdgcn_readfirstlane(ep) + 1u;
  char* const wr = ws + WS_ROT + (size_t)(tag & 7u) * WS_USED;
  const auto rs = __builtin_amdgcn_make_buffer_rsrc(wr, 0, WS_USED, 0x00020000);
  const v4u srd = {(u32)(uintptr_t)wr, (u32)((uintptr_t)wr >> 32) & 0xffffu, WS_USED, 0x00020000u};
#pragma unroll
  for (int i = 0; i < 4; ++i)
    __builtin_amdgcn_raw_buffer_store_b128(v4u{xq[i][0], tag, xq[i][1], tag}, rs, (16 * i + (lane >> 2)) * 16384 + (lane & 3) * 16, WS_XG + w * (64 * 16384) + b * 64, 16);
  SB();
  {
    float v[2][8], s[2], q[2];
#pragma unroll
    for (int i = 0; i < 2; ++i) {
#pragma unroll
      for (int k = 0; k < 4; ++k) { v[i][2 * k] = bflo(xa[i][k]); v[i][2 * k + 1] = bfhi(xa[i][k]); }
      s[i] = ((v[i][0] + v[i][1]) + (v[i][2] + v[i][3])) + ((v[i][4] + v[i][5]) + (v[i][6] + v[i][7]));
    }
#pragma unroll
    for (int i = 0; i < 2; ++i) s[i] = wave_sum(s[i]) * (1.0f / D);
#pragma unroll
    for (int i = 0; i < 2; ++i) {
      q[i] = 0;
#pragma unroll
      for (int k = 0; k < 8; ++k) { const float d = v[i][k] - s[i]; q[i] += d * d; }
    }
#pragma unroll
    for (int i = 0; i < 2; ++i) q[i] = wave_sum(q[i]) * (1.0f / D);
    const float m = (lane & 1) ? s[1] : s[0], vv = (lane & 1) ? q[1] : q[0];
    const u32 pk = pkbf(m, __builtin_sqrtf(rbf(rbf(vv) + 1e-5f)));
    SB();
    WAITV(11);
    if (lane < 16) __builtin_amdgcn_raw_buffer_store_b64(v2u{pk, tag}, rs, (lane >> 1) * 16384 + (lane & 1) * 8, WS_GRAN + (8 * b + 2 * w) * 8, 16);
    SB();
  }
  STAMP(1);
  const float dt = rbf(expf(bf2f(r_ldt)));
  const float ar = -rbf(expf(bf2f(r_lar))) * dt, ai = bf2f(r_aim) * dt;
  int ex = (int)(wave_umax(max(max(r_cre & 0x7fffu, r_cim & 0x7fffu), max(r_cre2 & 0x7fffu, r_cim2 & 0x7fffu))) >> 7);
  ex = ex < 8 ? 8 : (ex > 250 ? 250 : ex);
  const float scl = __uint_as_float((u32)(257 - ex) << 23);
  const float yscl = 2.0f * dt * __uint_as_float((u32)(ex - 3) << 23);
  {
    LAS char* tb = cb + n * 4;
#define PUT(col, v) *(LAS u32*)(tb + (col) * TCOL) = pkh((v)[0], (v)[1])
    const v4u sent = {~0u, ~0u, ~0u, ~0u};
#define SENT(i) do { SB(); __builtin_amdgcn_raw_buffer_store_b128(sent, rs, lane16 + ((i) & 3) * 1024, WS_ACT + ((mt * 4 + w) * 112 + j * 7 + ((i) & 4)) * 1024, 16); SB(); } while (0)
    cf zp[17], zb[8], a[16], pa, pb;
    zp[1] = cexp_p(ar, ai, 1.0f); zp[16] = cexp_p(ar, ai, 16.0f); zb[1] = cexp_p(ar, ai, 256.0f);
    const cf wc = {zp[16][0], -zp[16][1]};
    a[0] = cf{bf2f(r_cre) * scl, -bf2f(r_cim) * scl};
    *(LAS u32*)(tb + 16 * TCOL) = 0x00003c00u;
    PUT(17, zp[1]); PUT(32, zb[1]); PUT(0, a[0]);
    SENT(0);
    cmul2(zp[2], zp[1], zp[1], zb[2], zb[1], zb[1]);
    cmul2(zp[3], zp[2], zp[1], zp[4], zp[2], zp[2]);
    cmul2(zb[3], zb[2], zb[1], zb[4], zb[2], zb[2]);
    cmul2(a[1], a[0], wc, zp[8], zp[4], zp[4]);
    PUT(18, zp[2]); PUT(19, zp[3]); PUT(20, zp[4]); PUT(41, zb[2]); PUT(46, zb[3]); PUT(51, zb[4]);
    SENT(1);
    cmul2(a[2], a[1], wc, zp[5], zp[4], zp[1]);
    cmul2(a[3], a[2], wc, zp[6], zp[4], zp[2]);
    cmul2(a[4], a[3], wc, zp[7], zp[4], zp[3]);
    PUT(1, a[1]); PUT(2, a[2]); PUT(3, a[3]); PUT(4, a[4]); PUT(21, zp[5]); PUT(22, zp[6]); PUT(23, zp[7]); PUT(24, zp[8]);
    SENT(2);
    sfor<9, 16>([&](auto i) { cmul2(a[i - 4], a[i - 5], wc, zp[i], zp[8], zp[i - 8]); PUT(i - 4, a[i - 4]); PUT(16 + i, zp[i]); });
    SENT(3);
    cmul2(a[12], a[11], wc, zb[5], zb[4], zb[1]);
    cmul2(a[13], a[12], wc, zb[6], zb[4], zb[2]);
    cmul2(a[14], a[13], wc, zb[7], zb[4], zb[3]);
    cmul2(a[15], a[14], wc, pa, zb[1], zp[16]);
    PUT(12, a[12]); PUT(13, a[13]); PUT(14, a[14]); PUT(15, a[15]); PUT(54, zb[5]); PUT(57, zb[6]); PUT(60, zb[7]); PUT(40, pa);
    SENT(4);
    sfor<0, 3>([&](auto i) { cmul2(pa, zb[1], zp[4 * i + 2], pb, zb[1], zp[4 * i + 4]); PUT(33 + 2 * i, pa); PUT(34 + 2 * i, pb); });
    cmul2(pa, zb[1], zp[14], pb, zb[1], zp[14]); PUT(39, pa);
    SENT(5);
    sfor<1, 5>([&](auto i) { cmul2(pa, zb[2], zp[4 * i], pb, zb[3], zp[4 * i]); PUT(41 + i, pa); PUT(46 + i, pb); });
    SENT(6);
    sfor<4, 8>([&](auto m) { cmul2(pa, zb[m], zp[8], pb, zb[m], zp[16]); PUT(52 + 3 * (m - 4), pa); PUT(53 + 3 * (m - 4), pb); });
#undef PUT
#undef SENT
  }
  STAMP(2);
  SB();
#pragma unroll
  for (int i = 0; i < 4; ++i) *(LAS v4u*)(sm + L_RT1 + ((t >> 3) + 32 * i) * 144 + (t & 7) * 16) = wa[i];
#pragma unroll
  for (int i = 0; i < 2; ++i) *(LAS v4u*)(sm + L_RT2 + ((t >> 3) + 32 * i) * 144 + (t & 7) * 16) = wb[i];
  BAR();
  STAMP(3);
  if (b < 224) {
    const int g16 = 4 * cg + w, jj = g16 / 7, pt = g16 % 7, h = G & 1;
    float cs = 0;
#pragma unroll
    for (int it = 0; it < 4; ++it) {
      const int ksl = 2 * it + (G >> 1), ks = 8 * rq + ksl;
      const v4u o = trfrag(sm + L_RT1 + (16 * ksl + 8 * h + q4) * 144 + 32 * w + 8 * p4, 4 * 144);
#pragma unroll
      for (int e = 0; e < 4; ++e) cs = dot2bf(o[e], 0x3f803f80u, cs);
      const int fr = pt < 4 ? pt * 32 + ks : 128 + ks * 3 + (pt - 4);
      __builtin_amdgcn_raw_buffer_store_b128(o, rs, fr * 1024 + (32 * h + 16 * mat + i16) * 16, WS_WT1 + jj * 229376, 16);
    }
    cs = sum32x(sum16x(cs));
    if (lane < 16) __builtin_amdgcn_raw_buffer_store_b32(__float_as_uint(cs), rs, lane * 4, WS_CSUM + jj * 4096 + (rq * 224 + mat * 112 + pt * 16) * 4, 16);
#pragma unroll
    for (int it = 0; it < 2; ++it) {
      const int sl = 2 * it + (G >> 1), s = 4 * fb + sl, jp = s / 7, pp = s % 7;
      const v4u o = trfrag(sm + L_RT2 + (16 * sl + 4 * h + q4) * 144 + 32 * w + 8 * p4, 8 * 144);
      const int pos = pp < 4 ? (pp >> 1) * 8 + (jp & 3) * 2 + (pp & 1) : 16 + (jp & 3) * 3 + (pp - 4);
      const int xrow = (i16 & 3) + 8 * (i16 >> 2) + 4 * (w & 1);
      __builtin_amdgcn_raw_buffer_store_b128(o, rs, (((jp >> 2) * 28 + pos) * 2 + (w >> 1)) * 1024 + (32 * h + xrow) * 16, WS_WT2 + dgp * 229376, 16);
    }
  }
  SB();
  STAMP(18);
  {
    v4u Bf[8], Af[2][8];
    const int c2 = (i16 + ((i16 >> 1) & 1)) >> 1, c4 = (i16 + 1 + ((i16 >> 2) & 1)) >> 2, c8 = (i16 + 3 + ((i16 >> 3) & 1)) >> 3;
    LAS char* ap[4];
    ap[0] = cb + (nh ? 32 + c2 : 16 + i16) * TCOL + G * 16;
    ap[1] = cb + (41 + 5 * nh + c4) * TCOL + G * 16;
    ap[2] = cb + (51 + 3 * nh + c8) * TCOL + G * 16;
    ap[3] = ap[2] + 6 * TCOL;
#pragma unroll
    for (int ks = 0; ks < 8; ++ks) { Bf[ks] = *(LAS v4u*)(cb + i16 * TCOL + ks * 64 + G * 16); Af[0][ks] = *(LAS v4u*)(ap[0] + ks * 64); }
    SB();
    v4f d[4];
    sfor<0, 4>([&](auto mm) {
      sfor<0, 8>([&](auto ks) {
        if constexpr (ks == 0) mfma16z(d[mm], Af[mm & 1][ks], Bf[ks]);
        else mfma16(d[mm], Af[mm & 1][ks], Bf[ks]);
        if constexpr (mm < 3) Af[(mm & 1) ^ 1][ks] = *(LAS v4u*)(ap[mm + 1] + ks * 64);
        SB();
      });
    });
    asm volatile("s_nop 7" : "+v"(d[0]), "+v"(d[1]), "+v"(d[2]), "+v"(d[3]));
    LAS char* kp = cb + OFF_K + 16 + (2038 - 256 * nh - 16 * i16 - 4 * G - 3 * 512) * 2;
#pragma unroll
    for (int mm = 0; mm < 4; ++mm) {
      const u32 p0 = pkh(d[mm][3], d[mm][2]), p1 = pkh(d[mm][1], d[mm][0]);
#pragma unroll
      for (int e = 0; e < 4; ++e) {
        LAS char* k2 = kp + (3 - mm) * 1024 + e * (KC_B + 4);
        if (e & 1) *(LAS v2u*)k2 = v2u{p0, p1};
        else { *(LAS u32*)k2 = p0; *(LAS u32*)(k2 + 4) = p1; }
      }
    }
  }
  WAITV(0);
  asm volatile("" :: "v"(pfv));
  if (lane == 0 && b < 224) __builtin_amdgcn_raw_buffer_store_b32(tag, rs, 0, WS_WFLAG + (b * 4 + w) * 16, 16);
  STAMP(4);
  {
    v4u g[4], gx[4];
    for (int spin = 0; spin < SPIN_CAP; ++spin) {
      asm volatile("" ::: "memory");
#pragma unroll
      for (int i = 0; i < 4; ++i) gx[i] = __builtin_amdgcn_raw_buffer_load_b128(rs, t * 16 + i * 4096, WS_XG + pr * 16384, 16);
#pragma unroll
      for (int i = 0; i < 4; ++i) g[i] = __builtin_amdgcn_raw_buffer_load_b128(rs, t * 16 + i * 4096, WS_GRAN + xcd * 16384, 16);
      u32 df = 0;
#pragma unroll
      for (int i = 0; i < 4; ++i) df |= ((g[i][1] ^ tag) | (g[i][3] ^ tag)) | ((gx[i][1] ^ tag) | (gx[i][3] ^ tag));
      if (__all(df == 0)) break;
      COUNT(0);
    }
    STAMP(5);
    LAS char* cc[2] = {sm + t * 4, sm + CH_B + t * 4};
    asm volatile("" : "+v"(cc[1]));
    const u32 ub0 = t * 4, ub1 = CH_B + t * 4;
    sfor<0, 4>([&](auto i) {
      const float m0 = bflo(g[i][0]), m1 = bflo(g[i][2]);
      const float r0 = __builtin_amdgcn_rcpf(bfhi(g[i][0])), r1 = __builtin_amdgcn_rcpf(bfhi(g[i][2]));
#pragma unroll
      for (int c = 0; c < 2; ++c) {
        const u32 d = pkbf((c ? bfhi(gx[i][0]) : bflo(gx[i][0])) - m0, (c ? bfhi(gx[i][2]) : bflo(gx[i][2])) - m1);
        const u32 o = pkbf(bflo(d) * r0, bfhi(d) * r1);
        const u32 uh = pkh(bflo(o), bfhi(o));
        LAS char* c2 = cc[c] + i * 1024;
        *(LAS u32*)(c2 + OFF_U + UC_B + 112) = uh;
        dsw_halves<OFF_U + 114 + i * 1024>(c ? ub1 : ub0, uh);
        *(LAS u32*)(c2 + OFF_X) = __builtin_amdgcn_perm(gx[i][2], gx[i][0], c ? 0x07060302u : 0x05040100u);
      }
    });
  }
  BAR();
  if (b == 0 && t == 0) __builtin_amdgcn_raw_buffer_store_b32(tag, rs0, 0, WS_EPOCH, 16);
  STAMP(6);
  v4f acc[4];
  {
    u32 fo;
    {
      int g16, pw;
      if (lane < 8) g16 = 7 * j + w; else g16 = 7 * j + 4 + ((lane - 8) >> 3);
      pw = ((lane & 1) * 112 + ((lane >> 1) & 3) * 28 + (g16 >> 2)) * 4 + (g16 & 3);
      if (lane >= 32) { const int k = lane < 60 ? lane - 32 : 0; pw = ((7 * w + (k >> 2)) * 8 + dg) * 4 + (k & 3); }
      fo = pw * 16;
    }
    const u32 sW1 = WS_WT1 + j * 229376 + w * 32768, sW2 = WS_WT1 + j * 229376 + 131072;
    u32 fl;
    auto extra = [&](auto r, auto m) {
      if constexpr (r == 0 && m == 0) ld_flag(fl, srd, fo, WS_WFLAG);
      if constexpr (r == 1 && m == 5) {
        STAMP(16);
        wait0_pin(fl);
        for (int spin = 0; spin < SPIN_CAP && !__all(fl == tag); ++spin) { ld_flag(fl, srd, fo, WS_WFLAG); wait0_pin(fl); COUNT(1); }
        STAMP(17);
      }
      if constexpr (r * 18 + m >= 24 && (r * 18 + m) % 3 == 0) {
        constexpr int k = (r * 18 + m) / 3 - 8;
        if constexpr (k < 32) ld_agpr<A_W + 4 * k, (k & 3) * 1024>(srd, lane16, sW1 + (k >> 2) * 4096);
        else if constexpr (k < 38) dma16(srd, lane16, sW2 + (w * 6 + k - 32) * 1024, L_W2 + (w * 6 + k - 32) * 1024);
      }
    };
    LAS char* ub = cb + OFF_U + (i16 & 1) * UC_B + ((i16 >> 1) + G) * 16;
    LAS char* kb = cb + OFF_K + (i16 & 3) * KC_B + 16 + (248 - 8 * (i16 >> 2) + G) * 16;
    if (nh == 0) conv_main<0>(ub, kb, acc, extra);
    else conv_main<1>(ub, kb, acc, extra);
  }
  STAMP(7);
  {
    const float dsk = bf2f(r_dsk);
#pragma unroll
    for (int a = 0; a < 4; ++a) {
#pragma unroll
      for (int hh = 0; hh < 2; ++hh) {
        const int l = 256 * (4 * (a >> 1) + ((a & 1) ? 3 - nh : nh)) + 8 * (2 * G + hh) + 2 * (i16 & 3) + 64 * (i16 >> 2);
        const v2h up = *(LAS v2h*)(cb + OFF_U + UC_B + (l + 56) * 2);
        LAS u32* xp = (LAS u32*)(cb + OFF_X + l * 2);
        const u32 xv = *xp;
        const u32 yb = pkbf(acc[a][2 * hh] * yscl, acc[a][2 * hh + 1] * yscl);
        const u32 sk = pkbf((float)up[0] * dsk, (float)up[1] * dsk);
        const u32 zz = pkbf(bflo(yb) + bflo(sk), bfhi(yb) + bfhi(sk));
        *xp = min(pkbf(bflo(xv) + bflo(zz), bfhi(xv) + bfhi(zz)), 0xfffffffeu);
      }
    }
    WAITL0();
#pragma unroll
    for (int i = 0; i < 2; ++i) {
      const int c = 4 * i + ((lane >> 5) ? 3 - nh : nh);
      const int l = 256 * c + (lane & 31) * 8;
      __builtin_amdgcn_raw_buffer_store_b128(*(LAS v4u*)(cb + OFF_X + l * 2), rs0, l * 2, WS_HT + ch * 4096, 16);
    }
  }
  const u32 sWD = WS_WT2 + dg * 229376 + w * 57344;
  sfor<0, 12>([&](auto f) { ld_agpr<A_ACC2 + 4 * f, (f & 3) * 1024>(srd, lane16, sWD + (e2 * 16 + (f & 12)) * 1024); });
  sfor<0, 4>([&](auto f) { ld_agpr<240 + 4 * f, (f & 1) * 1024>(srd, lane16, sWD + 32768 + (f >> 1) * 6144); });
  BAR();
  STAMP(8);
  const u32 vtok = (lane >> 4) * 4096 + (((lane & 15) ^ (4 * (lane >> 4))) * 16);
  const u32 stok = WS_HT + mt * 256 + w * 65536;
  auto tok_dma = [&](int c) {
#pragma unroll
    for (int i = 0; i < 4; ++i) dma16(srd0, vtok, stok + (64 * c + 4 * i) * 4096, c * 16384 + (4 * w + i) * 1024);
  };
  dma16(srd, lane16, WS_CSUM + j * 4096 + w * 1024, L_CSP + w * 1024);
#pragma unroll
  for (int c = 0; c < 3; ++c) tok_dma(c);
  LAS char* tp[4][2];
#pragma unroll
  for (int i = 0; i < 4; ++i) {
    tp[i][0] = sm + (8 * (G >> 1) + q4) * 256 + 8 * (p4 & 1) + (((4 * (w ^ i) + 2 * (G & 1) + (p4 >> 1)) ^ (4 * q4)) * 16);
    tp[i][1] = tp[i][0] + 65536;
    asm volatile("" : "+v"(tp[i][1]));
  }
  LAS char* vp[2];
  vp[0] = sm + w * 4096 + lane16; vp[1] = vp[0] + 65536;
  asm volatile("" : "+v"(vp[1]));
  v4u own[32], vq[4];
  u32 vm = 0;
  float sA = 0, sB = 0, qA = 0, qB = 0;
  auto mx = [&](u32 m, const v4u& v) { return max(max(m, max(v[0], v[1])), max(v[2], v[3])); };
  auto own_rd = [&](auto s) { own[s] = trfrag(tp[0][s >> 4] + (s & 15) * 4096, 1024); };
  auto own_acc = [&](auto s) {
#pragma unroll
    for (int e = 0; e < 4; e += 2) {
      sA = dot2bf(own[s][e], 0x3f803f80u, sA); qA = dot2bf(own[s][e], own[s][e], qA);
      sB = dot2bf(own[s][e + 1], 0x3f803f80u, sB); qB = dot2bf(own[s][e + 1], own[s][e + 1], qB);
    }
    pin2(sA, qA); pin2(sB, qB);
  };
  auto v_rd = [&](auto c, auto i) { vq[i] = *(LAS v4u*)(vp[c >> 2] + (c & 3) * 16384 + i * 1024); };
  auto fix = [&](auto c0, auto c1) {
    for (int spin = 0; spin < SPIN_CAP && __any(vm == ~0u); ++spin) {
      COUNT(2 + (c0 + 2) / 4);
#pragma unroll
      for (int c = c0; c < c1; ++c) tok_dma(c);
      WAITV(0);
      vm = 0;
      sfor<c0, c1>([&](auto c) {
        sfor<0, 4>([&](auto i) { v_rd(c, i); });
        sfor<0, 4>([&](auto i) { vm = mx(vm, vq[i]); });
      });
    }
    vm = 0;
  };
  STAMP(52);
  WAITV(8);
  STAMP(53);
  sfor<0, 4>([&](auto i) { v_rd(IC<0>{}, i); });
  sfor<0, 4>([&](auto i) { vm = mx(vm, vq[i]); });
  SB();
  WAITV(4);
  sfor<0, 4>([&](auto i) { v_rd(IC<1>{}, i); });
  sfor<0, 4>([&](auto i) { vm = mx(vm, vq[i]); });
  fix(IC<0>{}, IC<2>{});
  BAR();
  STAMP(9);
  const u32 sW2 = WS_WT1 + j * 229376 + 131072;
  auto w2_lds = [&](int k) { return (k / 3) * 16384 + (3 * w + k % 3) * 1024; };
  auto w2_src = [&](int k) { return sW2 + (2 + k / 3) * 12288 + (3 * w + k % 3) * 1024; };
  auto ld_wd = [&](auto f) { ld_agpr<A_W + 4 * f, (f & 3) * 1024>(srd, lane16, sWD + ((f < 16 ? e2 : 1 - e2) * 16 + (f & 12)) * 1024); };
  v4u hres[2];
  {
    v4u ot[2][3];
    LAS char* hp = sm + (64 * dg + 32 * (w >> 1) + 16 * (G >> 1) + q4) * 256 + 8 * (p4 & 1) +
                   (((4 * (2 * e2 + (w & 1)) + 2 * (G & 1) + (p4 >> 1)) ^ (4 * q4)) * 16);
    auto ot_rd = [&](auto s, auto i) { ot[s & 1][i] = trfrag(tp[i + 1][s >> 4] + (s & 15) * 4096, 1024); };
    own_rd(IC<0>{});
    sfor<0, 3>([&](auto i) { ot_rd(IC<0>{}, i); });
    if (t < 224) {
      LAS float* cp = (LAS float*)(sm + L_CSP) + t;
      *(LAS float*)(sm + L_CS + t * 4) = (cp[0] + cp[224]) + (cp[448] + cp[672]);
    }
    SB();
    sfor<0, 32>([&](auto s) {
      constexpr int vc = s == 2 ? 2 : s == 5 ? 3 : s == 9 ? 4 : s == 12 ? 5 : s == 17 ? 6 : s == 19 ? 7 : -1;
      constexpr int vy = s < 9 ? 3 : s < 17 ? 4 : 8;
      constexpr bool vmx = s == 3 || s == 6 || s == 10 || s == 13 || s == 18 || s == 20;
      constexpr bool two = (s >= 15 && s < 19) || (s >= 23 && s < 27);
      constexpr int o0 = s < 15 ? s + 1 : two ? (s < 19 ? 16 + 2 * (s - 15) : 24 + 2 * (s - 23)) : -1, o1 = two ? o0 + 1 : -1;
      constexpr int p0 = s < 15 ? (s / 3) * 4 + (s % 3 == 0 ? 0 : s % 3 + 1) : -1, p1 = (s < 15 && s % 3 == 0) ? p0 + 1 : -1;
      if constexpr (s == 16 || s == 20 || s == 23 || s == 24 || s == 27) STAMP(40 + s - 16);
      if constexpr (s == 5) sfor<0, 6>([&](auto k) { own_acc(k); });
      if constexpr (s == 12) sfor<6, 13>([&](auto k) { own_acc(k); });
      if constexpr (s == 20) sfor<13, 24>([&](auto k) { own_acc(k); });
      if constexpr (s == 28) sfor<24, 32>([&](auto k) { own_acc(k); });
      if constexpr (s == 20) STAMP(45);
      if constexpr (vc >= 0) WAITV(vy);
      if constexpr (p0 >= 0) set_m0((3 + p0 / 4) * 16384 + (4 * w + p0 % 4) * 1024);
      if constexpr (s >= 15) set_m0(w2_lds(s - 15));
      mfma_aav<A_ACC1, A_W + 4 * s, s == 0>(own[s]);
      if constexpr (s < 31) ot_rd(IC<s + 1>{}, IC<0>{});
      if constexpr (p0 >= 0) dma16n(srd0, vtok, stok + (64 * (3 + p0 / 4) + 4 * (p0 % 4)) * 4096);
      if constexpr (s >= 15) dma16n(srd, lane16, w2_src(s - 15));
      if constexpr (vmx) { vm = mx(vm, vq[0]); vm = mx(vm, vq[1]); }
      SB();
      mfma_aav<A_ACC1 + 16, A_W + 4 * s, s == 0>(ot[s & 1][0]);
      if constexpr (s < 31) ot_rd(IC<s + 1>{}, IC<1>{});
      if constexpr (o1 >= 0) own_rd(IC<o1>{});
      if constexpr (vmx) { vm = mx(vm, vq[2]); vm = mx(vm, vq[3]); }
      if constexpr (s >= 12 && s < 24) accmov4<A_W + 4 * (s - 12), A_ACC2 + 4 * (s - 12)>();
      if constexpr (p1 >= 0) set_m0((3 + p1 / 4) * 16384 + (4 * w + p1 % 4) * 1024);
      SB();
      mfma_aav<A_ACC1 + 32, A_W + 4 * s, s == 0>(ot[s & 1][1]);
      if constexpr (s < 31) ot_rd(IC<s + 1>{}, IC<2>{});
      if constexpr (p1 >= 0) dma16n(srd0, vtok, stok + (64 * (3 + p1 / 4) + 4 * (p1 % 4)) * 4096);
      if constexpr (s >= 15) ld_wd(IC<s - 3>{});
      if constexpr (vc >= 0) { v_rd(IC<vc>{}, IC<0>{}); v_rd(IC<vc>{}, IC<1>{}); }
      SB();
      mfma_aav<A_ACC1 + 48, A_W + 4 * s, s == 0>(ot[s & 1][2]);
      if constexpr (o0 >= 0) own_rd(IC<o0>{});
      if constexpr (vc >= 0) { v_rd(IC<vc>{}, IC<2>{}); v_rd(IC<vc>{}, IC<3>{}); }
      if constexpr (s == 1 || s == 7 || s == 16 || s == 23) {
        if ((dg >> 1) == (s + 1) / 8) { hres[0] = trfrag(hp, 1024); hres[1] = trfrag(hp + 2048, 1024); }
      }
      SB();
      if constexpr (s == 6) { fix(IC<2>{}, IC<4>{}); BAR(); STAMP(19); SB(); }
      if constexpr (s == 14) { fix(IC<4>{}, IC<6>{}); BAR(); STAMP(20); SB(); }
      if constexpr (s == 22) { fix(IC<6>{}, IC<8>{}); BAR(); SB(); }
      if constexpr (s == 29) {
        const float sa = sum32x(sA + sB), qa = sum32x(qA + qB);
        const float m = sa * (1.0f / D);
        const float var = fmaxf(qa * (1.0f / D) - m * m, 0.0f);
        const float sd = rbf(__builtin_sqrtf(rbf(rbf(var) + 1e-5f)));
        if (lane < 32) *(LAS v4f*)(sm + L_MR + (w * 32 + lane) * 16) = v4f{rbf(m), -1.44269504088896340736f / sd, sd * sd, 0.0f};
        SB();
      }
    });
  }
  WAITV(11);
  BAR();
  STAMP(10);
  const u32 sAct = WS_ACT + ((mt * 4) * 112 + j * 7) * 1024;
  const u32 sAin = WS_ACT + ((mt * 4 + 2 * e2) * 112 + 28 * w) * 1024;
  v4u af[7][8];
  auto ld_af = [&](auto g, auto i) {
    if constexpr (g < 4) {
      constexpr int k = (i & 1) ^ (g & 1);
      ld_vgpr<(i & 1) * 1024>(af[g][i], srd, lane16, sAin + (k * 112 + 7 * (i >> 1) + (g < 2 ? 2 * e2 : 2 - 2 * e2)) * 1024);
    } else {
      ld_vgpr<(g - 4) * 1024>(af[g][i], srd, lane16, sAin + ((i & 1) * 112 + 7 * (i >> 1) + 4) * 1024);
    }
  };
  const int hh = lane >> 5;
  v4f mr[4], cs[4][4];
  auto cs_rd = [&](int k, int pt) {
    LAS char* cp = sm + L_CS + (pt * 16 + 4 * hh) * 4;
    cs[k][0] = *(LAS v4f*)cp; cs[k][1] = *(LAS v4f*)(cp + 32); cs[k][2] = *(LAS v4f*)(cp + 448); cs[k][3] = *(LAS v4f*)(cp + 480);
  };
  float ag[8], au[8], tg[8], tu[8], xe[8], pp[8], dn[8], rc[8], vv[8];
  v4u ob[2];
  auto s0 = [&](auto q) { constexpr int A = (q < 32 ? A_ACC1 + 16 * (q >> 3) : A_ACC2 + 16 * ((q >> 3) - 4)) + (q & 7); ag[q & 7] = accrd<A>(); au[q & 7] = accrd<A + 8>(); };
  auto s1 = [&](auto q) {
    constexpr int e = q & 7, U = q < 32 ? q >> 3 : 0, C = q < 32 ? 0 : (q >> 3) - 3;
    tg[e] = x_fnma(mr[U][0], cs[C][e >> 2][e & 3], ag[e]);
    tu[e] = x_fnma(mr[U][0], cs[C][2 + (e >> 2)][e & 3], au[e]);
    xe[e] = x_mul(tg[e], mr[U][1]);
  };
  auto s2a = [&](auto q) { constexpr int e = q & 7; xe[e] = x_exp(xe[e]); pp[e] = x_mul(tg[e], tu[e]); };
  auto s2b = [&](auto q) { constexpr int e = q & 7, U = q < 32 ? q >> 3 : 0; dn[e] = x_fma(xe[e], mr[U][2], mr[U][2]); };
  auto s3 = [&](auto q) { rc[q & 7] = x_rcp(dn[q & 7]); };
  auto s4a = [&](auto q) { constexpr int e = q & 7; vv[e] = x_mul(pp[e], rc[e]); };
  auto s4b = [&](auto q) { constexpr int e = q & 7; if constexpr (e & 1) ob[(q >> 3) & 1][e >> 1] = x_cap(x_pk(vv[e - 1], vv[e])); };
  auto s5 = [&](auto q) {
    constexpr int un = q >> 3;
    if constexpr ((q & 7) == 7)
      __builtin_amdgcn_raw_buffer_store_b128(ob[un & 1], rs, lane16, un < 4 ? sAct + ((w ^ un) * 112 + w) * 1024 : sAct + (w * 112 + un) * 1024, 16);
  };
  Sw st;
  auto breg = [](int o) { return o < 2 ? 240 + 8 * o : o < 10 ? 8 * (o - 2) : A_ACC2 + 8 * (o - 10); };
  auto wb_lds = [&](int x) { return x < 8 ? 98304 + w * 8192 + x * 1024 : x < 14 ? (x - 8) * 16384 + 12288 + w * 1024 : L_W2 + w * 6144 + (x - 14) * 1024; };
  {
    u32 wp[3] = {lane16, 65536 + lane16, L_W2 + lane16};
    asm volatile("" : "+v"(wp[1]), "+v"(wp[2]));
    v4u w2[3][3];
    auto w2_rd = [&](auto s, auto i) {
      constexpr int g = s >> 2;
      constexpr int off = (s & 3) * 3072 + i * 1024;
      if constexpr (g < 2) dsr128<g * 12288 + off>(w2[s % 3][i], wp[2]);
      else if constexpr (g < 6) dsr128<(g - 2) * 16384 + off>(w2[s % 3][i], wp[0]);
      else dsr128<(g - 6) * 16384 + off>(w2[s % 3][i], wp[1]);
    };
#pragma unroll
    for (int i = 0; i < 4; ++i) mr[i] = *(LAS v4f*)(sm + L_MR + ((w ^ i) * 32 + (lane & 31)) * 16);
    cs_rd(0, w);
    sfor<0, 3>([&](auto i) { w2_rd(IC<0>{}, i); });
    sfor<0, 3>([&](auto i) { w2_rd(IC<1>{}, i); });
    s0(IC<0>{}); s1(IC<0>{}); s2a(IC<0>{}); s2b(IC<0>{});
    st.dn = dn[0]; st.p = pp[0]; st.v = 0;
    dma16(srd, lane16, w2_src(17), w2_lds(17));
    SB();
    sfor<0, 32>([&](auto s) {
      if constexpr (s == 1 || s == 8 || s == 16 || s == 24) STAMP(24 + (s >> 2));
      if constexpr (s == 20) { WAITV(19); BARX(); }
      constexpr int q = s < 31 ? s + 1 : 31, e = q & 7, U = q >> 3;
      constexpr int r = s < 30 ? s + 2 : 31, g = r >> 2;
      constexpr int off = (g < 2 ? g * 12288 : g < 6 ? (g - 2) * 16384 : (g - 6) * 16384) + (r & 3) * 3072;
      constexpr int x = (s >= 3 && s < 17) ? s - 3 : (s >= 20 && s < 26) ? s - 6 : -1;
      if constexpr (x >= 0) set_m0(wb_lds(x));
      p2_a<s == 0, off>(w2[s % 3][0], w2[(s + 2) % 3], own[s], wp[g < 2 ? 2 : g < 6 ? 0 : 1]);
      if constexpr (s < 3) ld_wd(IC<29 + s>{});
      if constexpr (x >= 0) dma16n(srd, lane16, sWD + 32768 + (3 * ((2 + (x >> 1)) & 3) + ((2 + (x >> 1)) >> 2)) * 2048 + (x & 1) * 1024);
      if constexpr (s >= 24) { ld_af(IC<(s >= 28)>{}, IC<2 * (s & 3)>{}); ld_af(IC<(s >= 28)>{}, IC<2 * (s & 3) + 1>{}); }
      if constexpr (s > 0) s5(IC<s - 1>{});
      u32 o;
      p2_b<s == 0, (s & 1) != 0, A_ACC1 + 16 * U + e>(w2[s % 3][1], w2[s % 3][2], own[s], st, o,
                                                      mr[U][0], mr[U][1], mr[U][2], cs[0][e >> 2][e & 3], cs[0][2 + (e >> 2)][e & 3]);
      if constexpr (s & 1) ob[(s >> 3) & 1][(s & 7) >> 1] = o;
      if constexpr (s == 31) s5(s);
      SB();
    });
    waitl3<0>(w2[0]); waitl3<0>(w2[1]); waitl3<0>(w2[2]);
  }
  STAMP(11);
  cs_rd(1, 4); cs_rd(2, 5); cs_rd(3, 6);
  asm volatile("s_nop 7\n\ts_nop 3" ::: "memory");
  sfor<32, 40>([&](auto q) { s0(q); });
  sfor<32, 40>([&](auto q) { s1(q); });
  sfor<32, 40>([&](auto q) { s2a(q); });
  sfor<32, 40>([&](auto q) { s2b(q); });
  sfor<32, 40>([&](auto q) { s3(q); });
  sfor<32, 40>([&](auto q) { s4a(q); });
  sfor<32, 40>([&](auto q) { s4b(q); });
  s5(IC<39>{});
  SB();
  STAMP(12);
  u32 am = 0;
  auto af_fix = [&](auto g) {
    for (int spin = 0; spin < SPIN_CAP && __any(am == ~0u); ++spin) {
      COUNT(g < 2 ? 5 : g < 4 ? 6 : 7);
      sfor<0, 8>([&](auto i) { ld_af(g, i); });
      waitv8<0>(af[g]);
      am = 0;
      sfor<0, 8>([&](auto i) { x_max(am, af[g][i]); });
    }
    am = 0;
  };
  s0(IC<40>{}); s1(IC<40>{}); s2a(IC<40>{});
  waitv8<10>(af[0]);
  s2b(IC<40>{});
  st.dn = dn[0]; st.p = pp[0];
  sfor<0, 8>([&](auto i) { x_max(am, af[0][i]); });
  af_fix(IC<0>{});
  const u32 wbp[4] = {98304 + w * 8192 + lane16, 12288 + w * 1024 + lane16, 65536 + 12288 + w * 1024 + lane16, L_W2 + w * 6144 + lane16};
  auto wb_rd = [&](auto x) {
    dsr_agpr<breg(2 + (x >> 1)) + 4 * (x & 1), (x < 8 ? x * 1024 : x < 14 ? ((x - 8) & 3) * 16384 : (x - 14) * 1024)>(wbp[x < 8 ? 0 : x < 12 ? 1 : x < 14 ? 2 : 3]);
  };
  SB();
  STAMP(13);
  sfor<0, 7>([&](auto g) {
    constexpr int WV[6] = {10, 1, 9, 8, 4, 0};
    sfor<0, 16>([&](auto m) {
      if constexpr (m == 8 && g < 6) waitv8<WV[g < 6 ? g : 0]>(af[g < 6 ? g + 1 : 0]);
      if constexpr (g < 2) {
        if constexpr ((m & 1) == 0) {
          constexpr int i = m >> 1, k = (i & 1) ^ g, q = 40 + 8 * g + i, nq = q < 55 ? q + 1 : 55, e = nq & 7, C = (nq >> 3) - 3;
          u32 o;
          g2_pair<(g == 0 && m < 4), (i & 1) != 0, A_ACC2 + 16 * (C - 1) + e, A_ACC1 + 16 * k, A_W + 8 * i>(
              af[g][i], st, o, mr[0][0], mr[0][1], mr[0][2], cs[C][e >> 2][e & 3], cs[C][2 + (e >> 2)][e & 3]);
          if constexpr (i & 1) ob[(q >> 3) & 1][i >> 1] = o;
        }
      } else if constexpr (g < 4) {
        constexpr int i = m >> 1, dt = m & 1, k = (i & 1) ^ (g & 1);
        mfma_aav<A_ACC1 + 32 * dt + 16 * k, A_W + 64 + 8 * i + 4 * dt, false>(af[g][i]);
      } else {
        constexpr int jl = m >> 2, dt = (m >> 1) & 1, k = m & 1;
        mfma_aav<A_ACC1 + 32 * dt + 16 * k, breg(4 * (g - 4) + jl) + 4 * dt, false>(af[g][2 * jl + k]);
      }
      if constexpr (m >= 8 && g < 6) x_max(am, af[g < 6 ? g + 1 : 0][m - 8]);
      if constexpr (g == 0 && m < 8) ld_af(IC<2>{}, m);
      if constexpr (g == 1 && m < 4) wb_rd(IC<16 + m>{});
      if constexpr (g == 1 && m >= 8) ld_af(IC<3>{}, IC<m - 8>{});
      if constexpr (g == 2 && m < 8) ld_af(IC<4>{}, m);
      if constexpr (g == 2) wb_rd(m);
      if constexpr (g == 3 && m < 8) ld_af(IC<5>{}, m);
      if constexpr (g == 4 && m >= 4 && m < 12) ld_af(IC<6>{}, IC<m - 4>{});
      if constexpr (g < 2 && m == 15) s5(IC<47 + 8 * g>{});
      SB();
    });
    if constexpr (g < 6) af_fix(IC<g + 1>{});
    if constexpr (g == 3) BAR();
    STAMP(32 + g);
    SB();
  });
  STAMP(14);
  asm volatile("s_nop 7\n\ts_nop 3" ::: "memory");
  float a[16];
  auto xch = [&](auto W) {
    const u32 ad = W * 4096 + lane16;
    sfor<0, 4>([&](auto k) {
      if constexpr (k != W) {
        sfor<0, 4>([&](auto e) {
          dsw_agpr<A_ACC1 + 16 * k + 4 * e, k * 16384 + e * 1024>(ad);
        });
      }
    });
    sfor<0, 16>([&](auto e) { a[e] = accrd<A_ACC1 + 16 * W + e>(); });
  };
  if (w == 0) xch(IC<0>{}); else if (w == 1) xch(IC<1>{}); else if (w == 2) xch(IC<2>{}); else xch(IC<3>{});
  BAR();
#pragma unroll
  for (int k = 1; k < 4; ++k) {
#pragma unroll
    for (int e = 0; e < 4; ++e) {
      const v4f p = *(LAS v4f*)(sm + w * 16384 + (w ^ k) * 4096 + e * 1024 + lane16);
#pragma unroll
      for (int c = 0; c < 4; ++c) a[4 * e + c] += p[c];
    }
  }
  {
    u32 o[8];
#pragma unroll
    for (int e = 0; e < 8; ++e) {
      const u32 hv = hres[e >> 2][e & 3];
      const u32 f = pkbf(a[2 * e], a[2 * e + 1]);
      o[e] = pkbf(bflo(hv) + bflo(f), bfhi(hv) + bfhi(f));
    }
    u16* op = out + (size_t)(128 * mt + 64 * e2 + 32 * (w & 1) + (lane & 31)) * D + 64 * dg + 32 * (w >> 1) + 16 * hh;
    *(v4u*)op = v4u{o[0], o[1], o[2], o[3]};
    *(v4u*)(op + 8) = v4u{o[4], o[5], o[6], o[7]};
  }
  STAMP(15);
  asm volatile(".Lkf_end:");
}

extern "C" void kernel_launch(void* const* d_in, const int* in_sizes, int n_in, void* d_out, int out_size, void* d_ws, size_t ws_size,
                              hipStream_t stream) {
  (void)hipFuncSetAttribute((const void*)k_fused, hipFuncAttributeMaxDynamicSharedMemorySize, LDS_B);
  k_fused<<<256, 256, LDS_B, stream>>>((const u16*)d_in[0], (const u16*)d_in[1], (const u16*)d_in[2], (const u16*)d_in[3], (const u16*)d_in[4],
                                       (const u16*)d_in[5], (const u16*)d_in[6], (const u16*)d_in[8], (const u16*)d_in[9],
                                       (const u16*)d_in[10], (char*)d_ws, (u16*)d_out);
}
```

```cpp
#define NSLOT 2
#include <hip/hip_runtime.h>
#include <hip/hip_bf16.h>
#include <stdint.h>
#include <type_traits>

typedef unsigned short u16;
typedef unsigned int u32;
typedef unsigned long long u64;
typedef short v4s __attribute__((ext_vector_type(4)));
typedef __bf16 v2bf __attribute__((ext_vector_type(2)));
typedef _Float16 v2h __attribute__((ext_vector_type(2)));
typedef float v2f __attribute__((ext_vector_type(2)));
typedef float v4f __attribute__((ext_vector_type(4)));
typedef u32 v2u __attribute__((ext_vector_type(2)));
typedef u32 v4u __attribute__((ext_vector_type(4)));
#define LAS __attribute__((address_space(3)))
#define GAS __attribute__((address_space(1)))
#define DEV __device__ __forceinline__

constexpr int L = 2048, D = 512, NS = 128, F = 1792;

constexpr u32 WS_EPOCH = 0;
constexpr u32 WS_WFLAG = 4096;
constexpr u32 WS_GRAN  = 64 << 10;
constexpr u32 WS_CSUM  = 256 << 10;
constexpr u32 WS_HT    = 1 << 20;
constexpr u32 WS_WT1   = 4 << 20;
constexpr u32 WS_WT2   = 8 << 20;
constexpr u32 WS_ACT   = 12 << 20;
constexpr u32 WS_XG    = 20 << 20;
constexpr u32 WS_USED  = 24 << 20;
constexpr u32 WS_FIX   = 4 << 20;
constexpr size_t WS_ROT = 40 << 20;
constexpr size_t WS_DBG = 32 << 20;

template <int I> using IC = std::integral_constant<int, I>;
template <int B, int E, class Fn> DEV void sfor(Fn&& f) {
  if constexpr (B < E) { f(IC<B>{}); sfor<B + 1, E>(f); }
}

DEV float bf2f(u32 b) { return __uint_as_float(b << 16); }
DEV float bflo(u32 p) { return __uint_as_float(p << 16); }
DEV float bfhi(u32 p) { return __uint_as_float(p & 0xffff0000u); }
DEV u32 pkbf(float a, float b) { v2f f = {a, b}; return __builtin_bit_cast(u32, __builtin_convertvector(f, v2bf)); }
DEV u32 pkh(float a, float b) { v2f f = {a, b}; return __builtin_bit_cast(u32, __builtin_convertvector(f, v2h)); }
DEV float rbf(float a) { return bflo(pkbf(a, a)); }
DEV float dot2bf(u32 a, u32 b, float c) { return __builtin_amdgcn_fdot2_f32_bf16(__builtin_bit_cast(v2bf, a), __builtin_bit_cast(v2bf, b), c, false); }
DEV v2u trread(LAS char* p) { return __builtin_bit_cast(v2u, __builtin_amdgcn_ds_read_tr16_b64_v4i16((LAS v4s*)p)); }
DEV v4u trfrag(LAS char* p, int second) { const v2u a = trread(p), b = trread(p + second); return v4u{a[0], a[1], b[0], b[1]}; }
#define WAITV(n) asm volatile("s_waitcnt vmcnt(%0)" :: "i"(n) : "memory")
#define WAITL0() asm volatile("s_waitcnt lgkmcnt(0)" ::: "memory")
#define BAR() asm volatile("s_waitcnt lgkmcnt(0)\n\ts_barrier" ::: "memory")
#define BARX() asm volatile("s_barrier" ::: "memory")
#define SB() __builtin_amdgcn_sched_barrier(0)
constexpr int SPIN_CAP = 1 << 20;

#define STAMP(slot) do {} while (0)
#define COUNT(slot) do {} while (0)

template <int CTRL> DEV float dpp(float v) { return __builtin_bit_cast(float, __builtin_amdgcn_update_dpp(0, __builtin_bit_cast(int, v), CTRL, 0xf, 0xf, true)); }
DEV float swap16_other(float v, float& o) { auto r = __builtin_amdgcn_permlane16_swap(__float_as_uint(v), __float_as_uint(v), false, false); o = __uint_as_float(r[1]); return __uint_as_float(r[0]); }
DEV float swap32_other(float v, float& o) { auto r = __builtin_amdgcn_permlane32_swap(__float_as_uint(v), __float_as_uint(v), false, false); o = __uint_as_float(r[1]); return __uint_as_float(r[0]); }
DEV float sum16x(float v) { float o; const float a = swap16_other(v, o); return a + o; }
DEV float sum32x(float v) { float o; const float a = swap32_other(v, o); return a + o; }
DEV float wave_sum(float v) {
  v += dpp<0xB1>(v); v += dpp<0x4E>(v); v += dpp<0x141>(v); v += dpp<0x140>(v);
  return sum32x(sum16x(v));
}
DEV float wave_max(float v) {
  v = fmaxf(v, dpp<0xB1>(v)); v = fmaxf(v, dpp<0x4E>(v)); v = fmaxf(v, dpp<0x141>(v)); v = fmaxf(v, dpp<0x140>(v));
  float o; float a = swap16_other(v, o); v = fmaxf(a, o); a = swap32_other(v, o); return fmaxf(a, o);
}

DEV void dma16(v4u srd, u32 voff, u32 soff, u32 lds) {
  asm volatile("s_mov_b32 m0, %3\n\ts_nop 0\n\tbuffer_load_dwordx4 %0, %1, %2 offen sc1 lds" :: "v"(voff), "s"(srd), "s"(soff), "s"(lds) : "memory");
}
template <int A, int IMM> DEV void ld_agpr(v4u srd, u32 voff, u32 soff) {
  asm volatile("buffer_load_dwordx4 a[%c3:%c4], %0, %1, %2 offen offset:%c5 sc1" :: "v"(voff), "s"(srd), "s"(soff), "i"(A), "i"(A + 3), "i"(IMM) : "memory");
}
template <int IMM> DEV void ld_vgpr(v4u& d, v4u srd, u32 voff, u32 soff) {
  asm volatile("buffer_load_dwordx4 %0, %1, %2, %3 offen offset:%c4 sc1" : "=v"(d) : "v"(voff), "s"(srd), "s"(soff), "i"(IMM) : "memory");
}
template <int ACC, int A, bool FIRST> DEV void mfma_aav(const v4u& b) {
  if constexpr (FIRST) asm volatile("v_mfma_f32_32x32x16_bf16 a[%c1:%c2], a[%c3:%c4], %0, 0" :: "v"(b), "i"(ACC), "i"(ACC + 15), "i"(A), "i"(A + 3));
  else asm volatile("v_mfma_f32_32x32x16_bf16 a[%c1:%c2], a[%c3:%c4], %0, a[%c1:%c2]" :: "v"(b), "i"(ACC), "i"(ACC + 15), "i"(A), "i"(A + 3));
}
template <int ACC, bool FIRST> DEV void mfma_avv(const v4u& a, const v4u& b) {
  if constexpr (FIRST) asm volatile("v_mfma_f32_32x32x16_bf16 a[%c2:%c3], %0, %1, 0" :: "v"(a), "v"(b), "i"(ACC), "i"(ACC + 15));
  else asm volatile("v_mfma_f32_32x32x16_bf16 a[%c2:%c3], %0, %1, a[%c2:%c3]" :: "v"(a), "v"(b), "i"(ACC), "i"(ACC + 15));
}
template <int A, int OFF> DEV void dsw_agpr(u32 ad) {
  asm volatile("ds_write_b128 %0, a[%c1:%c2] offset:%c3" :: "v"(ad), "i"(A), "i"(A + 3), "i"(OFF) : "memory");
}
template <int OFF> DEV void dsw_halves(u32 ad, u32 v) {
  asm volatile("ds_write_b16 %0, %1 offset:%c2\n\tds_write_b16_d16_hi %0, %1 offset:%c3" :: "v"(ad), "v"(v), "i"(OFF), "i"(OFF + 2) : "memory");
}
DEV void ld_flag(u32& d, v4u srd, u32 voff, u32 soff) {
  asm volatile("buffer_load_dword %0, %1, %2, %3 offen sc1" : "=v"(d) : "v"(voff), "s"(srd), "s"(soff) : "memory");
}
DEV void wait0_pin(u32& d) { asm volatile("s_waitcnt vmcnt(0)" : "+v"(d) :: "memory"); }
DEV void pin2(float& a, float& b) { asm volatile("" : "+v"(a), "+v"(b)); }
template <int A> DEV float accrd() { float v; asm volatile("v_accvgpr_read_b32 %0, a%c1" : "=v"(v) : "i"(A)); return v; }
DEV float x_mul(float a, float b) { float d; asm volatile("v_mul_f32 %0, %1, %2" : "=v"(d) : "v"(a), "v"(b)); return d; }
DEV float x_fma(float a, float b, float c) { float d; asm volatile("v_fma_f32 %0, %1, %2, %3" : "=v"(d) : "v"(a), "v"(b), "v"(c)); return d; }
DEV float x_fnma(float a, float b, float c) { float d; asm volatile("v_fma_f32 %0, -%1, %2, %3" : "=v"(d) : "v"(a), "v"(b), "v"(c)); return d; }
DEV float x_exp(float a) { float d; asm volatile("v_exp_f32 %0, %1" : "=v"(d) : "v"(a)); return d; }
DEV float x_rcp(float a) { float d; asm volatile("v_rcp_f32 %0, %1" : "=v"(d) : "v"(a)); return d; }
DEV u32 x_pk(float a, float b) { u32 d; asm volatile("v_cvt_pk_bf16_f32 %0, %1, %2" : "=v"(d) : "v"(a), "v"(b)); return d; }
DEV u32 x_cap(u32 a) { u32 d; asm volatile("v_min_u32 %0, -2, %1" : "=v"(d) : "v"(a)); return d; }
DEV void x_max(u32& m, const v4u& v) { asm volatile("v_max3_u32 %0, %0, %1, %2\n\tv_max3_u32 %0, %0, %3, %4" : "+v"(m) : "v"(v[0]), "v"(v[1]), "v"(v[2]), "v"(v[3])); }
template <int OFF> DEV void dsr128(v4u& d, u32 ad) { asm volatile("ds_read_b128 %0, %1 offset:%c2" : "=v"(d) : "v"(ad), "i"(OFF) : "memory"); }
template <int N> DEV void waitl3(v4u (&a)[3]) { asm volatile("s_waitcnt lgkmcnt(%c3)" : "+v"(a[0]), "+v"(a[1]), "+v"(a[2]) : "i"(N) : "memory"); }
template <int N> DEV void waitv8(v4u (&a)[8]) {
  asm volatile("s_waitcnt vmcnt(%c8)" : "+v"(a[0]), "+v"(a[1]), "+v"(a[2]), "+v"(a[3]), "+v"(a[4]), "+v"(a[5]), "+v"(a[6]), "+v"(a[7]) : "i"(N) : "memory");
}
struct Sw { float dn, p, v; };
#define SW_A "v_rcp_f32 %[rc], %[dn]\n\tv_accvgpr_read_b32 %[ag], a%c[q]\n\tv_accvgpr_read_b32 %[au], a%c[q8]\n\t"
#define SW_B "v_fma_f32 %[ag], -%[m], %[cg], %[ag]\n\tv_fma_f32 %[au], -%[m], %[cu], %[au]\n\tv_mul_f32 %[rc], %[p], %[rc]\n\tv_mul_f32 %[dn], %[ag], %[rl]\n\t"
#define SW_C0 "v_exp_f32 %[dn], %[dn]\n\tv_mul_f32 %[p], %[ag], %[au]\n\tv_mov_b32 %[v], %[rc]\n\tv_fma_f32 %[dn], %[dn], %[s2], %[s2]"
#define SW_C1 "v_exp_f32 %[dn], %[dn]\n\tv_mul_f32 %[p], %[ag], %[au]\n\tv_cvt_pk_bf16_f32 %[o], %[v], %[rc]\n\tv_fma_f32 %[dn], %[dn], %[s2], %[s2]\n\tv_min_u32 %[o], -2, %[o]"
template <bool FIRST, int OFF> DEV void p2_a(const v4u& c0, v4u (&n)[3], const v4u& b, u32 ad) {
#define P2A(Z) asm volatile("s_waitcnt lgkmcnt(3)\n\tv_mfma_f32_32x32x16_bf16 a[192:207], %[w0], %[b], " Z "\n\t" \
    "ds_read_b128 %[n0], %[ad] offset:%c[of]\n\tds_read_b128 %[n1], %[ad] offset:%c[of]+1024\n\tds_read_b128 %[n2], %[ad] offset:%c[of]+2048" \
    : [n0] "=&v"(n[0]), [n1] "=&v"(n[1]), [n2] "=&v"(n[2]) : [w0] "v"(c0), [b] "v"(b), [ad] "v"(ad), [of] "i"(OFF) : "memory")
  if constexpr (FIRST) P2A("0"); else P2A("a[192:207]");
}
template <bool FIRST, bool ODD, int Q>
DEV void p2_b(const v4u& c1, const v4u& c2, const v4u& b, Sw& st, u32& o, float m, float rl, float s2, float cg, float cu) {
  float ag, au, t0;
#define P2B(Z1, Z2, RC, C) asm volatile("v_accvgpr_read_b32 %[ag], a%c[q]\n\tv_accvgpr_read_b32 %[au], a%c[q8]\n\t" \
    "v_mfma_f32_32x32x16_bf16 a[208:223], %[w1], %[b], " Z1 "\n\tv_rcp_f32 " RC ", %[dn]\n\t" \
    "v_fma_f32 %[ag], -%[m], %[cg], %[ag]\n\tv_fma_f32 %[au], -%[m], %[cu], %[au]\n\tv_mul_f32 %[dn], %[ag], %[rl]\n\tv_mul_f32 " RC ", %[p], " RC "\n\t" \
    "v_mfma_f32_32x32x16_bf16 a[224:239], %[w2], %[b], " Z2 "\n\tv_exp_f32 %[dn], %[dn]\n\tv_mul_f32 %[p], %[ag], %[au]\n\t" C \
    : [dn] "+v"(st.dn), [p] "+v"(st.p), [v] "+v"(st.v), [o] "=&v"(o), [t0] "=&v"(t0), [ag] "=&v"(ag), [au] "=&v"(au) \
    : [w1] "v"(c1), [w2] "v"(c2), [b] "v"(b), [m] "v"(m), [rl] "v"(rl), [s2] "v"(s2), [cg] "v"(cg), [cu] "v"(cu), [q] "i"(Q), [q8] "i"(Q + 8))
#define P2_EVEN "v_fma_f32 %[dn], %[dn], %[s2], %[s2]"
#define P2_ODD "v_cvt_pk_bf16_f32 %[o], %[v], %[t0]\n\tv_fma_f32 %[dn], %[dn], %[s2], %[s2]\n\tv_min_u32 %[o], -2, %[o]"
  if constexpr (FIRST) P2B("0", "0", "%[v]", P2_EVEN);
  else if constexpr (ODD) P2B("a[208:223]", "a[224:239]", "%[t0]", P2_ODD);
  else P2B("a[208:223]", "a[224:239]", "%[v]", P2_EVEN);
}
template <int A, int B> DEV void accmov4() {
  asm volatile("v_accvgpr_mov_b32 a%c0, a%c1\n\tv_accvgpr_mov_b32 a%c2, a%c3\n\tv_accvgpr_mov_b32 a%c4, a%c5\n\tv_accvgpr_mov_b32 a%c6, a%c7"
               :: "i"(A), "i"(B), "i"(A + 1), "i"(B + 1), "i"(A + 2), "i"(B + 2), "i"(A + 3), "i"(B + 3));
}
DEV void set_m0(u32 lds) { asm volatile("s_mov_b32 m0, %0" :: "s"(lds) : "memory"); }
DEV void dma16n(v4u srd, u32 voff, u32 soff) { asm volatile("buffer_load_dwordx4 %0, %1, %2 offen sc1 lds" :: "v"(voff), "s"(srd), "s"(soff) : "memory"); }
#define G2_TXT(Z0, Z1, C) \
  "v_mfma_f32_32x32x16_bf16 a[%c[d0]:%c[d0]+15], a[%c[wa]:%c[wa]+3], %[b], " Z0 "\n\t" SW_A SW_B \
  "v_mfma_f32_32x32x16_bf16 a[%c[d0]+32:%c[d0]+47], a[%c[wa]+4:%c[wa]+7], %[b], " Z1 "\n\t" C
#define G2_OPS : [dn] "+v"(st.dn), [p] "+v"(st.p), [v] "+v"(st.v), [o] "=&v"(o), [rc] "=&v"(rc), [ag] "=&v"(ag), [au] "=&v"(au) \
  : [b] "v"(b), [m] "v"(m), [rl] "v"(rl), [s2] "v"(s2), [cg] "v"(cg), [cu] "v"(cu), [q] "i"(Q), [q8] "i"(Q + 8), [d0] "i"(D0), [wa] "i"(WA)
template <bool FIRST, bool ODD, int Q, int D0, int WA>
DEV void g2_pair(const v4u& b, Sw& st, u32& o, float m, float rl, float s2, float cg, float cu) {
  float rc, ag, au;
  if constexpr (FIRST && ODD) asm volatile(G2_TXT("0", "0", SW_C1) G2_OPS);
  else if constexpr (FIRST) asm volatile(G2_TXT("0", "0", SW_C0) G2_OPS);
  else if constexpr (ODD) asm volatile(G2_TXT("a[%c[d0]:%c[d0]+15]", "a[%c[d0]+32:%c[d0]+47]", SW_C1) G2_OPS);
  else asm volatile(G2_TXT("a[%c[d0]:%c[d0]+15]", "a[%c[d0]+32:%c[d0]+47]", SW_C0) G2_OPS);
}
template <int A, int OFF> DEV void dsr_agpr(u32 ad) { asm volatile("ds_read_b128 a[%c1:%c2], %0 offset:%c3" :: "v"(ad), "i"(A), "i"(A + 3), "i"(OFF) : "memory"); }
DEV void mfma16(v4f& c, const v4u& a, const v4u& b) { asm volatile("v_mfma_f32_16x16x32_f16 %0, %1, %2, %0" : "+v"(c) : "v"(a), "v"(b)); }
DEV void mfma16z(v4f& c, const v4u& a, const v4u& b) { asm volatile("v_mfma_f32_16x16x32_f16 %0, %1, %2, 0" : "=v"(c) : "v"(a), "v"(b)); }
constexpr int A_W = 0, A_ACC1 = 128, A_ACC2 = 192;

constexpr int TCOL = 528, NCOL = 63, TAB_B = NCOL * TCOL;
constexpr int KC_B = 4640, UC_B = 4224;
constexpr int OFF_K = TAB_B, OFF_U = OFF_K + 4 * KC_B, OFF_X = OFF_U + 2 * UC_B, CH_B = OFF_X + 4096;
constexpr int L_RT1 = 2 * CH_B, L_RT2 = L_RT1 + 128 * 144, L_MISC = L_RT2 + 64 * 144;
constexpr int L_W2 = 131072;
constexpr int L_CSP = L_MISC, L_CS = L_CSP + 4096, L_MR = L_CS + 1024, LDS_B = L_MR + 2048;
static_assert(L_W2 >= L_RT1 && L_W2 + 24576 <= L_MISC && LDS_B <= 163840 && L_MISC % 16 == 0, "lds map");

typedef v2f cf;
DEV void cmul2(cf& r0, cf a0, cf b0, cf& r1, cf a1, cf b1) {
  asm("v_pk_mul_f32 %0, %2, %3 op_sel_hi:[0,1]\n\t"
      "v_pk_mul_f32 %1, %4, %5 op_sel_hi:[0,1]\n\t"
      "v_pk_fma_f32 %0, %2, %3, %0 op_sel:[1,1,0] op_sel_hi:[1,0,1] neg_lo:[1,0,0]\n\t"
      "v_pk_fma_f32 %1, %4, %5, %1 op_sel:[1,1,0] op_sel_hi:[1,0,1] neg_lo:[1,0,0]"
      : "=&v"(r0), "=&v"(r1) : "v"(a0), "v"(b0), "v"(a1), "v"(b1));
}
DEV cf cexp_p(float ar, float ai, float p) {
  const float e = __builtin_amdgcn_exp2f(ar * p * 1.44269504088896340736f);
  const float xx = ai * p;
  const float chi = 0.15915493667125702f, clo = 6.4206383e-09f;
  const float th = xx * chi;
  float tl = __builtin_fmaf(xx, chi, -th);
  tl = __builtin_fmaf(xx, clo, tl);
  const float fr = (th - __builtin_rintf(th)) + tl;
  return cf{e * __builtin_amdgcn_cosf(fr), e * __builtin_amdgcn_sinf(fr)};
}
template <int CTRL> DEV u32 dppu(u32 v) { return (u32)__builtin_amdgcn_update_dpp(0, (int)v, CTRL, 0xf, 0xf, true); }
DEV u32 wave_umax(u32 v) {
  v = max(v, dppu<0xB1>(v)); v = max(v, dppu<0x4E>(v)); v = max(v, dppu<0x141>(v)); v = max(v, dppu<0x140>(v));
  auto r = __builtin_amdgcn_permlane16_swap(v, v, false, false); v = max(r[0], r[1]);
  auto q = __builtin_amdgcn_permlane32_swap(v, v, false, false); return max(q[0], q[1]);
}

struct ConvSched { int nm, ma[18], mq[18], mk[18]; int nr, ru[16], ri[16]; };
constexpr ConvSched mk_sched(int c0, int c1, int c2, int c3) {
  ConvSched s{};
  const int c[4] = {c0, c1, c2, c3};
  bool hu[8] = {}, hk[8] = {};
  for (int q = 0; q <= c3; ++q)
    for (int a = 3; a >= 0; --a)
      if (q <= c[a]) {
        const int k = c[a] - q;
        s.ma[s.nm] = a; s.mq[s.nm] = q; s.mk[s.nm] = k; ++s.nm;
        if (!hu[q]) { hu[q] = true; s.ru[s.nr] = 1; s.ri[s.nr] = q; ++s.nr; }
        if (!hk[k]) { hk[k] = true; s.ru[s.nr] = 0; s.ri[s.nr] = k; ++s.nr; }
      }
  return s;
}
template <int V> struct CS { static constexpr ConvSched s = V == 0 ? mk_sched(0, 3, 4, 7) : mk_sched(1, 2, 5, 6); };

template <int V, class Ex> DEV void conv_main(LAS char* ub, LAS char* kb, v4f (&acc)[4], Ex&& extra) {
  constexpr ConvSched S = CS<V>::s;
  v4u U[2][8], K[2][8];
  sfor<0, S.nr>([&](auto i) {
    if constexpr (S.ru[i]) U[0][S.ri[i]] = *(LAS v4u*)(ub + 512 * S.ri[i]);
    else K[0][S.ri[i]] = *(LAS v4u*)(kb - 512 * S.ri[i]);
  });
  SB();
  sfor<0, 8>([&](auto r) {
    constexpr int cur = r & 1, nxt = cur ^ 1;
    sfor<0, 18>([&](auto m) {
      if constexpr (r == 0 && S.mq[m] == 0) mfma16z(acc[S.ma[m]], U[cur][S.mq[m]], K[cur][S.mk[m]]);
      else mfma16(acc[S.ma[m]], U[cur][S.mq[m]], K[cur][S.mk[m]]);
      if constexpr (r < 7 && m < S.nr) {
        if constexpr (S.ru[m]) U[nxt][S.ri[m]] = *(LAS v4u*)(ub + 64 * (r + 1) + 512 * S.ri[m]);
        else K[nxt][S.ri[m]] = *(LAS v4u*)(kb + 64 * (r + 1) - 512 * S.ri[m]);
      }
      extra(r, m);
      SB();
    });
  });
  asm volatile("s_nop 7" : "+v"(acc[0]), "+v"(acc[1]), "+v"(acc[2]), "+v"(acc[3]));
}

__global__ __launch_bounds__(256, 1) void k_fused(const u16* __restrict__ x, const u16* __restrict__ lar_, const u16* __restrict__ aim_,
                                                  const u16* __restrict__ cre_, const u16* __restrict__ cim_, const u16* __restrict__ ldt_,
                                                  const u16* __restrict__ dsk_, const u16* __restrict__ wg, const u16* __restrict__ wu,
                                                  const u16* __restrict__ wd, char* __restrict__ ws, u16* __restrict__ out) {
  extern __shared__ __attribute__((aligned(16))) char smx[];
  LAS char* sm = (LAS char*)smx;
  asm volatile("" ::: "a0", "a255");
  const int t = threadIdx.x, lane = t & 63, w = __builtin_amdgcn_readfirstlane(t >> 6), b = blockIdx.x;
  STAMP(0);
  const int xcd = b & 7, bi = b >> 3;
  u32 pfv;
  {
    u64 pc; u32 len;
    asm volatile("s_getpc_b64 %0\n.Lkf_here:\n\ts_mov_b32 %1, .Lkf_end-.Lkf_here" : "=s"(pc), "=s"(len));
    const u32 off = min((u32)((bi * 32 + (t & 31)) * 64), len);
    asm volatile("global_load_dword %0, %1, %2" : "=v"(pfv) : "v"(off), "s"(pc));
  }
  SB();
  const auto rs0 = __builtin_amdgcn_make_buffer_rsrc(ws, 0, WS_FIX, 0x00020000);
  const v4u srd0 = {(u32)(uintptr_t)ws, (u32)((uintptr_t)ws >> 32) & 0xffffu, WS_FIX, 0x00020000u};
  const int pr = xcd * 32 + bi;
  const int cw = w >> 1, nh = w & 1, ch = 2 * pr + cw;
  const int mt = 4 * (xcd >> 1) + (bi & 3), j = 8 * (xcd & 1) + (bi >> 2);
  const int e2 = j & 1, dg = j >> 1;
  const int G = lane >> 4, q4 = (lane >> 2) & 3, p4 = lane & 3, i16 = lane & 15;
  const u32 lane16 = lane * 16;
  LAS char* cb = sm + cw * CH_B;

  u32 pfw;
  {
    const char* pa = lane < 60 ? ws + WS_ROT + min((size_t)lane * (2u << 20), (size_t)NSLOT * WS_USED - 64) : (const char*)out + (size_t)(lane - 60) * (L * D * 2 / 4);
    asm volatile("global_load_dword %0, %1, off" : "=v"(pfw) : "v"(pa));
  }
  v4u xa[2];
#pragma unroll
  for (int i = 0; i < 2; ++i) xa[i] = *(const v4u*)(x + (size_t)(8 * b + 2 * w + i) * D + lane * 8);
  u32 xq[4][2];
#pragma unroll
  for (int i = 0; i < 4; ++i)
#pragma unroll
    for (int k = 0; k < 2; ++k) xq[i][k] = *(const u32*)(x + (size_t)(8 * b + 2 * (lane & 3) + k) * D + 2 * (64 * w + 16 * i + (lane >> 2)));
  const u32 ep = __builtin_amdgcn_raw_buffer_load_b32(rs0, WS_EPOCH, 0, 16);
  const int n = 64 * nh + lane;
  const u32 r_lar = lar_[ch * NS + n], r_aim = aim_[ch * NS + n], r_cre = cre_[ch * NS + n], r_cim = cim_[ch * NS + n];
  const u32 r_cre2 = cre_[ch * NS + (n ^ 64)], r_cim2 = cim_[ch * NS + (n ^ 64)];
  const u32 r_ldt = ldt_[ch], r_dsk = dsk_[ch];
  SB();
  {
    const v4u s = {~0u, ~0u, ~0u, ~0u};
#pragma unroll
    for (int i = 0; i < 2; ++i) __builtin_amdgcn_raw_buffer_store_b128(s, rs0, t * 16 + i * 4096, WS_HT + pr * 8192, 16);
  }
  SB();
  const int rb = b < 224 ? b : 223;
  const int mat = rb / 112, cg = (rb % 112) % 28, rq = (rb % 112) / 28, dgp = rb & 7, fb = rb >> 3;
  v4u wa[4], wb[2];
  {
    const u16* W = mat ? wu : wg;
#pragma unroll
    for (int i = 0; i < 4; ++i) wa[i] = *(const v4u*)(W + (size_t)(128 * rq + (t >> 3) + 32 * i) * F + 64 * cg + (t & 7) * 8);
#pragma unroll
    for (int i = 0; i < 2; ++i) wb[i] = *(const v4u*)(wd + (size_t)(64 * fb + (t >> 3) + 32 * i) * D + 64 * dgp + (t & 7) * 8);
  }
  SB();
  {
    const v4u z = {0, 0, 0, 0};
    const int tt = t & 127;
    LAS char* c2 = sm + (t >> 7) * CH_B;
    *(LAS v4u*)(c2 + OFF_K + (tt / 33) * KC_B + (255 + tt % 33) * 16) = z;
    if (tt < 4) *(LAS v4u*)(c2 + OFF_K + 3 * KC_B + (255 + 29 + tt) * 16) = z;
    if (tt < 16) *(LAS v4u*)(c2 + OFF_U + (tt >> 3) * UC_B + (tt & 7) * 16) = z;
  }
  const u32 tag = __builtin_amdgcn_readfirstlane(ep) + 1u;
  char* const wr = ws + WS_ROT + (size_t)(tag & (NSLOT - 1u)) * WS_USED;
  const auto rs = __builtin_amdgcn_make_buffer_rsrc(wr, 0, WS_USED, 0x00020000);
  const v4u srd = {(u32)(uintptr_t)wr, (u32)((uintptr_t)wr >> 32) & 0xffffu, WS_USED, 0x00020000u};
#pragma unroll
  for (int i = 0; i < 4; ++i)
    __builtin_amdgcn_raw_buffer_store_b128(v4u{xq[i][0], tag, xq[i][1], tag}, rs, (16 * i + (lane >> 2)) * 16384 + (lane & 3) * 16, WS_XG + w * (64 * 16384) + b * 64, 16);
  SB();
  {
    float v[2][8], s[2], q[2];
#pragma unroll
    for (int i = 0; i < 2; ++i) {
#pragma unroll
      for (int k = 0; k < 4; ++k) { v[i][2 * k] = bflo(xa[i][k]); v[i][2 * k + 1] = bfhi(xa[i][k]); }
      s[i] = ((v[i][0] + v[i][1]) + (v[i][2] + v[i][3])) + ((v[i][4] + v[i][5]) + (v[i][6] + v[i][7]));
    }
#pragma unroll
    for (int i = 0; i < 2; ++i) s[i] = wave_sum(s[i]) * (1.0f / D);
#pragma unroll
    for (int i = 0; i < 2; ++i) {
      q[i] = 0;
#pragma unroll
      for (int k = 0; k < 8; ++k) { const float d = v[i][k] - s[i]; q[i] += d * d; }
    }
#pragma unroll
    for (int i = 0; i < 2; ++i) q[i] = wave_sum(q[i]) * (1.0f / D);
    const float m = (lane & 1) ? s[1] : s[0], vv = (lane & 1) ? q[1] : q[0];
    const u32 pk = pkbf(m, __builtin_sqrtf(rbf(rbf(vv) + 1e-5f)));
    SB();
    WAITV(10);
    if (lane < 16) __builtin_amdgcn_raw_buffer_store_b64(v2u{pk, tag}, rs, (lane >> 1) * 16384 + (lane & 1) * 8, WS_GRAN + (8 * b + 2 * w) * 8, 16);
    SB();
  }
  STAMP(1);
  const float dt = rbf(expf(bf2f(r_ldt)));
  const float ar = -rbf(expf(bf2f(r_lar))) * dt, ai = bf2f(r_aim) * dt;
  int ex = (int)(wave_umax(max(max(r_cre & 0x7fffu, r_cim & 0x7fffu), max(r_cre2 & 0x7fffu, r_cim2 & 0x7fffu))) >> 7);
  ex = ex < 8 ? 8 : (ex > 250 ? 250 : ex);
  const float scl = __uint_as_float((u32)(257 - ex) << 23);
  const float yscl = 2.0f * dt * __uint_as_float((u32)(ex - 3) << 23);
  {
    LAS char* tb = cb + n * 4;
#define PUT(col, v) *(LAS u32*)(tb + (col) * TCOL) = pkh((v)[0], (v)[1])
    const v4u sent = {~0u, ~0u, ~0u, ~0u};
#define SENT(i) do { SB(); __builtin_amdgcn_raw_buffer_store_b128(sent, rs, lane16 + ((i) & 3) * 1024, WS_ACT + ((mt * 4 + w) * 112 + j * 7 + ((i) & 4)) * 1024, 16); SB(); } while (0)
    cf zp[17], zb[8], a[16], pa, pb;
    zp[1] = cexp_p(ar, ai, 1.0f); zp[16] = cexp_p(ar, ai, 16.0f); zb[1] = cexp_p(ar, ai, 256.0f);
    const cf wc = {zp[16][0], -zp[16][1]};
    a[0] = cf{bf2f(r_cre) * scl, -bf2f(r_cim) * scl};
    *(LAS u32*)(tb + 16 * TCOL) = 0x00003c00u;
    PUT(17, zp[1]); PUT(32, zb[1]); PUT(0, a[0]);
    SENT(0);
    cmul2(zp[2], zp[1], zp[1], zb[2], zb[1], zb[1]);
    cmul2(zp[3], zp[2], zp[1], zp[4], zp[2], zp[2]);
    cmul2(zb[3], zb[2], zb[1], zb[4], zb[2], zb[2]);
    cmul2(a[1], a[0], wc, zp[8], zp[4], zp[4]);
    PUT(18, zp[2]); PUT(19, zp[3]); PUT(20, zp[4]); PUT(41, zb[2]); PUT(46, zb[3]); PUT(51, zb[4]);
    SENT(1);
    cmul2(a[2], a[1], wc, zp[5], zp[4], zp[1]);
    cmul2(a[3], a[2], wc, zp[6], zp[4], zp[2]);
    cmul2(a[4], a[3], wc, zp[7], zp[4], zp[3]);
    PUT(1, a[1]); PUT(2, a[2]); PUT(3, a[3]); PUT(4, a[4]); PUT(21, zp[5]); PUT(22, zp[6]); PUT(23, zp[7]); PUT(24, zp[8]);
    SENT(2);
    sfor<9, 16>([&](auto i) { cmul2(a[i - 4], a[i - 5], wc, zp[i], zp[8], zp[i - 8]); PUT(i - 4, a[i - 4]); PUT(16 + i, zp[i]); });
    SENT(3);
    cmul2(a[12], a[11], wc, zb[5], zb[4], zb[1]);
    cmul2(a[13], a[12], wc, zb[6], zb[4], zb[2]);
    cmul2(a[14], a[13], wc, zb[7], zb[4], zb[3]);
    cmul2(a[15], a[14], wc, pa, zb[1], zp[16]);
    PUT(12, a[12]); PUT(13, a[13]); PUT(14, a[14]); PUT(15, a[15]); PUT(54, zb[5]); PUT(57, zb[6]); PUT(60, zb[7]); PUT(40, pa);
    SENT(4);
    sfor<0, 3>([&](auto i) { cmul2(pa, zb[1], zp[4 * i + 2], pb, zb[1], zp[4 * i + 4]); PUT(33 + 2 * i, pa); PUT(34 + 2 * i, pb); });
    cmul2(pa, zb[1], zp[14], pb, zb[1], zp[14]); PUT(39, pa);
    SENT(5);
    sfor<1, 5>([&](auto i) { cmul2(pa, zb[2], zp[4 * i], pb, zb[3], zp[4 * i]); PUT(41 + i, pa); PUT(46 + i, pb); });
    SENT(6);
    sfor<4, 8>([&](auto m) { cmul2(pa, zb[m], zp[8], pb, zb[m], zp[16]); PUT(52 + 3 * (m - 4), pa); PUT(53 + 3 * (m - 4), pb); });
#undef PUT
#undef SENT
  }
  STAMP(2);
  SB();
#pragma unroll
  for (int i = 0; i < 4; ++i) *(LAS v4u*)(sm + L_RT1 + ((t >> 3) + 32 * i) * 144 + (t & 7) * 16) = wa[i];
#pragma unroll
  for (int i = 0; i < 2; ++i) *(LAS v4u*)(sm + L_RT2 + ((t >> 3) + 32 * i) * 144 + (t & 7) * 16) = wb[i];
  BAR();
  STAMP(3);
  if (b < 224) {
    const int g16 = 4 * cg + w, jj = g16 / 7, pt = g16 % 7, h = G & 1;
    float cs = 0;
#pragma unroll
    for (int it = 0; it < 4; ++it) {
      const int ksl = 2 * it + (G >> 1), ks = 8 * rq + ksl;
      const v4u o = trfrag(sm + L_RT1 + (16 * ksl + 8 * h + q4) * 144 + 32 * w + 8 * p4, 4 * 144);
#pragma unroll
      for (int e = 0; e < 4; ++e) cs = dot2bf(o[e], 0x3f803f80u, cs);
      const int fr = pt < 4 ? pt * 32 + ks : 128 + ks * 3 + (pt - 4);
      __builtin_amdgcn_raw_buffer_store_b128(o, rs, fr * 1024 + (32 * h + 16 * mat + i16) * 16, WS_WT1 + jj * 229376, 16);
    }
    cs = sum32x(sum16x(cs));
    if (lane < 16) __builtin_amdgcn_raw_buffer_store_b32(__float_as_uint(cs), rs, lane * 4, WS_CSUM + jj * 4096 + (rq * 224 + mat * 112 + pt * 16) * 4, 16);
#pragma unroll
    for (int it = 0; it < 2; ++it) {
      const int sl = 2 * it + (G >> 1), s = 4 * fb + sl, jp = s / 7, pp = s % 7;
      const v4u o = trfrag(sm + L_RT2 + (16 * sl + 4 * h + q4) * 144 + 32 * w + 8 * p4, 8 * 144);
      const int pos = pp < 4 ? (pp >> 1) * 8 + (jp & 3) * 2 + (pp & 1) : 16 + (jp & 3) * 3 + (pp - 4);
      const int xrow = (i16 & 3) + 8 * (i16 >> 2) + 4 * (w & 1);
      __builtin_amdgcn_raw_buffer_store_b128(o, rs, (((jp >> 2) * 28 + pos) * 2 + (w >> 1)) * 1024 + (32 * h + xrow) * 16, WS_WT2 + dgp * 229376, 16);
    }
  }
  SB();
  STAMP(18);
  {
    v4u Bf[8], Af[2][8];
    const int c2 = (i16 + ((i16 >> 1) & 1)) >> 1, c4 = (i16 + 1 + ((i16 >> 2) & 1)) >> 2, c8 = (i16 + 3 + ((i16 >> 3) & 1)) >> 3;
    LAS char* ap[4];
    ap[0] = cb + (nh ? 32 + c2 : 16 + i16) * TCOL + G * 16;
    ap[1] = cb + (41 + 5 * nh + c4) * TCOL + G * 16;
    ap[2] = cb + (51 + 3 * nh + c8) * TCOL + G * 16;
    ap[3] = ap[2] + 6 * TCOL;
#pragma unroll
    for (int ks = 0; ks < 8; ++ks) { Bf[ks] = *(LAS v4u*)(cb + i16 * TCOL + ks * 64 + G * 16); Af[0][ks] = *(LAS v4u*)(ap[0] + ks * 64); }
    SB();
    v4f d[4];
    sfor<0, 4>([&](auto mm) {
      sfor<0, 8>([&](auto ks) {
        if constexpr (ks == 0) mfma16z(d[mm], Af[mm & 1][ks], Bf[ks]);
        else mfma16(d[mm], Af[mm & 1][ks], Bf[ks]);
        if constexpr (mm < 3) Af[(mm & 1) ^ 1][ks] = *(LAS v4u*)(ap[mm + 1] + ks * 64);
        SB();
      });
    });
    asm volatile("s_nop 7" : "+v"(d[0]), "+v"(d[1]), "+v"(d[2]), "+v"(d[3]));
    LAS char* kp = cb + OFF_K + 16 + (2038 - 256 * nh - 16 * i16 - 4 * G - 3 * 512) * 2;
#pragma unroll
    for (int mm = 0; mm < 4; ++mm) {
      const u32 p0 = pkh(d[mm][3], d[mm][2]), p1 = pkh(d[mm][1], d[mm][0]);
#pragma unroll
      for (int e = 0; e < 4; ++e) {
        LAS char* k2 = kp + (3 - mm) * 1024 + e * (KC_B + 4);
        if (e & 1) *(LAS v2u*)k2 = v2u{p0, p1};
        else { *(LAS u32*)k2 = p0; *(LAS u32*)(k2 + 4) = p1; }
      }
    }
  }
  WAITV(0);
  asm volatile("" :: "v"(pfv), "v"(pfw));
  if (lane == 0 && b < 224) __builtin_amdgcn_raw_buffer_store_b32(tag, rs, 0, WS_WFLAG + (b * 4 + w) * 16, 16);
  STAMP(4);
  {
    v4u g[4], gx[4];
    for (int spin = 0; spin < SPIN_CAP; ++spin) {
      asm volatile("" ::: "memory");
#pragma unroll
      for (int i = 0; i < 4; ++i) gx[i] = __builtin_amdgcn_raw_buffer_load_b128(rs, t * 16 + i * 4096, WS_XG + pr * 16384, 16);
#pragma unroll
      for (int i = 0; i < 4; ++i) g[i] = __builtin_amdgcn_raw_buffer_load_b128(rs, t * 16 + i * 4096, WS_GRAN + xcd * 16384, 16);
      u32 df = 0;
#pragma unroll
      for (int i = 0; i < 4; ++i) df |= ((g[i][1] ^ tag) | (g[i][3] ^ tag)) | ((gx[i][1] ^ tag) | (gx[i][3] ^ tag));
      if (__all(df == 0)) break;
      COUNT(0);
    }
    STAMP(5);
    LAS char* cc[2] = {sm + t * 4, sm + CH_B + t * 4};
    asm volatile("" : "+v"(cc[1]));
    const u32 ub0 = t * 4, ub1 = CH_B + t * 4;
    sfor<0, 4>([&](auto i) {
      const float m0 = bflo(g[i][0]), m1 = bflo(g[i][2]);
      const float r0 = __builtin_amdgcn_rcpf(bfhi(g[i][0])), r1 = __builtin_amdgcn_rcpf(bfhi(g[i][2]));
#pragma unroll
      for (int c = 0; c < 2; ++c) {
        const u32 d = pkbf((c ? bfhi(gx[i][0]) : bflo(gx[i][0])) - m0, (c ? bfhi(gx[i][2]) : bflo(gx[i][2])) - m1);
        const u32 o = pkbf(bflo(d) * r0, bfhi(d) * r1);
        const u32 uh = pkh(bflo(o), bfhi(o));
        LAS char* c2 = cc[c] + i * 1024;
        *(LAS u32*)(c2 + OFF_U + UC_B + 112) = uh;
        dsw_halves<OFF_U + 114 + i * 1024>(c ? ub1 : ub0, uh);
        *(LAS u32*)(c2 + OFF_X) = __builtin_amdgcn_perm(gx[i][2], gx[i][0], c ? 0x07060302u : 0x05040100u);
      }
    });
  }
  BAR();
  if (b == 0 && t == 0) __builtin_amdgcn_raw_buffer_store_b32(tag, rs0, 0, WS_EPOCH, 16);
  STAMP(6);
  v4f acc[4];
  {
    u32 fo;
    {
      int g16, pw;
      if (lane < 8) g16 = 7 * j + w; else g16 = 7 * j + 4 + ((lane - 8) >> 3);
      pw = ((lane & 1) * 112 + ((lane >> 1) & 3) * 28 + (g16 >> 2)) * 4 + (g16 & 3);
      if (lane >= 32) { const int k = lane < 60 ? lane - 32 : 0; pw = ((7 * w + (k >> 2)) * 8 + dg) * 4 + (k & 3); }
      fo = pw * 16;
    }
    const u32 sW1 = WS_WT1 + j * 229376 + w * 32768, sW2 = WS_WT1 + j * 229376 + 131072;
    u32 fl;
    auto extra = [&](auto r, auto m) {
      if constexpr (r == 0 && m == 0) ld_flag(fl, srd, fo, WS_WFLAG);
      if constexpr (r == 1 && m == 5) {
        STAMP(16);
        wait0_pin(fl);
        for (int spin = 0; spin < SPIN_CAP && !__all(fl == tag); ++spin) { ld_flag(fl, srd, fo, WS_WFLAG); wait0_pin(fl); COUNT(1); }
        STAMP(17);
      }
      if constexpr (r * 18 + m >= 24 && (r * 18 + m) % 3 == 0) {
        constexpr int k = (r * 18 + m) / 3 - 8;
        if constexpr (k < 32) ld_agpr<A_W + 4 * k, (k & 3) * 1024>(srd, lane16, sW1 + (k >> 2) * 4096);
        else if constexpr (k < 38) dma16(srd, lane16, sW2 + (w * 6 + k - 32) * 1024, L_W2 + (w * 6 + k - 32) * 1024);
      }
    };
    LAS char* ub = cb + OFF_U + (i16 & 1) * UC_B + ((i16 >> 1) + G) * 16;
    LAS char* kb = cb + OFF_K + (i16 & 3) * KC_B + 16 + (248 - 8 * (i16 >> 2) + G) * 16;
    if (nh == 0) conv_main<0>(ub, kb, acc, extra);
    else conv_main<1>(ub, kb, acc, extra);
  }
  STAMP(7);
  {
    const float dsk = bf2f(r_dsk);
#pragma unroll
    for (int a = 0; a < 4; ++a) {
#pragma unroll
      for (int hh = 0; hh < 2; ++hh) {
        const int l = 256 * (4 * (a >> 1) + ((a & 1) ? 3 - nh : nh)) + 8 * (2 * G + hh) + 2 * (i16 & 3) + 64 * (i16 >> 2);
        const v2h up = *(LAS v2h*)(cb + OFF_U + UC_B + (l + 56) * 2);
        LAS u32* xp = (LAS u32*)(cb + OFF_X + l * 2);
        const u32 xv = *xp;
        const u32 yb = pkbf(acc[a][2 * hh] * yscl, acc[a][2 * hh + 1] * yscl);
        const u32 sk = pkbf((float)up[0] * dsk, (float)up[1] * dsk);
        const u32 zz = pkbf(bflo(yb) + bflo(sk), bfhi(yb) + bfhi(sk));
        *xp = min(pkbf(bflo(xv) + bflo(zz), bfhi(xv) + bfhi(zz)), 0xfffffffeu);
      }
    }
    WAITL0();
#pragma unroll
    for (int i = 0; i < 2; ++i) {
      const int c = 4 * i + ((lane >> 5) ? 3 - nh : nh);
      const int l = 256 * c + (lane & 31) * 8;
      __builtin_amdgcn_raw_buffer_store_b128(*(LAS v4u*)(cb + OFF_X + l * 2), rs0, l * 2, WS_HT + ch * 4096, 16);
    }
  }
  const u32 sWD = WS_WT2 + dg * 229376 + w * 57344;
  sfor<0, 12>([&](auto f) { ld_agpr<A_ACC2 + 4 * f, (f & 3) * 1024>(srd, lane16, sWD + (e2 * 16 + (f & 12)) * 1024); });
  sfor<0, 4>([&](auto f) { ld_agpr<240 + 4 * f, (f & 1) * 1024>(srd, lane16, sWD + 32768 + (f >> 1) * 6144); });
  BAR();
  STAMP(8);
  const u32 vtok = (lane >> 4) * 4096 + (((lane & 15) ^ (4 * (lane >> 4))) * 16);
  const u32 stok = WS_HT + mt * 256 + w * 65536;
  auto tok_dma = [&](int c) {
#pragma unroll
    for (int i = 0; i < 4; ++i) dma16(srd0, vtok, stok + (64 * c + 4 * i) * 4096, c * 16384 + (4 * w + i) * 1024);
  };
  dma16(srd, lane16, WS_CSUM + j * 4096 + w * 1024, L_CSP + w * 1024);
#pragma unroll
  for (int c = 0; c < 3; ++c) tok_dma(c);
  LAS char* tp[4][2];
#pragma unroll
  for (int i = 0; i < 4; ++i) {
    tp[i][0] = sm + (8 * (G >> 1) + q4) * 256 + 8 * (p4 & 1) + (((4 * (w ^ i) + 2 * (G & 1) + (p4 >> 1)) ^ (4 * q4)) * 16);
    tp[i][1] = tp[i][0] + 65536;
    asm volatile("" : "+v"(tp[i][1]));
  }
  LAS char* vp[2];
  vp[0] = sm + w * 4096 + lane16; vp[1] = vp[0] + 65536;
  asm volatile("" : "+v"(vp[1]));
  v4u own[32], vq[4];
  u32 vm = 0;
  float sA = 0, sB = 0, qA = 0, qB = 0;
  auto mx = [&](u32 m, const v4u& v) { return max(max(m, max(v[0], v[1])), max(v[2], v[3])); };
  auto own_rd = [&](auto s) { own[s] = trfrag(tp[0][s >> 4] + (s & 15) * 4096, 1024); };
  auto own_acc = [&](auto s) {
#pragma unroll
    for (int e = 0; e < 4; e += 2) {
      sA = dot2bf(own[s][e], 0x3f803f80u, sA); qA = dot2bf(own[s][e], own[s][e], qA);
      sB = dot2bf(own[s][e + 1], 0x3f803f80u, sB); qB = dot2bf(own[s][e + 1], own[s][e + 1], qB);
    }
    pin2(sA, qA); pin2(sB, qB);
  };
  auto v_rd = [&](auto c, auto i) { vq[i] = *(LAS v4u*)(vp[c >> 2] + (c & 3) * 16384 + i * 1024); };
  auto fix = [&](auto c0, auto c1) {
    for (int spin = 0; spin < SPIN_CAP && __any(vm == ~0u); ++spin) {
      COUNT(2 + (c0 + 2) / 4);
#pragma unroll
      for (int c = c0; c < c1; ++c) tok_dma(c);
      WAITV(0);
      vm = 0;
      sfor<c0, c1>([&](auto c) {
        sfor<0, 4>([&](auto i) { v_rd(c, i); });
        sfor<0, 4>([&](auto i) { vm = mx(vm, vq[i]); });
      });
    }
    vm = 0;
  };
  STAMP(52);
  WAITV(8);
  STAMP(53);
  sfor<0, 4>([&](auto i) { v_rd(IC<0>{}, i); });
  sfor<0, 4>([&](auto i) { vm = mx(vm, vq[i]); });
  SB();
  WAITV(4);
  sfor<0, 4>([&](auto i) { v_rd(IC<1>{}, i); });
  sfor<0, 4>([&](auto i) { vm = mx(vm, vq[i]); });
  fix(IC<0>{}, IC<2>{});
  BAR();
  STAMP(9);
  const u32 sW2 = WS_WT1 + j * 229376 + 131072;
  auto w2_lds = [&](int k) { return (k / 3) * 16384 + (3 * w + k % 3) * 1024; };
  auto w2_src = [&](int k) { return sW2 + (2 + k / 3) * 12288 + (3 * w + k % 3) * 1024; };
  auto ld_wd = [&](auto f) { ld_agpr<A_W + 4 * f, (f & 3) * 1024>(srd, lane16, sWD + ((f < 16 ? e2 : 1 - e2) * 16 + (f & 12)) * 1024); };
  v4u hres[2];
  {
    v4u ot[2][3];
    LAS char* hp = sm + (64 * dg + 32 * (w >> 1) + 16 * (G >> 1) + q4) * 256 + 8 * (p4 & 1) +
                   (((4 * (2 * e2 + (w & 1)) + 2 * (G & 1) + (p4 >> 1)) ^ (4 * q4)) * 16);
    auto ot_rd = [&](auto s, auto i) { ot[s & 1][i] = trfrag(tp[i + 1][s >> 4] + (s & 15) * 4096, 1024); };
    own_rd(IC<0>{});
    sfor<0, 3>([&](auto i) { ot_rd(IC<0>{}, i); });
    if (t < 224) {
      LAS float* cp = (LAS float*)(sm + L_CSP) + t;
      *(LAS float*)(sm + L_CS + t * 4) = (cp[0] + cp[224]) + (cp[448] + cp[672]);
    }
    SB();
    sfor<0, 32>([&](auto s) {
      constexpr int vc = s == 2 ? 2 : s == 5 ? 3 : s == 9 ? 4 : s == 12 ? 5 : s == 17 ? 6 : s == 19 ? 7 : -1;
      constexpr int vy = s < 9 ? 3 : s < 17 ? 4 : 8;
      constexpr bool vmx = s == 3 || s == 6 || s == 10 || s == 13 || s == 18 || s == 20;
      constexpr bool two = (s >= 15 && s < 19) || (s >= 23 && s < 27);
      constexpr int o0 = s < 15 ? s + 1 : two ? (s < 19 ? 16 + 2 * (s - 15) : 24 + 2 * (s - 23)) : -1, o1 = two ? o0 + 1 : -1;
      constexpr int p0 = s < 15 ? (s / 3) * 4 + (s % 3 == 0 ? 0 : s % 3 + 1) : -1, p1 = (s < 15 && s % 3 == 0) ? p0 + 1 : -1;
      if constexpr (s == 16 || s == 20 || s == 23 || s == 24 || s == 27) STAMP(40 + s - 16);
      if constexpr (s == 5) sfor<0, 6>([&](auto k) { own_acc(k); });
      if constexpr (s == 12) sfor<6, 13>([&](auto k) { own_acc(k); });
      if constexpr (s == 20) sfor<13, 24>([&](auto k) { own_acc(k); });
      if constexpr (s == 28) sfor<24, 32>([&](auto k) { own_acc(k); });
      if constexpr (s == 20) STAMP(45);
      if constexpr (vc >= 0) WAITV(vy);
      if constexpr (p0 >= 0) set_m0((3 + p0 / 4) * 16384 + (4 * w + p0 % 4) * 1024);
      if constexpr (s >= 15) set_m0(w2_lds(s - 15));
      mfma_aav<A_ACC1, A_W + 4 * s, s == 0>(own[s]);
      if constexpr (s < 31) ot_rd(IC<s + 1>{}, IC<0>{});
      if constexpr (p0 >= 0) dma16n(srd0, vtok, stok + (64 * (3 + p0 / 4) + 4 * (p0 % 4)) * 4096);
      if constexpr (s >= 15) dma16n(srd, lane16, w2_src(s - 15));
      if constexpr (vmx) { vm = mx(vm, vq[0]); vm = mx(vm, vq[1]); }
      SB();
      mfma_aav<A_ACC1 + 16, A_W + 4 * s, s == 0>(ot[s & 1][0]);
      if constexpr (s < 31) ot_rd(IC<s + 1>{}, IC<1>{});
      if constexpr (o1 >= 0) own_rd(IC<o1>{});
      if constexpr (vmx) { vm = mx(vm, vq[2]); vm = mx(vm, vq[3]); }
      if constexpr (s >= 12 && s < 24) accmov4<A_W + 4 * (s - 12), A_ACC2 + 4 * (s - 12)>();
      if constexpr (p1 >= 0) set_m0((3 + p1 / 4) * 16384 + (4 * w + p1 % 4) * 1024);
      SB();
      mfma_aav<A_ACC1 + 32, A_W + 4 * s, s == 0>(ot[s & 1][1]);
      if constexpr (s < 31) ot_rd(IC<s + 1>{}, IC<2>{});
      if constexpr (p1 >= 0) dma16n(srd0, vtok, stok + (64 * (3 + p1 / 4) + 4 * (p1 % 4)) * 4096);
      if constexpr (s >= 15) ld_wd(IC<s - 3>{});
      if constexpr (vc >= 0) { v_rd(IC<vc>{}, IC<0>{}); v_rd(IC<vc>{}, IC<1>{}); }
      SB();
      mfma_aav<A_ACC1 + 48, A_W + 4 * s, s == 0>(ot[s & 1][2]);
      if constexpr (o0 >= 0) own_rd(IC<o0>{});
      if constexpr (vc >= 0) { v_rd(IC<vc>{}, IC<2>{}); v_rd(IC<vc>{}, IC<3>{}); }
      if constexpr (s == 1 || s == 7 || s == 16 || s == 23) {
        if ((dg >> 1) == (s + 1) / 8) { hres[0] = trfrag(hp, 1024); hres[1] = trfrag(hp + 2048, 1024); }
      }
      SB();
      if constexpr (s == 6) { fix(IC<2>{}, IC<4>{}); BAR(); STAMP(19); SB(); }
      if constexpr (s == 14) { fix(IC<4>{}, IC<6>{}); BAR(); STAMP(20); SB(); }
      if constexpr (s == 22) { fix(IC<6>{}, IC<8>{}); BAR(); SB(); }
      if constexpr (s == 29) {
        const float sa = sum32x(sA + sB), qa = sum32x(qA + qB);
        const float m = sa * (1.0f / D);
        const float var = fmaxf(qa * (1.0f / D) - m * m, 0.0f);
        const float sd = rbf(__builtin_sqrtf(rbf(rbf(var) + 1e-5f)));
        if (lane < 32) *(LAS v4f*)(sm + L_MR + (w * 32 + lane) * 16) = v4f{rbf(m), -1.44269504088896340736f / sd, sd * sd, 0.0f};
        SB();
      }
    });
  }
  WAITV(11);
  BAR();
  STAMP(10);
  const u32 sAct = WS_ACT + ((mt * 4) * 112 + j * 7) * 1024;
  const u32 sAin = WS_ACT + ((mt * 4 + 2 * e2) * 112 + 28 * w) * 1024;
  v4u af[7][8];
  auto ld_af = [&](auto g, auto i) {
    if constexpr (g < 4) {
      constexpr int k = (i & 1) ^ (g & 1);
      ld_vgpr<(i & 1) * 1024>(af[g][i], srd, lane16, sAin + (k * 112 + 7 * (i >> 1) + (g < 2 ? 2 * e2 : 2 - 2 * e2)) * 1024);
    } else {
      ld_vgpr<(g - 4) * 1024>(af[g][i], srd, lane16, sAin + ((i & 1) * 112 + 7 * (i >> 1) + 4) * 1024);
    }
  };
  const int hh = lane >> 5;
  v4f mr[4], cs[4][4];
  auto cs_rd = [&](int k, int pt) {
    LAS char* cp = sm + L_CS + (pt * 16 + 4 * hh) * 4;
    cs[k][0] = *(LAS v4f*)cp; cs[k][1] = *(LAS v4f*)(cp + 32); cs[k][2] = *(LAS v4f*)(cp + 448); cs[k][3] = *(LAS v4f*)(cp + 480);
  };
  float ag[8], au[8], tg[8], tu[8], xe[8], pp[8], dn[8], rc[8], vv[8];
  v4u ob[2];
  auto s0 = [&](auto q) { constexpr int A = (q < 32 ? A_ACC1 + 16 * (q >> 3) : A_ACC2 + 16 * ((q >> 3) - 4)) + (q & 7); ag[q & 7] = accrd<A>(); au[q & 7] = accrd<A + 8>(); };
  auto s1 = [&](auto q) {
    constexpr int e = q & 7, U = q < 32 ? q >> 3 : 0, C = q < 32 ? 0 : (q >> 3) - 3;
    tg[e] = x_fnma(mr[U][0], cs[C][e >> 2][e & 3], ag[e]);
    tu[e] = x_fnma(mr[U][0], cs[C][2 + (e >> 2)][e & 3], au[e]);
    xe[e] = x_mul(tg[e], mr[U][1]);
  };
  auto s2a = [&](auto q) { constexpr int e = q & 7; xe[e] = x_exp(xe[e]); pp[e] = x_mul(tg[e], tu[e]); };
  auto s2b = [&](auto q) { constexpr int e = q & 7, U = q < 32 ? q >> 3 : 0; dn[e] = x_fma(xe[e], mr[U][2], mr[U][2]); };
  auto s3 = [&](auto q) { rc[q & 7] = x_rcp(dn[q & 7]); };
  auto s4a = [&](auto q) { constexpr int e = q & 7; vv[e] = x_mul(pp[e], rc[e]); };
  auto s4b = [&](auto q) { constexpr int e = q & 7; if constexpr (e & 1) ob[(q >> 3) & 1][e >> 1] = x_cap(x_pk(vv[e - 1], vv[e])); };
  auto s5 = [&](auto q) {
    constexpr int un = q >> 3;
    if constexpr ((q & 7) == 7)
      __builtin_amdgcn_raw_buffer_store_b128(ob[un & 1], rs, lane16, un < 4 ? sAct + ((w ^ un) * 112 + w) * 1024 : sAct + (w * 112 + un) * 1024, 16);
  };
  Sw st;
  auto breg = [](int o) { return o < 2 ? 240 + 8 * o : o < 10 ? 8 * (o - 2) : A_ACC2 + 8 * (o - 10); };
  auto wb_lds = [&](int x) { return x < 8 ? 98304 + w * 8192 + x * 1024 : x < 14 ? (x - 8) * 16384 + 12288 + w * 1024 : L_W2 + w * 6144 + (x - 14) * 1024; };
  {
    u32 wp[3] = {lane16, 65536 + lane16, L_W2 + lane16};
    asm volatile("" : "+v"(wp[1]), "+v"(wp[2]));
    v4u w2[3][3];
    auto w2_rd = [&](auto s, auto i) {
      constexpr int g = s >> 2;
      constexpr int off = (s & 3) * 3072 + i * 1024;
      if constexpr (g < 2) dsr128<g * 12288 + off>(w2[s % 3][i], wp[2]);
      else if constexpr (g < 6) dsr128<(g - 2) * 16384 + off>(w2[s % 3][i], wp[0]);
      else dsr128<(g - 6) * 16384 + off>(w2[s % 3][i], wp[1]);
    };
#pragma unroll
    for (int i = 0; i < 4; ++i) mr[i] = *(LAS v4f*)(sm + L_MR + ((w ^ i) * 32 + (lane & 31)) * 16);
    cs_rd(0, w);
    sfor<0, 3>([&](auto i) { w2_rd(IC<0>{}, i); });
    sfor<0, 3>([&](auto i) { w2_rd(IC<1>{}, i); });
    s0(IC<0>{}); s1(IC<0>{}); s2a(IC<0>{}); s2b(IC<0>{});
    st.dn = dn[0]; st.p = pp[0]; st.v = 0;
    dma16(srd, lane16, w2_src(17), w2_lds(17));
    SB();
    sfor<0, 32>([&](auto s) {
      if constexpr (s == 1 || s == 8 || s == 16 || s == 24) STAMP(24 + (s >> 2));
      if constexpr (s == 20) { WAITV(19); BARX(); }
      constexpr int q = s < 31 ? s + 1 : 31, e = q & 7, U = q >> 3;
      constexpr int r = s < 30 ? s + 2 : 31, g = r >> 2;
      constexpr int off = (g < 2 ? g * 12288 : g < 6 ? (g - 2) * 16384 : (g - 6) * 16384) + (r & 3) * 3072;
      constexpr int x = (s >= 3 && s < 17) ? s - 3 : (s >= 20 && s < 26) ? s - 6 : -1;
      if constexpr (x >= 0) set_m0(wb_lds(x));
      p2_a<s == 0, off>(w2[s % 3][0], w2[(s + 2) % 3], own[s], wp[g < 2 ? 2 : g < 6 ? 0 : 1]);
      if constexpr (s < 3) ld_wd(IC<29 + s>{});
      if constexpr (x >= 0) dma16n(srd, lane16, sWD + 32768 + (3 * ((2 + (x >> 1)) & 3) + ((2 + (x >> 1)) >> 2)) * 2048 + (x & 1) * 1024);
      if constexpr (s >= 24) { ld_af(IC<(s >= 28)>{}, IC<2 * (s & 3)>{}); ld_af(IC<(s >= 28)>{}, IC<2 * (s & 3) + 1>{}); }
      if constexpr (s > 0) s5(IC<s - 1>{});
      u32 o;
      p2_b<s == 0, (s & 1) != 0, A_ACC1 + 16 * U + e>(w2[s % 3][1], w2[s % 3][2], own[s], st, o,
                                                      mr[U][0], mr[U][1], mr[U][2], cs[0][e >> 2][e & 3], cs[0][2 + (e >> 2)][e & 3]);
      if constexpr (s & 1) ob[(s >> 3) & 1][(s & 7) >> 1] = o;
      if constexpr (s == 31) s5(s);
      SB();
    });
    waitl3<0>(w2[0]); waitl3<0>(w2[1]); waitl3<0>(w2[2]);
  }
  STAMP(11);
  cs_rd(1, 4); cs_rd(2, 5); cs_rd(3, 6);
  asm volatile("s_nop 7\n\ts_nop 3" ::: "memory");
  sfor<32, 40>([&](auto q) { s0(q); });
  sfor<32, 40>([&](auto q) { s1(q); });
  sfor<32, 40>([&](auto q) { s2a(q); });
  sfor<32, 40>([&](auto q) { s2b(q); });
  sfor<32, 40>([&](auto q) { s3(q); });
  sfor<32, 40>([&](auto q) { s4a(q); });
  sfor<32, 40>([&](auto q) { s4b(q); });
  s5(IC<39>{});
  SB();
  STAMP(12);
  u32 am = 0;
  auto af_fix = [&](auto g) {
    for (int spin = 0; spin < SPIN_CAP && __any(am == ~0u); ++spin) {
      COUNT(g < 2 ? 5 : g < 4 ? 6 : 7);
      sfor<0, 8>([&](auto i) { ld_af(g, i); });
      waitv8<0>(af[g]);
      am = 0;
      sfor<0, 8>([&](auto i) { x_max(am, af[g][i]); });
    }
    am = 0;
  };
  s0(IC<40>{}); s1(IC<40>{}); s2a(IC<40>{});
  waitv8<10>(af[0]);
  s2b(IC<40>{});
  st.dn = dn[0]; st.p = pp[0];
  sfor<0, 8>([&](auto i) { x_max(am, af[0][i]); });
  af_fix(IC<0>{});
  const u32 wbp[4] = {98304 + w * 8192 + lane16, 12288 + w * 1024 + lane16, 65536 + 12288 + w * 1024 + lane16, L_W2 + w * 6144 + lane16};
  auto wb_rd = [&](auto x) {
    dsr_agpr<breg(2 + (x >> 1)) + 4 * (x & 1), (x < 8 ? x * 1024 : x < 14 ? ((x - 8) & 3) * 16384 : (x - 14) * 1024)>(wbp[x < 8 ? 0 : x < 12 ? 1 : x < 14 ? 2 : 3]);
  };
  SB();
  STAMP(13);
  sfor<0, 7>([&](auto g) {
    constexpr int WV[6] = {10, 1, 9, 8, 4, 0};
    sfor<0, 16>([&](auto m) {
      if constexpr (m == 8 && g < 6) waitv8<WV[g < 6 ? g : 0]>(af[g < 6 ? g + 1 : 0]);
      if constexpr (g < 2) {
        if constexpr ((m & 1) == 0) {
          constexpr int i = m >> 1, k = (i & 1) ^ g, q = 40 + 8 * g + i, nq = q < 55 ? q + 1 : 55, e = nq & 7, C = (nq >> 3) - 3;
          u32 o;
          g2_pair<(g == 0 && m < 4), (i & 1) != 0, A_ACC2 + 16 * (C - 1) + e, A_ACC1 + 16 * k, A_W + 8 * i>(
              af[g][i], st, o, mr[0][0], mr[0][1], mr[0][2], cs[C][e >> 2][e & 3], cs[C][2 + (e >> 2)][e & 3]);
          if constexpr (i & 1) ob[(q >> 3) & 1][i >> 1] = o;
        }
      } else if constexpr (g < 4) {
        constexpr int i = m >> 1, dt = m & 1, k = (i & 1) ^ (g & 1);
        mfma_aav<A_ACC1 + 32 * dt + 16 * k, A_W + 64 + 8 * i + 4 * dt, false>(af[g][i]);
      } else {
        constexpr int jl = m >> 2, dt = (m >> 1) & 1, k = m & 1;
        mfma_aav<A_ACC1 + 32 * dt + 16 * k, breg(4 * (g - 4) + jl) + 4 * dt, false>(af[g][2 * jl + k]);
      }
      if constexpr (m >= 8 && g < 6) x_max(am, af[g < 6 ? g + 1 : 0][m - 8]);
      if constexpr (g == 0 && m < 8) ld_af(IC<2>{}, m);
      if constexpr (g == 1 && m < 4) wb_rd(IC<16 + m>{});
      if constexpr (g == 1 && m >= 8) ld_af(IC<3>{}, IC<m - 8>{});
      if constexpr (g == 2 && m < 8) ld_af(IC<4>{}, m);
      if constexpr (g == 2) wb_rd(m);
      if constexpr (g == 3 && m < 8) ld_af(IC<5>{}, m);
      if constexpr (g == 4 && m >= 4 && m < 12) ld_af(IC<6>{}, IC<m - 4>{});
      if constexpr (g < 2 && m == 15) s5(IC<47 + 8 * g>{});
      SB();
    });
    if constexpr (g < 6) af_fix(IC<g + 1>{});
    if constexpr (g == 3) BAR();
    STAMP(32 + g);
    SB();
  });
  STAMP(14);
  asm volatile("s_nop 7\n\ts_nop 3" ::: "memory");
  float a[16];
  auto xch = [&](auto W) {
    const u32 ad = W * 4096 + lane16;
    sfor<0, 4>([&](auto k) {
      if constexpr (k != W) {
        sfor<0, 4>([&](auto e) {
          dsw_agpr<A_ACC1 + 16 * k + 4 * e, k * 16384 + e * 1024>(ad);
        });
      }
    });
    sfor<0, 16>([&](auto e) { a[e] = accrd<A_ACC1 + 16 * W + e>(); });
  };
  if (w == 0) xch(IC<0>{}); else if (w == 1) xch(IC<1>{}); else if (w == 2) xch(IC<2>{}); else xch(IC<3>{});
  BAR();
#pragma unroll
  for (int k = 1; k < 4; ++k) {
#pragma unroll
    for (int e = 0; e < 4; ++e) {
      const v4f p = *(LAS v4f*)(sm + w * 16384 + (w ^ k) * 4096 + e * 1024 + lane16);
#pragma unroll
      for (int c = 0; c < 4; ++c) a[4 * e + c] += p[c];
    }
  }
  {
    u32 o[8];
#pragma unroll
    for (int e = 0; e < 8; ++e) {
      const u32 hv = hres[e >> 2][e & 3];
      const u32 f = pkbf(a[2 * e], a[2 * e + 1]);
      o[e] = pkbf(bflo(hv) + bflo(f), bfhi(hv) + bfhi(f));
    }
    u16* op = out + (size_t)(128 * mt + 64 * e2 + 32 * (w & 1) + (lane & 31)) * D + 64 * dg + 32 * (w >> 1) + 16 * hh;
    *(v4u*)op = v4u{o[0], o[1], o[2], o[3]};
    *(v4u*)(op + 8) = v4u{o[4], o[5], o[6], o[7]};
  }
  STAMP(15);
  asm volatile(".Lkf_end:");
}

extern "C" void kernel_launch(void* const* d_in, const int* in_sizes, int n_in, void* d_out, int out_size, void* d_ws, size_t ws_size,
                              hipStream_t stream) {
  (void)hipFuncSetAttribute((const void*)k_fused, hipFuncAttributeMaxDynamicSharedMemorySize, LDS_B);
  k_fused<<<256, 256, LDS_B, stream>>>((const u16*)d_in[0], (const u16*)d_in[1], (const u16*)d_in[2], (const u16*)d_in[3], (const u16*)d_in[4],
                                       (const u16*)d_in[5], (const u16*)d_in[6], (const u16*)d_in[8], (const u16*)d_in[9],
                                       (const u16*)d_in[10], (char*)d_ws, (u16*)d_out);
}
```

```cpp
#define STOPAT 1
#include <hip/hip_runtime.h>
#include <hip/hip_bf16.h>
#include <stdint.h>
#include <type_traits>

typedef unsigned short u16;
typedef unsigned int u32;
typedef unsigned long long u64;
typedef short v4s __attribute__((ext_vector_type(4)));
typedef __bf16 v2bf __attribute__((ext_vector_type(2)));
typedef _Float16 v2h __attribute__((ext_vector_type(2)));
typedef float v2f __attribute__((ext_vector_type(2)));
typedef float v4f __attribute__((ext_vector_type(4)));
typedef u32 v2u __attribute__((ext_vector_type(2)));
typedef u32 v4u __attribute__((ext_vector_type(4)));
#define LAS __attribute__((address_space(3)))
#define GAS __attribute__((address_space(1)))
#define DEV __device__ __forceinline__

constexpr int L = 2048, D = 512, NS = 128, F = 1792;

constexpr u32 WS_EPOCH = 0;
constexpr u32 WS_WFLAG = 512 << 10;
constexpr u32 WS_GRAN  = 64 << 10;
constexpr u32 WS_CSUM  = 256 << 10;
constexpr u32 WS_HT    = 1 << 20;
constexpr u32 WS_WT1   = 4 << 20;
constexpr u32 WS_WT2   = 8 << 20;
constexpr u32 WS_ACT   = 12 << 20;
constexpr u32 WS_XG    = 20 << 20;
constexpr u32 HROW = 4352, WJ = 233472 + 4096, WPT = 33, WDW = 57, AT = 117;
constexpr u32 WS_USED  = 24 << 20;
constexpr u32 WS_FIX   = 4 << 20;
constexpr size_t WS_ROT = 40 << 20;
constexpr size_t WS_DBG = 32 << 20;

template <int I> using IC = std::integral_constant<int, I>;
template <int B, int E, class Fn> DEV void sfor(Fn&& f) {
  if constexpr (B < E) { f(IC<B>{}); sfor<B + 1, E>(f); }
}

DEV float bf2f(u32 b) { return __uint_as_float(b << 16); }
DEV float bflo(u32 p) { return __uint_as_float(p << 16); }
DEV float bfhi(u32 p) { return __uint_as_float(p & 0xffff0000u); }
DEV u32 pkbf(float a, float b) { v2f f = {a, b}; return __builtin_bit_cast(u32, __builtin_convertvector(f, v2bf)); }
DEV u32 pkh(float a, float b) { v2f f = {a, b}; return __builtin_bit_cast(u32, __builtin_convertvector(f, v2h)); }
DEV float rbf(float a) { return bflo(pkbf(a, a)); }
DEV float dot2bf(u32 a, u32 b, float c) { return __builtin_amdgcn_fdot2_f32_bf16(__builtin_bit_cast(v2bf, a), __builtin_bit_cast(v2bf, b), c, false); }
DEV v2u trread(LAS char* p) { return __builtin_bit_cast(v2u, __builtin_amdgcn_ds_read_tr16_b64_v4i16((LAS v4s*)p)); }
DEV v4u trfrag(LAS char* p, int second) { const v2u a = trread(p), b = trread(p + second); return v4u{a[0], a[1], b[0], b[1]}; }
#define WAITV(n) asm volatile("s_waitcnt vmcnt(%0)" :: "i"(n) : "memory")
#define WAITL0() asm volatile("s_waitcnt lgkmcnt(0)" ::: "memory")
#define BAR() asm volatile("s_waitcnt lgkmcnt(0)\n\ts_barrier" ::: "memory")
#define BARX() asm volatile("s_barrier" ::: "memory")
#define SB() __builtin_amdgcn_sched_barrier(0)
#define WSC " sc1"
constexpr int SPIN_CAP = 1 << 20;

#define STAMP(slot) do { if (slot == 0) pt0 = __builtin_amdgcn_s_memrealtime(); if (slot == STOPAT) ptk = __builtin_amdgcn_s_memrealtime(); if (slot == 15) { const u64 e = __builtin_amdgcn_s_memrealtime() + 4 * (ptk - pt0); while (__builtin_amdgcn_s_memrealtime() < e) __builtin_amdgcn_s_sleep(1); } } while (0)
#define COUNT(slot) do {} while (0)

template <int CTRL> DEV float dpp(float v) { return __builtin_bit_cast(float, __builtin_amdgcn_update_dpp(0, __builtin_bit_cast(int, v), CTRL, 0xf, 0xf, true)); }
DEV float swap16_other(float v, float& o) { auto r = __builtin_amdgcn_permlane16_swap(__float_as_uint(v), __float_as_uint(v), false, false); o = __uint_as_float(r[1]); return __uint_as_float(r[0]); }
DEV float swap32_other(float v, float& o) { auto r = __builtin_amdgcn_permlane32_swap(__float_as_uint(v), __float_as_uint(v), false, false); o = __uint_as_float(r[1]); return __uint_as_float(r[0]); }
DEV float sum16x(float v) { float o; const float a = swap16_other(v, o); return a + o; }
DEV float sum32x(float v) { float o; const float a = swap32_other(v, o); return a + o; }
DEV float wave_sum(float v) {
  v += dpp<0xB1>(v); v += dpp<0x4E>(v); v += dpp<0x141>(v); v += dpp<0x140>(v);
  return sum32x(sum16x(v));
}
DEV float wave_max(float v) {
  v = fmaxf(v, dpp<0xB1>(v)); v = fmaxf(v, dpp<0x4E>(v)); v = fmaxf(v, dpp<0x141>(v)); v = fmaxf(v, dpp<0x140>(v));
  float o; float a = swap16_other(v, o); v = fmaxf(a, o); a = swap32_other(v, o); return fmaxf(a, o);
}

DEV void dma16(v4u srd, u32 voff, u32 soff, u32 lds) {
  asm volatile("s_mov_b32 m0, %3\n\ts_nop 0\n\tbuffer_load_dwordx4 %0, %1, %2 offen sc1 lds" :: "v"(voff), "s"(srd), "s"(soff), "s"(lds) : "memory");
}
template <int A, int IMM> DEV void ld_agpr(v4u srd, u32 voff, u32 soff) {
  asm volatile("buffer_load_dwordx4 a[%c3:%c4], %0, %1, %2 offen offset:%c5" WSC :: "v"(voff), "s"(srd), "s"(soff), "i"(A), "i"(A + 3), "i"(IMM) : "memory");
}
template <int IMM> DEV void ld_vgpr(v4u& d, v4u srd, u32 voff, u32 soff) {
  asm volatile("buffer_load_dwordx4 %0, %1, %2, %3 offen offset:%c4 sc1" : "=v"(d) : "v"(voff), "s"(srd), "s"(soff), "i"(IMM) : "memory");
}
template <int ACC, int A, bool FIRST> DEV void mfma_aav(const v4u& b) {
  if constexpr (FIRST) asm volatile("v_mfma_f32_32x32x16_bf16 a[%c1:%c2], a[%c3:%c4], %0, 0" :: "v"(b), "i"(ACC), "i"(ACC + 15), "i"(A), "i"(A + 3));
  else asm volatile("v_mfma_f32_32x32x16_bf16 a[%c1:%c2], a[%c3:%c4], %0, a[%c1:%c2]" :: "v"(b), "i"(ACC), "i"(ACC + 15), "i"(A), "i"(A + 3));
}
template <int ACC, bool FIRST> DEV void mfma_avv(const v4u& a, const v4u& b) {
  if constexpr (FIRST) asm volatile("v_mfma_f32_32x32x16_bf16 a[%c2:%c3], %0, %1, 0" :: "v"(a), "v"(b), "i"(ACC), "i"(ACC + 15));
  else asm volatile("v_mfma_f32_32x32x16_bf16 a[%c2:%c3], %0, %1, a[%c2:%c3]" :: "v"(a), "v"(b), "i"(ACC), "i"(ACC + 15));
}
template <int A, int OFF> DEV void dsw_agpr(u32 ad) {
  asm volatile("ds_write_b128 %0, a[%c1:%c2] offset:%c3" :: "v"(ad), "i"(A), "i"(A + 3), "i"(OFF) : "memory");
}
template <int OFF> DEV void dsw_halves(u32 ad, u32 v) {
  asm volatile("ds_write_b16 %0, %1 offset:%c2\n\tds_write_b16_d16_hi %0, %1 offset:%c3" :: "v"(ad), "v"(v), "i"(OFF), "i"(OFF + 2) : "memory");
}
DEV void ld_flag(u32& d, v4u srd, u32 voff, u32 soff) {
  asm volatile("buffer_load_dword %0, %1, %2, %3 offen sc1" : "=v"(d) : "v"(voff), "s"(srd), "s"(soff) : "memory");
}
DEV void wait0_pin(u32& d) { asm volatile("s_waitcnt vmcnt(0)" : "+v"(d) :: "memory"); }
DEV void pin2(float& a, float& b) { asm volatile("" : "+v"(a), "+v"(b)); }
template <int A> DEV float accrd() { float v; asm volatile("v_accvgpr_read_b32 %0, a%c1" : "=v"(v) : "i"(A)); return v; }
DEV float x_mul(float a, float b) { float d; asm volatile("v_mul_f32 %0, %1, %2" : "=v"(d) : "v"(a), "v"(b)); return d; }
DEV float x_fma(float a, float b, float c) { float d; asm volatile("v_fma_f32 %0, %1, %2, %3" : "=v"(d) : "v"(a), "v"(b), "v"(c)); return d; }
DEV float x_fnma(float a, float b, float c) { float d; asm volatile("v_fma_f32 %0, -%1, %2, %3" : "=v"(d) : "v"(a), "v"(b), "v"(c)); return d; }
DEV float x_exp(float a) { float d; asm volatile("v_exp_f32 %0, %1" : "=v"(d) : "v"(a)); return d; }
DEV float x_rcp(float a) { float d; asm volatile("v_rcp_f32 %0, %1" : "=v"(d) : "v"(a)); return d; }
DEV u32 x_pk(float a, float b) { u32 d; asm volatile("v_cvt_pk_bf16_f32 %0, %1, %2" : "=v"(d) : "v"(a), "v"(b)); return d; }
DEV u32 x_cap(u32 a) { u32 d; asm volatile("v_min_u32 %0, -2, %1" : "=v"(d) : "v"(a)); return d; }
DEV void x_max(u32& m, const v4u& v) { asm volatile("v_max3_u32 %0, %0, %1, %2\n\tv_max3_u32 %0, %0, %3, %4" : "+v"(m) : "v"(v[0]), "v"(v[1]), "v"(v[2]), "v"(v[3])); }
template <int OFF> DEV void dsr128(v4u& d, u32 ad) { asm volatile("ds_read_b128 %0, %1 offset:%c2" : "=v"(d) : "v"(ad), "i"(OFF) : "memory"); }
template <int N> DEV void waitl3(v4u (&a)[3]) { asm volatile("s_waitcnt lgkmcnt(%c3)" : "+v"(a[0]), "+v"(a[1]), "+v"(a[2]) : "i"(N) : "memory"); }
template <int N> DEV void waitv8(v4u (&a)[8]) {
  asm volatile("s_waitcnt vmcnt(%c8)" : "+v"(a[0]), "+v"(a[1]), "+v"(a[2]), "+v"(a[3]), "+v"(a[4]), "+v"(a[5]), "+v"(a[6]), "+v"(a[7]) : "i"(N) : "memory");
}
struct Sw { float dn, p, v; };
#define SW_A "v_rcp_f32 %[rc], %[dn]\n\tv_accvgpr_read_b32 %[ag], a%c[q]\n\tv_accvgpr_read_b32 %[au], a%c[q8]\n\t"
#define SW_B "v_fma_f32 %[ag], -%[m], %[cg], %[ag]\n\tv_fma_f32 %[au], -%[m], %[cu], %[au]\n\tv_mul_f32 %[rc], %[p], %[rc]\n\tv_mul_f32 %[dn], %[ag], %[rl]\n\t"
#define SW_C0 "v_exp_f32 %[dn], %[dn]\n\tv_mul_f32 %[p], %[ag], %[au]\n\tv_mov_b32 %[v], %[rc]\n\tv_fma_f32 %[dn], %[dn], %[s2], %[s2]"
#define SW_C1 "v_exp_f32 %[dn], %[dn]\n\tv_mul_f32 %[p], %[ag], %[au]\n\tv_cvt_pk_bf16_f32 %[o], %[v], %[rc]\n\tv_fma_f32 %[dn], %[dn], %[s2], %[s2]\n\tv_min_u32 %[o], -2, %[o]"
template <bool FIRST, int OFF> DEV void p2_a(const v4u& c0, v4u (&n)[3], const v4u& b, u32 ad) {
#define P2A(Z) asm volatile("s_waitcnt lgkmcnt(3)\n\tv_mfma_f32_32x32x16_bf16 a[192:207], %[w0], %[b], " Z "\n\t" \
    "ds_read_b128 %[n0], %[ad] offset:%c[of]\n\tds_read_b128 %[n1], %[ad] offset:%c[of]+1024\n\tds_read_b128 %[n2], %[ad] offset:%c[of]+2048" \
    : [n0] "=&v"(n[0]), [n1] "=&v"(n[1]), [n2] "=&v"(n[2]) : [w0] "v"(c0), [b] "v"(b), [ad] "v"(ad), [of] "i"(OFF) : "memory")
  if constexpr (FIRST) P2A("0"); else P2A("a[192:207]");
}
template <bool FIRST, bool ODD, int Q>
DEV void p2_b(const v4u& c1, const v4u& c2, const v4u& b, Sw& st, u32& o, float m, float rl, float s2, float cg, float cu) {
  float ag, au, t0;
#define P2B(Z1, Z2, RC, C) asm volatile("v_accvgpr_read_b32 %[ag], a%c[q]\n\tv_accvgpr_read_b32 %[au], a%c[q8]\n\t" \
    "v_mfma_f32_32x32x16_bf16 a[208:223], %[w1], %[b], " Z1 "\n\tv_rcp_f32 " RC ", %[dn]\n\t" \
    "v_fma_f32 %[ag], -%[m], %[cg], %[ag]\n\tv_fma_f32 %[au], -%[m], %[cu], %[au]\n\tv_mul_f32 %[dn], %[ag], %[rl]\n\tv_mul_f32 " RC ", %[p], " RC "\n\t" \
    "v_mfma_f32_32x32x16_bf16 a[224:239], %[w2], %[b], " Z2 "\n\tv_exp_f32 %[dn], %[dn]\n\tv_mul_f32 %[p], %[ag], %[au]\n\t" C \
    : [dn] "+v"(st.dn), [p] "+v"(st.p), [v] "+v"(st.v), [o] "=&v"(o), [t0] "=&v"(t0), [ag] "=&v"(ag), [au] "=&v"(au) \
    : [w1] "v"(c1), [w2] "v"(c2), [b] "v"(b), [m] "v"(m), [rl] "v"(rl), [s2] "v"(s2), [cg] "v"(cg), [cu] "v"(cu), [q] "i"(Q), [q8] "i"(Q + 8))
#define P2_EVEN "v_fma_f32 %[dn], %[dn], %[s2], %[s2]"
#define P2_ODD "v_cvt_pk_bf16_f32 %[o], %[v], %[t0]\n\tv_fma_f32 %[dn], %[dn], %[s2], %[s2]\n\tv_min_u32 %[o], -2, %[o]"
  if constexpr (FIRST) P2B("0", "0", "%[v]", P2_EVEN);
  else if constexpr (ODD) P2B("a[208:223]", "a[224:239]", "%[t0]", P2_ODD);
  else P2B("a[208:223]", "a[224:239]", "%[v]", P2_EVEN);
}
template <int A, int B> DEV void accmov4() {
  asm volatile("v_accvgpr_mov_b32 a%c0, a%c1\n\tv_accvgpr_mov_b32 a%c2, a%c3\n\tv_accvgpr_mov_b32 a%c4, a%c5\n\tv_accvgpr_mov_b32 a%c6, a%c7"
               :: "i"(A), "i"(B), "i"(A + 1), "i"(B + 1), "i"(A + 2), "i"(B + 2), "i"(A + 3), "i"(B + 3));
}
DEV void set_m0(u32 lds) { asm volatile("s_mov_b32 m0, %0" :: "s"(lds) : "memory"); }
DEV void dma16w(v4u srd, u32 voff, u32 soff) { asm volatile("buffer_load_dwordx4 %0, %1, %2 offen" WSC " lds" :: "v"(voff), "s"(srd), "s"(soff) : "memory"); }
DEV void dma16n(v4u srd, u32 voff, u32 soff) { asm volatile("buffer_load_dwordx4 %0, %1, %2 offen sc1 lds" :: "v"(voff), "s"(srd), "s"(soff) : "memory"); }
#define G2_TXT(Z0, Z1, C) \
  "v_mfma_f32_32x32x16_bf16 a[%c[d0]:%c[d0]+15], a[%c[wa]:%c[wa]+3], %[b], " Z0 "\n\t" SW_A SW_B \
  "v_mfma_f32_32x32x16_bf16 a[%c[d0]+32:%c[d0]+47], a[%c[wa]+4:%c[wa]+7], %[b], " Z1 "\n\t" C
#define G2_OPS : [dn] "+v"(st.dn), [p] "+v"(st.p), [v] "+v"(st.v), [o] "=&v"(o), [rc] "=&v"(rc), [ag] "=&v"(ag), [au] "=&v"(au) \
  : [b] "v"(b), [m] "v"(m), [rl] "v"(rl), [s2] "v"(s2), [cg] "v"(cg), [cu] "v"(cu), [q] "i"(Q), [q8] "i"(Q + 8), [d0] "i"(D0), [wa] "i"(WA)
template <bool FIRST, bool ODD, int Q, int D0, int WA>
DEV void g2_pair(const v4u& b, Sw& st, u32& o, float m, float rl, float s2, float cg, float cu) {
  float rc, ag, au;
  if constexpr (FIRST && ODD) asm volatile(G2_TXT("0", "0", SW_C1) G2_OPS);
  else if constexpr (FIRST) asm volatile(G2_TXT("0", "0", SW_C0) G2_OPS);
  else if constexpr (ODD) asm volatile(G2_TXT("a[%c[d0]:%c[d0]+15]", "a[%c[d0]+32:%c[d0]+47]", SW_C1) G2_OPS);
  else asm volatile(G2_TXT("a[%c[d0]:%c[d0]+15]", "a[%c[d0]+32:%c[d0]+47]", SW_C0) G2_OPS);
}
template <int A, int OFF> DEV void dsr_agpr(u32 ad) { asm volatile("ds_read_b128 a[%c1:%c2], %0 offset:%c3" :: "v"(ad), "i"(A), "i"(A + 3), "i"(OFF) : "memory"); }
DEV void mfma16(v4f& c, const v4u& a, const v4u& b) { asm volatile("v_mfma_f32_16x16x32_f16 %0, %1, %2, %0" : "+v"(c) : "v"(a), "v"(b)); }
DEV void mfma16z(v4f& c, const v4u& a, const v4u& b) { asm volatile("v_mfma_f32_16x16x32_f16 %0, %1, %2, 0" : "=v"(c) : "v"(a), "v"(b)); }
constexpr int A_W = 0, A_ACC1 = 128, A_ACC2 = 192;

constexpr int TCOL = 528, NCOL = 63, TAB_B = NCOL * TCOL;
constexpr int KC_B = 4640, UC_B = 4224;
constexpr int OFF_K = TAB_B, OFF_U = OFF_K + 4 * KC_B, OFF_X = OFF_U + 2 * UC_B, CH_B = OFF_X + 4096;
constexpr int L_RT1 = 2 * CH_B, L_RT2 = L_RT1 + 128 * 144, L_MISC = L_RT2 + 64 * 144;
constexpr int L_W2 = 131072;
constexpr int L_CSP = L_MISC, L_CS = L_CSP + 4096, L_MR = L_CS + 1024, LDS_B = L_MR + 2048;
static_assert(L_W2 >= L_RT1 && L_W2 + 24576 <= L_MISC && LDS_B <= 163840 && L_MISC % 16 == 0, "lds map");

typedef v2f cf;
DEV void cmul2(cf& r0, cf a0, cf b0, cf& r1, cf a1, cf b1) {
  asm("v_pk_mul_f32 %0, %2, %3 op_sel_hi:[0,1]\n\t"
      "v_pk_mul_f32 %1, %4, %5 op_sel_hi:[0,1]\n\t"
      "v_pk_fma_f32 %0, %2, %3, %0 op_sel:[1,1,0] op_sel_hi:[1,0,1] neg_lo:[1,0,0]\n\t"
      "v_pk_fma_f32 %1, %4, %5, %1 op_sel:[1,1,0] op_sel_hi:[1,0,1] neg_lo:[1,0,0]"
      : "=&v"(r0), "=&v"(r1) : "v"(a0), "v"(b0), "v"(a1), "v"(b1));
}
DEV cf cexp_p(float ar, float ai, float p) {
  const float e = __builtin_amdgcn_exp2f(ar * p * 1.44269504088896340736f);
  const float xx = ai * p;
  const float chi = 0.15915493667125702f, clo = 6.4206383e-09f;
  const float th = xx * chi;
  float tl = __builtin_fmaf(xx, chi, -th);
  tl = __builtin_fmaf(xx, clo, tl);
  const float fr = (th - __builtin_rintf(th)) + tl;
  return cf{e * __builtin_amdgcn_cosf(fr), e * __builtin_amdgcn_sinf(fr)};
}
template <int CTRL> DEV u32 dppu(u32 v) { return (u32)__builtin_amdgcn_update_dpp(0, (int)v, CTRL, 0xf, 0xf, true); }
DEV u32 wave_umax(u32 v) {
  v = max(v, dppu<0xB1>(v)); v = max(v, dppu<0x4E>(v)); v = max(v, dppu<0x141>(v)); v = max(v, dppu<0x140>(v));
  auto r = __builtin_amdgcn_permlane16_swap(v, v, false, false); v = max(r[0], r[1]);
  auto q = __builtin_amdgcn_permlane32_swap(v, v, false, false); return max(q[0], q[1]);
}

struct ConvSched { int mc[36], mq[36], need[36], ru[16], ri[16]; };
constexpr ConvSched mk_sched() {
  ConvSched s{};
  bool hu[8] = {}, hk[8] = {};
  int nm = 0, nr = 0;
  for (int q = 0; q < 8; ++q)
    for (int c = 7; c >= q; --c) {
      if (!hu[q]) { hu[q] = true; s.ru[nr] = 1; s.ri[nr] = q; ++nr; }
      if (!hk[c - q]) { hk[c - q] = true; s.ru[nr] = 0; s.ri[nr] = c - q; ++nr; }
      s.mc[nm] = c; s.mq[nm] = q; s.need[nm] = nr; ++nm;
    }
  return s;
}
constexpr ConvSched CSCH = mk_sched();

template <class Ex> DEV void conv_main(u32 ub, u32 kb, v4f (&acc)[8], Ex&& extra) {
  v4u U[2][8], K[2][8];
  auto rd = [&](auto r, auto i) {
    if constexpr (CSCH.ru[i]) dsr128<64 * r + 512 * CSCH.ri[i]>(U[r & 1][CSCH.ri[i]], ub);
    else dsr128<64 * r + 512 * (7 - CSCH.ri[i])>(K[r & 1][CSCH.ri[i]], kb);
  };
  sfor<0, 16>([&](auto i) { rd(IC<0>{}, i); });
  SB();
  sfor<0, 4>([&](auto r) {
    constexpr int cur = r & 1;
    sfor<0, 36>([&](auto m) {
      if constexpr (m % 3 == 0) {
        constexpr int n = 16 - CSCH.need[m + 2] + (r < 3 ? (m < 32 ? (m + 1) / 2 : 16) : 0);
        if constexpr (m == 0 || CSCH.need[m + 2] > CSCH.need[m > 0 ? m - 1 : 0]) asm volatile("s_waitcnt lgkmcnt(%c0)" :: "i"(n < 15 ? n : 15) : "memory");
      }
      constexpr int c = CSCH.mc[m], q = CSCH.mq[m];
      if constexpr (r == 0 && q == 0) mfma16z(acc[c], U[cur][q], K[cur][c - q]);
      else mfma16(acc[c], U[cur][q], K[cur][c - q]);
      if constexpr (r < 3 && m < 32 && m % 2 == 0) rd(IC<r + 1>{}, IC<m / 2>{});
      extra(r, m);
      SB();
    });
  });
  asm volatile("s_nop 7" : "+v"(acc[0]), "+v"(acc[1]), "+v"(acc[2]), "+v"(acc[3]), "+v"(acc[4]), "+v"(acc[5]), "+v"(acc[6]), "+v"(acc[7]));
}

__global__ __launch_bounds__(256, 1) void k_fused(const u16* __restrict__ x, const u16* __restrict__ lar_, const u16* __restrict__ aim_,
                                                  const u16* __restrict__ cre_, const u16* __restrict__ cim_, const u16* __restrict__ ldt_,
                                                  const u16* __restrict__ dsk_, const u16* __restrict__ wg, const u16* __restrict__ wu,
                                                  const u16* __restrict__ wd, char* __restrict__ ws, u16* __restrict__ out) {
  extern __shared__ __attribute__((aligned(16))) char smx[];
  LAS char* sm = (LAS char*)smx;
  asm volatile("" ::: "a0", "a255");
  const int t = threadIdx.x, lane = t & 63, w = __builtin_amdgcn_readfirstlane(t >> 6), b = blockIdx.x;
  u64 pt0 = 0, ptk = 0;
  STAMP(0);
  const int xcd = b & 7, bi = b >> 3;
  u32 pfv;
  {
    u64 pc; u32 len;
    asm volatile("s_getpc_b64 %0\n.Lkf_here:\n\ts_mov_b32 %1, .Lkf_end-.Lkf_here" : "=s"(pc), "=s"(len));
    const u32 off = min((u32)((bi * 32 + (t & 31)) * 64), len);
    asm volatile("global_load_dword %0, %1, %2" : "=v"(pfv) : "v"(off), "s"(pc));
  }
  SB();
  const auto rs0 = __builtin_amdgcn_make_buffer_rsrc(ws, 0, WS_FIX, 0x00020000);
  const v4u srd0 = {(u32)(uintptr_t)ws, (u32)((uintptr_t)ws >> 32) & 0xffffu, WS_FIX, 0x00020000u};
  const int pr = xcd * 32 + bi;
  const int cw = w >> 1, nh = w & 1, ch = 2 * pr + cw;
  const int mt = 4 * (xcd >> 1) + (bi & 3), j = 8 * (xcd & 1) + (bi >> 2);
  const int e2 = j & 1, dg = j >> 1;
  const int G = lane >> 4, q4 = (lane >> 2) & 3, p4 = lane & 3, i16 = lane & 15;
  const u32 lane16 = lane * 16;
  LAS char* cb = sm + cw * CH_B;

  v4u xa[2];
#pragma unroll
  for (int i = 0; i < 2; ++i) xa[i] = *(const v4u*)(x + (size_t)(8 * b + 2 * w + i) * D + lane * 8);
  u32 xq[4][2];
#pragma unroll
  for (int i = 0; i < 4; ++i)
#pragma unroll
    for (int k = 0; k < 2; ++k) xq[i][k] = *(const u32*)(x + (size_t)(8 * b + 2 * (lane & 3) + k) * D + 2 * (64 * w + 16 * i + (lane >> 2)));
  const u32 ep = __builtin_amdgcn_raw_buffer_load_b32(rs0, WS_EPOCH, 0, 16);
  const int n = 64 * nh + lane;
  const u32 r_lar = lar_[ch * NS + n], r_aim = aim_[ch * NS + n], r_cre = cre_[ch * NS + n], r_cim = cim_[ch * NS + n];
  const u32 r_cre2 = cre_[ch * NS + (n ^ 64)], r_cim2 = cim_[ch * NS + (n ^ 64)];
  const u32 r_ldt = ldt_[ch], r_dsk = dsk_[ch];
  SB();
  {
    const v4u s = {~0u, ~0u, ~0u, ~0u};
#pragma unroll
    for (int i = 0; i < 2; ++i) __builtin_amdgcn_raw_buffer_store_b128(s, rs0, t * 16 + i * HROW, WS_HT + pr * 2 * HROW, 16);
  }
  SB();
  const int rb = b < 224 ? b : 223;
  const int mat = rb / 112, cg = (rb % 112) % 28, rq = (rb % 112) / 28, dgp = rb & 7, fb = rb >> 3;
  SB();
  {
    const v4u z = {0, 0, 0, 0};
    const int tt = t & 127;
    LAS char* c2 = sm + (t >> 7) * CH_B;
    *(LAS v4u*)(c2 + OFF_K + (tt / 33) * KC_B + (255 + tt % 33) * 16) = z;
    if (tt < 4) *(LAS v4u*)(c2 + OFF_K + 3 * KC_B + (255 + 29 + tt) * 16) = z;
    if (tt < 16) *(LAS v4u*)(c2 + OFF_U + (tt >> 3) * UC_B + (tt & 7) * 16) = z;
  }
  const int g16 = 4 * cg + w, jj = g16 / 7, ptw = g16 % 7, hb = G & 1;
  LAS char* rt1 = sm + L_RT1 + (16 * (G >> 1) + 8 * hb + q4) * 144 + 32 * w + 8 * p4;
  LAS char* rt2 = sm + L_RT2 + (16 * (G >> 1) + 4 * hb + q4) * 144 + 32 * w + 8 * p4;
  LAS char* rtw = sm + L_RT1 + (t >> 3) * 144 + (t & 7) * 16;
  u32 vo1 = ((ptw < 4 ? 1 : 3) * (8 * rq + (G >> 1)) + (ptw < 4 ? ptw * WPT : 4 * WPT - 4 + ptw)) * 1024 + (32 * hb + 16 * mat + i16) * 16, vo2[2];
  const u32 so1 = WS_WT1 + jj * WJ, inc1 = ptw < 4 ? 2048 : 6144;
#pragma unroll
  for (int it = 0; it < 2; ++it) {
    const int sx = 4 * fb + 2 * it + (G >> 1), jp = sx / 7, pp = sx % 7;
    const int pos = pp < 4 ? (pp >> 1) * 8 + (jp & 3) * 2 + (pp & 1) : 16 + (jp & 3) * 3 + (pp - 4);
    vo2[it] = ((jp >> 2) * WDW + pos * 2 + (w >> 1)) * 1024 + (32 * hb + (i16 & 3) + 8 * (i16 >> 2) + 4 * (w & 1)) * 16;
  }
  LAS char* ap[4];
  {
    const int c2 = (i16 + ((i16 >> 1) & 1)) >> 1, c4 = (i16 + 1 + ((i16 >> 2) & 1)) >> 2, c8 = (i16 + 3 + ((i16 >> 3) & 1)) >> 3;
    ap[0] = cb + (nh ? 32 + c2 : 16 + i16) * TCOL + G * 16;
    ap[1] = cb + (41 + 5 * nh + c4) * TCOL + G * 16;
    ap[2] = cb + (51 + 3 * nh + c8) * TCOL + G * 16;
    ap[3] = cb + i16 * TCOL + G * 16;
  }
  LAS char* kp = cb + OFF_K + 16 + (2038 - 256 * nh - 16 * i16 - 4 * G - 3 * 512) * 2;
  u32 ub = cw * CH_B + OFF_U + (i16 & 1) * UC_B + ((i16 >> 1) + G) * 16 + 256 * nh;
  u32 kb = cw * CH_B + OFF_K + (i16 & 3) * KC_B + 16 + (248 - 8 * (i16 >> 2) + G) * 16 - 7 * 512 + 256 * nh;
  u32 fo;
  {
    const int gq = lane < 8 ? 7 * j + w : 7 * j + 4 + ((lane - 8) >> 3), k = lane < 60 ? lane - 32 : 0;
    fo = (lane < 32 ? ((lane & 1) * 112 + ((lane >> 1) & 3) * 28 + (gq >> 2)) * 4 + (gq & 3) : ((7 * w + (k >> 2)) * 8 + dg) * 4 + (k & 3)) * 128;
  }
  LAS char* he[2];
  he[0] = cb + (16 * G + 2 * (i16 & 3) + 64 * (i16 >> 2) + 256 * nh) * 2; he[1] = he[0] + (3 - 2 * nh) * 512;
  u32 ho = (256 * ((lane >> 5) ? 3 - nh : nh) + (lane & 31) * 8) * 2;
  u32 t16 = t * 16;
  asm volatile("" : "+v"(rt1), "+v"(rt2), "+v"(rtw), "+v"(vo1), "+v"(vo2[0]), "+v"(vo2[1]), "+v"(ap[0]), "+v"(ap[1]), "+v"(ap[2]), "+v"(ap[3]),
               "+v"(kp), "+v"(ub), "+v"(kb), "+v"(fo), "+v"(he[0]), "+v"(he[1]), "+v"(ho), "+v"(t16));
  SB();
  const u32 tag = __builtin_amdgcn_readfirstlane(ep) + 1u;
  char* const wr = ws + WS_ROT + (size_t)(tag & 3u) * WS_USED;
  const auto rs = __builtin_amdgcn_make_buffer_rsrc(wr, 0, WS_USED, 0x00020000);
  const v4u srd = {(u32)(uintptr_t)wr, (u32)((uintptr_t)wr >> 32) & 0xffffu, WS_USED, 0x00020000u};
#pragma unroll
  for (int i = 0; i < 4; ++i)
    __builtin_amdgcn_raw_buffer_store_b128(v4u{xq[i][0], tag, xq[i][1], tag}, rs, (16 * i + (lane >> 2)) * 16384 + (lane & 3) * 16, WS_XG + w * (64 * 16384) + b * 64, 16);
  SB();
  {
    float v[2][8], s[2], q[2];
#pragma unroll
    for (int i = 0; i < 2; ++i) {
#pragma unroll
      for (int k = 0; k < 4; ++k) { v[i][2 * k] = bflo(xa[i][k]); v[i][2 * k + 1] = bfhi(xa[i][k]); }
      s[i] = ((v[i][0] + v[i][1]) + (v[i][2] + v[i][3])) + ((v[i][4] + v[i][5]) + (v[i][6] + v[i][7]));
    }
#pragma unroll
    for (int i = 0; i < 2; ++i) s[i] = wave_sum(s[i]) * (1.0f / D);
#pragma unroll
    for (int i = 0; i < 2; ++i) {
      q[i] = 0;
#pragma unroll
      for (int k = 0; k < 8; ++k) { const float d = v[i][k] - s[i]; q[i] += d * d; }
    }
#pragma unroll
    for (int i = 0; i < 2; ++i) q[i] = wave_sum(q[i]) * (1.0f / D);
    const float m = (lane & 1) ? s[1] : s[0], vv = (lane & 1) ? q[1] : q[0];
    const u32 pk = pkbf(m, __builtin_sqrtf(rbf(rbf(vv) + 1e-5f)));
    SB();
    WAITV(4);
    if (lane < 16) __builtin_amdgcn_raw_buffer_store_b64(v2u{pk, tag}, rs, (lane >> 1) * 16384 + (lane & 1) * 8, WS_GRAN + (8 * b + 2 * w) * 8, 16);
    SB();
  }
  STAMP(1);
  v4u wa[4], wb[2];
  {
    const u16* W = mat ? wu : wg;
#pragma unroll
    for (int i = 0; i < 4; ++i) wa[i] = *(const v4u*)(W + (size_t)(128 * rq + (t >> 3) + 32 * i) * F + 64 * cg + (t & 7) * 8);
#pragma unroll
    for (int i = 0; i < 2; ++i) wb[i] = *(const v4u*)(wd + (size_t)(64 * fb + (t >> 3) + 32 * i) * D + 64 * dgp + (t & 7) * 8);
  }
  SB();
  const float dt = rbf(expf(bf2f(r_ldt)));
  const float ar = -rbf(expf(bf2f(r_lar))) * dt, ai = bf2f(r_aim) * dt;
  int ex = (int)(wave_umax(max(max(r_cre & 0x7fffu, r_cim & 0x7fffu), max(r_cre2 & 0x7fffu, r_cim2 & 0x7fffu))) >> 7);
  ex = ex < 8 ? 8 : (ex > 250 ? 250 : ex);
  const float scl = __uint_as_float((u32)(257 - ex) << 23);
  const float yscl = 2.0f * dt * __uint_as_float((u32)(ex - 3) << 23);
  {
    LAS char* tb = cb + n * 4;
#define PUT(col, v) *(LAS u32*)(tb + (col) * TCOL) = pkh((v)[0], (v)[1])
    cf zp[17], zb[8], a[16], pa, pb;
    zp[1] = cexp_p(ar, ai, 1.0f); zp[16] = cexp_p(ar, ai, 16.0f); zb[1] = cexp_p(ar, ai, 256.0f);
    const cf wc = {zp[16][0], -zp[16][1]};
    a[0] = cf{bf2f(r_cre) * scl, -bf2f(r_cim) * scl};
    *(LAS u32*)(tb + 16 * TCOL) = 0x00003c00u;
    PUT(17, zp[1]); PUT(32, zb[1]); PUT(0, a[0]);
    cmul2(zp[2], zp[1], zp[1], zb[2], zb[1], zb[1]);
    cmul2(zp[3], zp[2], zp[1], zp[4], zp[2], zp[2]);
    cmul2(zb[3], zb[2], zb[1], zb[4], zb[2], zb[2]);
    cmul2(a[1], a[0], wc, zp[8], zp[4], zp[4]);
    PUT(18, zp[2]); PUT(19, zp[3]); PUT(20, zp[4]); PUT(41, zb[2]); PUT(46, zb[3]); PUT(51, zb[4]);
    cmul2(a[2], a[1], wc, zp[5], zp[4], zp[1]);
    cmul2(a[3], a[2], wc, zp[6], zp[4], zp[2]);
    cmul2(a[4], a[3], wc, zp[7], zp[4], zp[3]);
    PUT(1, a[1]); PUT(2, a[2]); PUT(3, a[3]); PUT(4, a[4]); PUT(21, zp[5]); PUT(22, zp[6]); PUT(23, zp[7]); PUT(24, zp[8]);
    sfor<9, 16>([&](auto i) { cmul2(a[i - 4], a[i - 5], wc, zp[i], zp[8], zp[i - 8]); PUT(i - 4, a[i - 4]); PUT(16 + i, zp[i]); });
    cmul2(a[12], a[11], wc, zb[5], zb[4], zb[1]);
    cmul2(a[13], a[12], wc, zb[6], zb[4], zb[2]);
    cmul2(a[14], a[13], wc, zb[7], zb[4], zb[3]);
    cmul2(a[15], a[14], wc, pa, zb[1], zp[16]);
    PUT(12, a[12]); PUT(13, a[13]); PUT(14, a[14]); PUT(15, a[15]); PUT(54, zb[5]); PUT(57, zb[6]); PUT(60, zb[7]); PUT(40, pa);
    sfor<0, 3>([&](auto i) { cmul2(pa, zb[1], zp[4 * i + 2], pb, zb[1], zp[4 * i + 4]); PUT(33 + 2 * i, pa); PUT(34 + 2 * i, pb); });
    cmul2(pa, zb[1], zp[14], pb, zb[1], zp[14]); PUT(39, pa);
    sfor<1, 5>([&](auto i) { cmul2(pa, zb[2], zp[4 * i], pb, zb[3], zp[4 * i]); PUT(41 + i, pa); PUT(46 + i, pb); });
    sfor<4, 8>([&](auto m) { cmul2(pa, zb[m], zp[8], pb, zb[m], zp[16]); PUT(52 + 3 * (m - 4), pa); PUT(53 + 3 * (m - 4), pb); });
#undef PUT
  }
  STAMP(2);
#pragma unroll
  for (int i = 0; i < 4; ++i) *(LAS v4u*)(rtw + 32 * i * 144) = wa[i];
#pragma unroll
  for (int i = 0; i < 2; ++i) *(LAS v4u*)(rtw + (L_RT2 - L_RT1) + 32 * i * 144) = wb[i];
  BAR();
  STAMP(3);
  v4u o1[6];
#pragma unroll
  for (int it = 0; it < 4; ++it) o1[it] = trfrag(rt1 + it * 4608, 4 * 144);
#pragma unroll
  for (int it = 0; it < 2; ++it) o1[4 + it] = trfrag(rt2 + it * 4608, 8 * 144);
  auto rt_st = [&](int it) {
    if (b < 224) {
      if (it < 4) __builtin_amdgcn_raw_buffer_store_b128(o1[it], rs, vo1, so1 + it * inc1, 16);
      else __builtin_amdgcn_raw_buffer_store_b128(o1[it], rs, vo2[it - 4], WS_WT2 + dgp * WJ, 16);
    }
  };
  {
    float ca = 0, cc = 0;
#pragma unroll
    for (int it = 0; it < 6; ++it) rt_st(it);
#pragma unroll
    for (int it = 0; it < 4; ++it) {
      ca = dot2bf(o1[it][0], 0x3f803f80u, ca); cc = dot2bf(o1[it][1], 0x3f803f80u, cc);
      ca = dot2bf(o1[it][2], 0x3f803f80u, ca); cc = dot2bf(o1[it][3], 0x3f803f80u, cc);
    }
    ca = sum32x(sum16x(ca + cc));
    if (lane < 16 && b < 224) __builtin_amdgcn_raw_buffer_store_b32(__float_as_uint(ca), rs, lane * 4, WS_CSUM + jj * 4096 + (rq * 224 + mat * 112 + ptw * 16) * 4, 16);
  }
  SB();
  STAMP(18);
  v4u g[8];
  u32 fl;
  auto gather = [&]() {
    sfor<0, 4>([&](auto i) { ld_vgpr<0>(g[i], srd, t16, WS_XG + pr * 16384 + i * 4096); });
    sfor<0, 4>([&](auto i) { ld_vgpr<0>(g[4 + i], srd, t16, WS_GRAN + xcd * 16384 + i * 4096); });
  };
  gather();
  STAMP(22);
  const u32 sW1 = WS_WT1 + j * WJ + w * WPT * 1024, sW2 = WS_WT1 + j * WJ + 4 * WPT * 1024;
  auto ld_w1 = [&](auto k) { ld_agpr<A_W + 4 * k, (k & 3) * 1024>(srd, lane16, sW1 + (k >> 2) * 4096); };
  {
    v4u Bf[8], Af[2][8];
#pragma unroll
    for (int ks = 0; ks < 8; ++ks) { Bf[ks] = *(LAS v4u*)(ap[3] + ks * 64); Af[0][ks] = *(LAS v4u*)(ap[0] + ks * 64); }
    SB();
    v4f d[4];
    sfor<0, 4>([&](auto mm) {
      if constexpr (mm == 2) {
    SB();
    WAITV(0);
    asm volatile("" :: "v"(pfv));
    if (lane == 0 && b < 224) __builtin_amdgcn_raw_buffer_store_b32(tag, rs, 0, WS_WFLAG + (b * 4 + w) * 128, 16);
    SB();
      }
      sfor<0, 8>([&](auto ks) {
        if constexpr (ks == 0) mfma16z(d[mm], Af[mm & 1][ks], Bf[ks]);
        else mfma16(d[mm], Af[mm & 1][ks], Bf[ks]);
        if constexpr (mm < 3) Af[(mm & 1) ^ 1][ks] = *(LAS v4u*)(ap[mm < 2 ? mm + 1 : 2] + (mm == 2 ? 6 * TCOL : 0) + ks * 64);
        SB();
      });
    });
    asm volatile("s_nop 7" : "+v"(d[0]), "+v"(d[1]), "+v"(d[2]), "+v"(d[3]));
    sfor<0, 4>([&](auto mm) {
      const u32 p0 = pkh(d[mm][3], d[mm][2]), p1 = pkh(d[mm][1], d[mm][0]);
#pragma unroll
      for (int e = 0; e < 4; ++e) {
        LAS char* k2 = kp + (3 - mm) * 1024 + e * (KC_B + 4);
        if (e & 1) *(LAS v2u*)k2 = v2u{p0, p1};
        else { *(LAS u32*)k2 = p0; *(LAS u32*)(k2 + 4) = p1; }
      }
      SB();
    });
  }
  STAMP(4);
  {
    waitv8<0>(g);
    for (int spin = 0; spin < SPIN_CAP; ++spin) {
      u32 df = 0;
#pragma unroll
      for (int i = 0; i < 8; ++i) df |= (g[i][1] ^ tag) | (g[i][3] ^ tag);
      asm volatile("" : "+v"(df));
      if (__all(df == 0)) break;
      COUNT(0);
      __builtin_amdgcn_s_sleep(4);
      gather();
      waitv8<0>(g);
      }
    STAMP(5);
    ld_flag(fl, srd, fo, WS_WFLAG);
    LAS char* cc[2] = {sm + t * 4, sm + CH_B + t * 4};
    asm volatile("" : "+v"(cc[1]));
    const u32 ub0 = t * 4, ub1 = CH_B + t * 4;
    sfor<0, 4>([&](auto i) {
      const float m0 = bflo(g[4 + i][0]), m1 = bflo(g[4 + i][2]);
      const float r0 = __builtin_amdgcn_rcpf(bfhi(g[4 + i][0])), r1 = __builtin_amdgcn_rcpf(bfhi(g[4 + i][2]));
#pragma unroll
      for (int c = 0; c < 2; ++c) {
        const u32 d = pkbf((c ? bfhi(g[i][0]) : bflo(g[i][0])) - m0, (c ? bfhi(g[i][2]) : bflo(g[i][2])) - m1);
        const u32 o = pkbf(bflo(d) * r0, bfhi(d) * r1);
        const u32 uh = pkh(bflo(o), bfhi(o));
        LAS char* c2 = cc[c] + i * 1024;
        *(LAS u32*)(c2 + OFF_U + UC_B + 112) = uh;
        dsw_halves<OFF_U + 114 + i * 1024>(c ? ub1 : ub0, uh);
        *(LAS u32*)(c2 + OFF_X) = __builtin_amdgcn_perm(g[i][2], g[i][0], c ? 0x07060302u : 0x05040100u);
      }
    });
  }
  STAMP(16);
  wait0_pin(fl);
  for (int spin = 0; spin < SPIN_CAP && !__all(fl == tag); ++spin) { __builtin_amdgcn_s_sleep(4); ld_flag(fl, srd, fo, WS_WFLAG); wait0_pin(fl); COUNT(1); }
  STAMP(17);
  sfor<0, 6>([&](auto k) { ld_w1(k); });
  BAR();
  if (b == 0 && t == 0) __builtin_amdgcn_raw_buffer_store_b32(tag, rs0, 0, WS_EPOCH, 16);
  STAMP(6);
  const u32 sWD = WS_WT2 + dg * WJ + w * WDW * 1024;
  auto pre_wd = [&](auto f) {
    if constexpr (f < 12) ld_agpr<A_ACC2 + 4 * f, (f & 3) * 1024>(srd, lane16, sWD + (e2 * 16 + (f & 12)) * 1024);
    else ld_agpr<240 + 4 * (f - 12), (f & 1) * 1024>(srd, lane16, sWD + 32768 + ((f - 12) >> 1) * 6144);
  };
  v4f acc[4];
  v2h uq[8];
  u32 xv[8], sk[8];
  const float dsk = bf2f(r_dsk);
  {
    auto extra = [&](auto r, auto m) {
      constexpr int sl = r * 36 + m;
      if constexpr (sl == 1 || sl == 36 || sl == 72 || sl == 108 || sl == 143) STAMP(sl == 1 ? 23 : sl == 36 ? 25 : sl == 72 ? 27 : sl == 108 ? 29 : 31);
      if constexpr (sl % 8 == 5 && sl < 56) {
        constexpr int i = sl / 8;
        __builtin_amdgcn_raw_buffer_store_b128(v4u{~0u, ~0u, ~0u, ~0u}, rs, lane16 + (i & 3) * 1024, WS_ACT + ((mt * 4 + w) * AT + j * 7 + (j >> 2) + (i & 4)) * 1024, 16);
      }
      if constexpr (r == 3 && m < 8) {
        uq[m] = *(LAS v2h*)(he[(m >> 1) & 1] + OFF_U + UC_B + 112 + (m >> 2) * 2048 + (m & 1) * 16);
        xv[m] = *(LAS u32*)(he[(m >> 1) & 1] + OFF_X + (m >> 2) * 2048 + (m & 1) * 16);
      }
      if constexpr (sl % 4 == 3 && sl < 128) {
        constexpr int k = 6 + sl / 4;
        if constexpr (k < 32) ld_w1(IC<k>{});
        else { set_m0(L_W2 + (w * 6 + k - 32) * 1024); asm volatile("s_nop 0"); dma16w(srd, lane16, sW2 + (w * 6 + k - 32) * 1024); }
      }
    };
    v4f ac[8];
    conv_main(ub, kb, ac, extra);
    LAS char* xp = cb + lane16;
    auto give = [&](auto N) {
      sfor<0, 4>([&](auto a) {
        *(LAS v4f*)(xp + (4 * N + a) * 1024) = ac[4 * (a >> 1) + ((a & 1) ? 2 + N : 1 - N)];
        acc[a] = ac[4 * (a >> 1) + ((a & 1) ? 3 - N : N)];
      });
    };
    if (nh == 0) give(IC<0>{}); else give(IC<1>{});
#pragma unroll
    for (int m = 0; m < 8; ++m) { sk[m] = pkbf((float)uq[m][0] * dsk, (float)uq[m][1] * dsk); asm volatile("" : "+v"(sk[m])); }
    BAR();
#pragma unroll
    for (int a = 0; a < 4; ++a) acc[a] += *(LAS v4f*)(xp + (4 * (1 - nh) + a) * 1024);
  }
  STAMP(7);
  {
#pragma unroll
    for (int m = 0; m < 8; ++m) {
      const u32 yb = pkbf(acc[m >> 1][2 * (m & 1)] * yscl, acc[m >> 1][2 * (m & 1) + 1] * yscl);
      const u32 zz = pkbf(bflo(yb) + bflo(sk[m]), bfhi(yb) + bfhi(sk[m]));
      *(LAS u32*)(he[(m >> 1) & 1] + OFF_X + (m >> 2) * 2048 + (m & 1) * 16) =
          min(pkbf(bflo(xv[m]) + bflo(zz), bfhi(xv[m]) + bfhi(zz)), 0xfffffffeu);
    }
    WAITV(19);
    WAITL0();
#pragma unroll
    for (int i = 0; i < 2; ++i)
      __builtin_amdgcn_raw_buffer_store_b128(*(LAS v4u*)(cb + OFF_X + ho + i * 2048), rs0, ho + i * 2048, WS_HT + ch * HROW, 16);
  }
  STAMP(21);
  sfor<0, 16>([&](auto f) { pre_wd(f); });
  BAR();
  STAMP(8);
  const u32 vtok = (lane >> 4) * HROW + (((lane & 15) ^ (4 * (lane >> 4))) * 16);
  const u32 stok = WS_HT + mt * 256 + w * 16 * HROW;
  auto tok_dma = [&](int c) {
#pragma unroll
    for (int i = 0; i < 4; ++i) dma16(srd0, vtok, stok + (64 * c + 4 * i) * HROW, c * 16384 + (4 * w + i) * 1024);
  };
  dma16(srd, lane16, WS_CSUM + j * 4096 + w * 1024, L_CSP + w * 1024);
#pragma unroll
  for (int c = 0; c < 3; ++c) tok_dma(c);
  LAS char* tp[4][2];
#pragma unroll
  for (int i = 0; i < 4; ++i) {
    tp[i][0] = sm + (8 * (G >> 1) + q4) * 256 + 8 * (p4 & 1) + (((4 * (w ^ i) + 2 * (G & 1) + (p4 >> 1)) ^ (4 * q4)) * 16);
    tp[i][1] = tp[i][0] + 65536;
    asm volatile("" : "+v"(tp[i][1]));
  }
  LAS char* vp[2];
  vp[0] = sm + w * 4096 + lane16; vp[1] = vp[0] + 65536;
  asm volatile("" : "+v"(vp[1]));
  v4u own[32], vq[4];
  u32 vm = 0;
  float sA = 0, sB = 0, qA = 0, qB = 0;
  auto mx = [&](u32 m, const v4u& v) { return max(max(m, max(v[0], v[1])), max(v[2], v[3])); };
  auto own_rd = [&](auto s) { own[s] = trfrag(tp[0][s >> 4] + (s & 15) * 4096, 1024); };
  auto own_acc = [&](auto s) {
#pragma unroll
    for (int e = 0; e < 4; e += 2) {
      sA = dot2bf(own[s][e], 0x3f803f80u, sA); qA = dot2bf(own[s][e], own[s][e], qA);
      sB = dot2bf(own[s][e + 1], 0x3f803f80u, sB); qB = dot2bf(own[s][e + 1], own[s][e + 1], qB);
    }
    pin2(sA, qA); pin2(sB, qB);
  };
  auto v_rd = [&](auto c, auto i) { vq[i] = *(LAS v4u*)(vp[c >> 2] + (c & 3) * 16384 + i * 1024); };
  auto fix = [&](auto c0, auto c1) {
    for (int spin = 0; spin < SPIN_CAP && __any(vm == ~0u); ++spin) {
      COUNT(2 + (c0 + 2) / 4);
#pragma unroll
      for (int c = c0; c < c1; ++c) tok_dma(c);
      WAITV(0);
      vm = 0;
      sfor<c0, c1>([&](auto c) {
        sfor<0, 4>([&](auto i) { v_rd(c, i); });
        sfor<0, 4>([&](auto i) { vm = mx(vm, vq[i]); });
      });
    }
    vm = 0;
  };
  STAMP(52);
  WAITV(8);
  STAMP(53);
  sfor<0, 4>([&](auto i) { v_rd(IC<0>{}, i); });
  sfor<0, 4>([&](auto i) { vm = mx(vm, vq[i]); });
  SB();
  WAITV(4);
  sfor<0, 4>([&](auto i) { v_rd(IC<1>{}, i); });
  sfor<0, 4>([&](auto i) { vm = mx(vm, vq[i]); });
  fix(IC<0>{}, IC<2>{});
  BAR();
  STAMP(9);
  auto w2_lds = [&](int k) { return (k / 3) * 16384 + (3 * w + k % 3) * 1024; };
  auto w2_src = [&](int k) { return sW2 + (2 + k / 3) * 12288 + (3 * w + k % 3) * 1024; };
  auto ld_wd = [&](auto f) { ld_agpr<A_W + 4 * f, (f & 3) * 1024>(srd, lane16, sWD + ((f < 16 ? e2 : 1 - e2) * 16 + (f & 12)) * 1024); };
  v4u hres[2];
  {
    v4u ot[2][3];
    LAS char* hp = sm + (64 * dg + 32 * (w >> 1) + 16 * (G >> 1) + q4) * 256 + 8 * (p4 & 1) +
                   (((4 * (2 * e2 + (w & 1)) + 2 * (G & 1) + (p4 >> 1)) ^ (4 * q4)) * 16);
    auto ot_rd = [&](auto s, auto i) { ot[s & 1][i] = trfrag(tp[i + 1][s >> 4] + (s & 15) * 4096, 1024); };
    own_rd(IC<0>{});
    sfor<0, 3>([&](auto i) { ot_rd(IC<0>{}, i); });
    if (t < 224) {
      LAS float* cp = (LAS float*)(sm + L_CSP) + t;
      *(LAS float*)(sm + L_CS + t * 4) = (cp[0] + cp[224]) + (cp[448] + cp[672]);
    }
    SB();
    sfor<0, 32>([&](auto s) {
      constexpr int vc = s == 2 ? 2 : s == 5 ? 3 : s == 9 ? 4 : s == 12 ? 5 : s == 17 ? 6 : s == 19 ? 7 : -1;
      constexpr int vy = s < 9 ? 3 : s < 17 ? 4 : 8;
      constexpr bool vmx = s == 3 || s == 6 || s == 10 || s == 13 || s == 18 || s == 20;
      constexpr bool two = (s >= 15 && s < 19) || (s >= 23 && s < 27);
      constexpr int o0 = s < 15 ? s + 1 : two ? (s < 19 ? 16 + 2 * (s - 15) : 24 + 2 * (s - 23)) : -1, o1 = two ? o0 + 1 : -1;
      constexpr int p0 = s < 15 ? (s / 3) * 4 + (s % 3 == 0 ? 0 : s % 3 + 1) : -1, p1 = (s < 15 && s % 3 == 0) ? p0 + 1 : -1;
      if constexpr (s == 16 || s == 20 || s == 23 || s == 24 || s == 27) STAMP(40 + s - 16);
      if constexpr (s == 5) sfor<0, 6>([&](auto k) { own_acc(k); });
      if constexpr (s == 12) sfor<6, 13>([&](auto k) { own_acc(k); });
      if constexpr (s == 20) sfor<13, 24>([&](auto k) { own_acc(k); });
      if constexpr (s == 28) sfor<24, 32>([&](auto k) { own_acc(k); });
      if constexpr (s == 20) STAMP(45);
      if constexpr (vc >= 0) WAITV(vy);
      if constexpr (p0 >= 0) set_m0((3 + p0 / 4) * 16384 + (4 * w + p0 % 4) * 1024);
      if constexpr (s >= 15) set_m0(w2_lds(s - 15));
      mfma_aav<A_ACC1, A_W + 4 * s, s == 0>(own[s]);
      if constexpr (s < 31) ot_rd(IC<s + 1>{}, IC<0>{});
      if constexpr (p0 >= 0) dma16n(srd0, vtok, stok + (64 * (3 + p0 / 4) + 4 * (p0 % 4)) * HROW);
      if constexpr (s >= 15) dma16w(srd, lane16, w2_src(s - 15));
      if constexpr (vmx) { vm = mx(vm, vq[0]); vm = mx(vm, vq[1]); }
      SB();
      mfma_aav<A_ACC1 + 16, A_W + 4 * s, s == 0>(ot[s & 1][0]);
      if constexpr (s < 31) ot_rd(IC<s + 1>{}, IC<1>{});
      if constexpr (o1 >= 0) own_rd(IC<o1>{});
      if constexpr (vmx) { vm = mx(vm, vq[2]); vm = mx(vm, vq[3]); }
      if constexpr (s >= 12 && s < 24) accmov4<A_W + 4 * (s - 12), A_ACC2 + 4 * (s - 12)>();
      if constexpr (p1 >= 0) set_m0((3 + p1 / 4) * 16384 + (4 * w + p1 % 4) * 1024);
      SB();
      mfma_aav<A_ACC1 + 32, A_W + 4 * s, s == 0>(ot[s & 1][1]);
      if constexpr (s < 31) ot_rd(IC<s + 1>{}, IC<2>{});
      if constexpr (p1 >= 0) dma16n(srd0, vtok, stok + (64 * (3 + p1 / 4) + 4 * (p1 % 4)) * HROW);
      if constexpr (s >= 15) ld_wd(IC<s - 3>{});
      if constexpr (vc >= 0) { v_rd(IC<vc>{}, IC<0>{}); v_rd(IC<vc>{}, IC<1>{}); }
      SB();
      mfma_aav<A_ACC1 + 48, A_W + 4 * s, s == 0>(ot[s & 1][2]);
      if constexpr (o0 >= 0) own_rd(IC<o0>{});
      if constexpr (vc >= 0) { v_rd(IC<vc>{}, IC<2>{}); v_rd(IC<vc>{}, IC<3>{}); }
      if constexpr (s == 1 || s == 7 || s == 16 || s == 23) {
        if ((dg >> 1) == (s + 1) / 8) { hres[0] = trfrag(hp, 1024); hres[1] = trfrag(hp + 2048, 1024); }
      }
      SB();
      if constexpr (s == 6) { fix(IC<2>{}, IC<4>{}); BAR(); STAMP(19); SB(); }
      if constexpr (s == 14) { fix(IC<4>{}, IC<6>{}); BAR(); STAMP(20); SB(); }
      if constexpr (s == 22) { fix(IC<6>{}, IC<8>{}); BAR(); SB(); }
      if constexpr (s == 29) {
        const float sa = sum32x(sA + sB), qa = sum32x(qA + qB);
        const float m = sa * (1.0f / D);
        const float var = fmaxf(qa * (1.0f / D) - m * m, 0.0f);
        const float sd = rbf(__builtin_sqrtf(rbf(rbf(var) + 1e-5f)));
        if (lane < 32) *(LAS v4f*)(sm + L_MR + (w * 32 + lane) * 16) = v4f{rbf(m), -1.44269504088896340736f / sd, sd * sd, 0.0f};
        SB();
      }
    });
  }
  WAITV(11);
  BAR();
  STAMP(10);
  const u32 sAct = WS_ACT + ((mt * 4) * AT + j * 7 + (j >> 2)) * 1024;
  const u32 sAin = WS_ACT + ((mt * 4 + 2 * e2) * AT + 29 * w) * 1024;
  v4u af[7][8];
  auto ld_af = [&](auto g, auto i) {
    if constexpr (g < 4) {
      constexpr int k = (i & 1) ^ (g & 1);
      ld_vgpr<(i & 1) * 1024>(af[g][i], srd, lane16, sAin + (k * AT + 7 * (i >> 1) + (g < 2 ? 2 * e2 : 2 - 2 * e2)) * 1024);
    } else {
      ld_vgpr<(g - 4) * 1024>(af[g][i], srd, lane16, sAin + ((i & 1) * AT + 7 * (i >> 1) + 4) * 1024);
    }
  };
  const int hh = lane >> 5;
  v4f mr[4], cs[4][4];
  auto cs_rd = [&](int k, int pt) {
    LAS char* cp = sm + L_CS + (pt * 16 + 4 * hh) * 4;
    cs[k][0] = *(LAS v4f*)cp; cs[k][1] = *(LAS v4f*)(cp + 32); cs[k][2] = *(LAS v4f*)(cp + 448); cs[k][3] = *(LAS v4f*)(cp + 480);
  };
  float ag[8], au[8], tg[8], tu[8], xe[8], pp[8], dn[8], rc[8], vv[8];
  v4u ob[2];
  auto s0 = [&](auto q) { constexpr int A = (q < 32 ? A_ACC1 + 16 * (q >> 3) : A_ACC2 + 16 * ((q >> 3) - 4)) + (q & 7); ag[q & 7] = accrd<A>(); au[q & 7] = accrd<A + 8>(); };
  auto s1 = [&](auto q) {
    constexpr int e = q & 7, U = q < 32 ? q >> 3 : 0, C = q < 32 ? 0 : (q >> 3) - 3;
    tg[e] = x_fnma(mr[U][0], cs[C][e >> 2][e & 3], ag[e]);
    tu[e] = x_fnma(mr[U][0], cs[C][2 + (e >> 2)][e & 3], au[e]);
    xe[e] = x_mul(tg[e], mr[U][1]);
  };
  auto s2a = [&](auto q) { constexpr int e = q & 7; xe[e] = x_exp(xe[e]); pp[e] = x_mul(tg[e], tu[e]); };
  auto s2b = [&](auto q) { constexpr int e = q & 7, U = q < 32 ? q >> 3 : 0; dn[e] = x_fma(xe[e], mr[U][2], mr[U][2]); };
  auto s3 = [&](auto q) { rc[q & 7] = x_rcp(dn[q & 7]); };
  auto s4a = [&](auto q) { constexpr int e = q & 7; vv[e] = x_mul(pp[e], rc[e]); };
  auto s4b = [&](auto q) { constexpr int e = q & 7; if constexpr (e & 1) ob[(q >> 3) & 1][e >> 1] = x_cap(x_pk(vv[e - 1], vv[e])); };
  auto s5 = [&](auto q) {
    constexpr int un = q >> 3;
    if constexpr ((q & 7) == 7)
      __builtin_amdgcn_raw_buffer_store_b128(ob[un & 1], rs, lane16, un < 4 ? sAct + ((w ^ un) * AT + w) * 1024 : sAct + (w * AT + un) * 1024, 16);
  };
  Sw st;
  auto breg = [](int o) { return o < 2 ? 240 + 8 * o : o < 10 ? 8 * (o - 2) : A_ACC2 + 8 * (o - 10); };
  auto wb_lds = [&](int x) { return x < 8 ? 98304 + w * 8192 + x * 1024 : x < 14 ? (x - 8) * 16384 + 12288 + w * 1024 : L_W2 + w * 6144 + (x - 14) * 1024; };
  {
    u32 wp[3] = {lane16, 65536 + lane16, L_W2 + lane16};
    asm volatile("" : "+v"(wp[1]), "+v"(wp[2]));
    v4u w2[3][3];
    auto w2_rd = [&](auto s, auto i) {
      constexpr int g = s >> 2;
      constexpr int off = (s & 3) * 3072 + i * 1024;
      if constexpr (g < 2) dsr128<g * 12288 + off>(w2[s % 3][i], wp[2]);
      else if constexpr (g < 6) dsr128<(g - 2) * 16384 + off>(w2[s % 3][i], wp[0]);
      else dsr128<(g - 6) * 16384 + off>(w2[s % 3][i], wp[1]);
    };
#pragma unroll
    for (int i = 0; i < 4; ++i) mr[i] = *(LAS v4f*)(sm + L_MR + ((w ^ i) * 32 + (lane & 31)) * 16);
    cs_rd(0, w);
    sfor<0, 3>([&](auto i) { w2_rd(IC<0>{}, i); });
    sfor<0, 3>([&](auto i) { w2_rd(IC<1>{}, i); });
    s0(IC<0>{}); s1(IC<0>{}); s2a(IC<0>{}); s2b(IC<0>{});
    st.dn = dn[0]; st.p = pp[0]; st.v = 0;
    dma16(srd, lane16, w2_src(17), w2_lds(17));
    SB();
    sfor<0, 32>([&](auto s) {
      if constexpr (s == 1 || s == 8 || s == 16 || s == 24) STAMP(24 + (s >> 2));
      if constexpr (s == 20) { WAITV(19); BARX(); }
      constexpr int q = s < 31 ? s + 1 : 31, e = q & 7, U = q >> 3;
      constexpr int r = s < 30 ? s + 2 : 31, g = r >> 2;
      constexpr int off = (g < 2 ? g * 12288 : g < 6 ? (g - 2) * 16384 : (g - 6) * 16384) + (r & 3) * 3072;
      constexpr int x = (s >= 3 && s < 17) ? s - 3 : (s >= 20 && s < 26) ? s - 6 : -1;
      if constexpr (x >= 0) set_m0(wb_lds(x));
      p2_a<s == 0, off>(w2[s % 3][0], w2[(s + 2) % 3], own[s], wp[g < 2 ? 2 : g < 6 ? 0 : 1]);
      if constexpr (s < 3) ld_wd(IC<29 + s>{});
      if constexpr (x >= 0) dma16w(srd, lane16, sWD + 32768 + (3 * ((2 + (x >> 1)) & 3) + ((2 + (x >> 1)) >> 2)) * 2048 + (x & 1) * 1024);
      if constexpr (s >= 24) { ld_af(IC<(s >= 28)>{}, IC<2 * (s & 3)>{}); ld_af(IC<(s >= 28)>{}, IC<2 * (s & 3) + 1>{}); }
      if constexpr (s > 0) s5(IC<s - 1>{});
      u32 o;
      p2_b<s == 0, (s & 1) != 0, A_ACC1 + 16 * U + e>(w2[s % 3][1], w2[s % 3][2], own[s], st, o,
                                                      mr[U][0], mr[U][1], mr[U][2], cs[0][e >> 2][e & 3], cs[0][2 + (e >> 2)][e & 3]);
      if constexpr (s & 1) ob[(s >> 3) & 1][(s & 7) >> 1] = o;
      if constexpr (s == 31) s5(s);
      SB();
    });
    waitl3<0>(w2[0]); waitl3<0>(w2[1]); waitl3<0>(w2[2]);
  }
  STAMP(11);
  cs_rd(1, 4); cs_rd(2, 5); cs_rd(3, 6);
  asm volatile("s_nop 7\n\ts_nop 3" ::: "memory");
  sfor<32, 40>([&](auto q) { s0(q); });
  sfor<32, 40>([&](auto q) { s1(q); });
  sfor<32, 40>([&](auto q) { s2a(q); });
  sfor<32, 40>([&](auto q) { s2b(q); });
  sfor<32, 40>([&](auto q) { s3(q); });
  sfor<32, 40>([&](auto q) { s4a(q); });
  sfor<32, 40>([&](auto q) { s4b(q); });
  s5(IC<39>{});
  SB();
  STAMP(12);
  u32 am = 0;
  auto af_fix = [&](auto g) {
    for (int spin = 0; spin < SPIN_CAP && __any(am == ~0u); ++spin) {
      COUNT(g < 2 ? 5 : g < 4 ? 6 : 7);
      sfor<0, 8>([&](auto i) { ld_af(g, i); });
      waitv8<0>(af[g]);
      am = 0;
      sfor<0, 8>([&](auto i) { x_max(am, af[g][i]); });
    }
    am = 0;
  };
  s0(IC<40>{}); s1(IC<40>{}); s2a(IC<40>{});
  waitv8<10>(af[0]);
  s2b(IC<40>{});
  st.dn = dn[0]; st.p = pp[0];
  sfor<0, 8>([&](auto i) { x_max(am, af[0][i]); });
  af_fix(IC<0>{});
  const u32 wbp[4] = {98304 + w * 8192 + lane16, 12288 + w * 1024 + lane16, 65536 + 12288 + w * 1024 + lane16, L_W2 + w * 6144 + lane16};
  auto wb_rd = [&](auto x) {
    dsr_agpr<breg(2 + (x >> 1)) + 4 * (x & 1), (x < 8 ? x * 1024 : x < 14 ? ((x - 8) & 3) * 16384 : (x - 14) * 1024)>(wbp[x < 8 ? 0 : x < 12 ? 1 : x < 14 ? 2 : 3]);
  };
  SB();
  STAMP(13);
  float hf[16];
  GAS u16* op = (GAS u16*)out + (size_t)(128 * mt + 64 * e2 + 32 * (w & 1) + (lane & 31)) * D + 64 * dg + 32 * (w >> 1) + 16 * hh;
  sfor<0, 6>([&](auto g) {
    constexpr int WV[6] = {10, 1, 9, 8, 4, 0};
    sfor<0, 16>([&](auto m) {
      if constexpr (m == 8 && g < 6) waitv8<WV[g < 6 ? g : 0]>(af[g < 6 ? g + 1 : 0]);
      if constexpr (g < 2) {
        if constexpr ((m & 1) == 0) {
          constexpr int i = m >> 1, k = (i & 1) ^ g, q = 40 + 8 * g + i, nq = q < 55 ? q + 1 : 55, e = nq & 7, C = (nq >> 3) - 3;
          u32 o;
          g2_pair<(g == 0 && m < 4), (i & 1) != 0, A_ACC2 + 16 * (C - 1) + e, A_ACC1 + 16 * k, A_W + 8 * i>(
              af[g][i], st, o, mr[0][0], mr[0][1], mr[0][2], cs[C][e >> 2][e & 3], cs[C][2 + (e >> 2)][e & 3]);
          if constexpr (i & 1) ob[(q >> 3) & 1][i >> 1] = o;
        }
      } else if constexpr (g < 4) {
        constexpr int i = m >> 1, dt = m & 1, k = (i & 1) ^ (g & 1);
        mfma_aav<A_ACC1 + 32 * dt + 16 * k, A_W + 64 + 8 * i + 4 * dt, false>(af[g][i]);
      } else {
        constexpr int jl = m >> 2, dt = (m >> 1) & 1, k = m & 1;
        mfma_aav<A_ACC1 + 32 * dt + 16 * k, breg(4 * (g - 4) + jl) + 4 * dt, false>(af[g][2 * jl + k]);
      }
      if constexpr (m >= 8 && g < 6) x_max(am, af[g < 6 ? g + 1 : 0][m - 8]);
      if constexpr (g == 0 && m < 8) ld_af(IC<2>{}, m);
      if constexpr (g == 1 && m < 4) wb_rd(IC<16 + m>{});
      if constexpr (g == 1 && m >= 8) ld_af(IC<3>{}, IC<m - 8>{});
      if constexpr (g == 2 && m < 8) ld_af(IC<4>{}, m);
      if constexpr (g == 2) wb_rd(m);
      if constexpr (g == 3 && m < 8) ld_af(IC<5>{}, m);
      if constexpr (g == 4 && m >= 4 && m < 12) ld_af(IC<6>{}, IC<m - 4>{});
      if constexpr (g < 2 && m == 15) s5(IC<47 + 8 * g>{});
      if constexpr (g == 5 && m < 8) { hf[2 * m] = bflo(hres[m >> 2][m & 3]); hf[2 * m + 1] = bfhi(hres[m >> 2][m & 3]); pin2(hf[2 * m], hf[2 * m + 1]); }
      if constexpr (g == 4 && m == 0) asm volatile("" : "+v"(op));
      SB();
    });
    if constexpr (g < 6) af_fix(IC<g + 1>{});
    if constexpr (g == 3) BAR();
    STAMP(32 + g);
    SB();
  });
  float a[16];
  auto xch = [&](auto W) {
    const u32 ad = W * 4096 + lane16;
    sfor<0, 16>([&](auto m) {
      constexpr int k = W ^ (((m >> 2) + 1) & 3), jl = m & 3, kp = W ^ (m >> 2);
      mfma_aav<A_ACC1 + 16 * k, breg(8 + jl) + 4 * (k >> 1), false>(af[6][2 * jl + (k & 1)]);
      if constexpr (m >= 4 && jl >= 2) {
        dsw_agpr<A_ACC1 + 16 * kp + 8 * (jl - 2), kp * 16384 + (jl - 2) * 2048>(ad);
        dsw_agpr<A_ACC1 + 16 * kp + 8 * (jl - 2) + 4, kp * 16384 + (jl - 2) * 2048 + 1024>(ad);
      }
      SB();
    });
    asm volatile("s_nop 7\n\ts_nop 7\n\ts_nop 3" ::: "memory");
    sfor<0, 16>([&](auto e) { a[e] = accrd<A_ACC1 + 16 * W + e>(); });
  };
  STAMP(14);
  if (w == 0) xch(IC<0>{}); else if (w == 1) xch(IC<1>{}); else if (w == 2) xch(IC<2>{}); else xch(IC<3>{});
  BAR();
#pragma unroll
  for (int k = 1; k < 4; ++k) {
#pragma unroll
    for (int e = 0; e < 4; ++e) {
      const v4f p = *(LAS v4f*)(sm + w * 16384 + (w ^ k) * 4096 + e * 1024 + lane16);
#pragma unroll
      for (int c = 0; c < 4; ++c) a[4 * e + c] += p[c];
    }
  }
  {
    u32 o[8];
#pragma unroll
    for (int e = 0; e < 8; ++e) {
      const u32 f = pkbf(a[2 * e], a[2 * e + 1]);
      o[e] = pkbf(hf[2 * e] + bflo(f), hf[2 * e + 1] + bfhi(f));
    }
    *(GAS v4u*)op = v4u{o[0], o[1], o[2], o[3]};
    *(GAS v4u*)(op + 8) = v4u{o[4], o[5], o[6], o[7]};
  }
  STAMP(15);
  asm volatile(".Lkf_end:");
}

extern "C" void kernel_launch(void* const* d_in, const int* in_sizes, int n_in, void* d_out, int out_size, void* d_ws, size_t ws_size,
                              hipStream_t stream) {
  (void)hipFuncSetAttribute((const void*)k_fused, hipFuncAttributeMaxDynamicSharedMemorySize, LDS_B);
  k_fused<<<256, 256, LDS_B, stream>>>((const u16*)d_in[0], (const u16*)d_in[1], (const u16*)d_in[2], (const u16*)d_in[3], (const u16*)d_in[4],
                                       (const u16*)d_in[5], (const u16*)d_in[6], (const u16*)d_in[8], (const u16*)d_in[9],
                                       (const u16*)d_in[10], (char*)d_ws, (u16*)d_out);
}
```

```cpp
#include <hip/hip_runtime.h>
#include <hip/hip_bf16.h>
#include <stdint.h>
#include <type_traits>

typedef unsigned short u16;
typedef unsigned int u32;
typedef unsigned long long u64;
typedef short v4s __attribute__((ext_vector_type(4)));
typedef __bf16 v2bf __attribute__((ext_vector_type(2)));
typedef _Float16 v2h __attribute__((ext_vector_type(2)));
typedef float v2f __attribute__((ext_vector_type(2)));
typedef float v4f __attribute__((ext_vector_type(4)));
typedef u32 v2u __attribute__((ext_vector_type(2)));
typedef u32 v4u __attribute__((ext_vector_type(4)));
#define LAS __attribute__((address_space(3)))
#define GAS __attribute__((address_space(1)))
#define DEV __device__ __forceinline__

constexpr int L = 2048, D = 512, NS = 128, F = 1792;

constexpr u32 WS_EPOCH = 0;
constexpr u32 WS_WFLAG = 512 << 10;
constexpr u32 WS_GRAN  = 64 << 10;
constexpr u32 WS_CSUM  = 256 << 10;
constexpr u32 WS_HT    = 1 << 20;
constexpr u32 WS_WT1   = 4 << 20;
constexpr u32 WS_WT2   = 8 << 20;
constexpr u32 WS_ACT   = 12 << 20;
constexpr u32 WS_XG    = 20 << 20;
constexpr u32 HROW = 4352, WJ = 233472 + 4096, WPT = 33, WDW = 57, AT = 117;
constexpr u32 WS_USED  = 24 << 20;
constexpr u32 WS_FIX   = 4 << 20;
constexpr size_t WS_ROT = 40 << 20;
constexpr size_t WS_DBG = 32 << 20;

template <int I> using IC = std::integral_constant<int, I>;
template <int B, int E, class Fn> DEV void sfor(Fn&& f) {
  if constexpr (B < E) { f(IC<B>{}); sfor<B + 1, E>(f); }
}

DEV float bf2f(u32 b) { return __uint_as_float(b << 16); }
DEV float bflo(u32 p) { return __uint_as_float(p << 16); }
DEV float bfhi(u32 p) { return __uint_as_float(p & 0xffff0000u); }
DEV u32 pkbf(float a, float b) { v2f f = {a, b}; return __builtin_bit_cast(u32, __builtin_convertvector(f, v2bf)); }
DEV u32 pkh(float a, float b) { v2f f = {a, b}; return __builtin_bit_cast(u32, __builtin_convertvector(f, v2h)); }
DEV float rbf(float a) { return bflo(pkbf(a, a)); }
DEV float dot2bf(u32 a, u32 b, float c) { return __builtin_amdgcn_fdot2_f32_bf16(__builtin_bit_cast(v2bf, a), __builtin_bit_cast(v2bf, b), c, false); }
DEV v2u trread(LAS char* p) { return __builtin_bit_cast(v2u, __builtin_amdgcn_ds_read_tr16_b64_v4i16((LAS v4s*)p)); }
DEV v4u trfrag(LAS char* p, int second) { const v2u a = trread(p), b = trread(p + second); return v4u{a[0], a[1], b[0], b[1]}; }
#define WAITV(n) asm volatile("s_waitcnt vmcnt(%0)" :: "i"(n) : "memory")
#define WAITL0() asm volatile("s_waitcnt lgkmcnt(0)" ::: "memory")
#define BAR() asm volatile("s_waitcnt lgkmcnt(0)\n\ts_barrier" ::: "memory")
#define BARX() asm volatile("s_barrier" ::: "memory")
#define SB() __builtin_amdgcn_sched_barrier(0)
#define WSC " sc1"
constexpr int SPIN_CAP = 1 << 20;

#define STAMP(slot) do {} while (0)
#define COUNT(slot) do {} while (0)

template <int CTRL> DEV float dpp(float v) { return __builtin_bit_cast(float, __builtin_amdgcn_update_dpp(0, __builtin_bit_cast(int, v), CTRL, 0xf, 0xf, true)); }
DEV float swap16_other(float v, float& o) { auto r = __builtin_amdgcn_permlane16_swap(__float_as_uint(v), __float_as_uint(v), false, false); o = __uint_as_float(r[1]); return __uint_as_float(r[0]); }
DEV float swap32_other(float v, float& o) { auto r = __builtin_amdgcn_permlane32_swap(__float_as_uint(v), __float_as_uint(v), false, false); o = __uint_as_float(r[1]); return __uint_as_float(r[0]); }
DEV float sum16x(float v) { float o; const float a = swap16_other(v, o); return a + o; }
DEV float sum32x(float v) { float o; const float a = swap32_other(v, o); return a + o; }
DEV float wave_sum(float v) {
  v += dpp<0xB1>(v); v += dpp<0x4E>(v); v += dpp<0x141>(v); v += dpp<0x140>(v);
  return sum32x(sum16x(v));
}
DEV float wave_max(float v) {
  v = fmaxf(v, dpp<0xB1>(v)); v = fmaxf(v, dpp<0x4E>(v)); v = fmaxf(v, dpp<0x141>(v)); v = fmaxf(v, dpp<0x140>(v));
  float o; float a = swap16_other(v, o); v = fmaxf(a, o); a = swap32_other(v, o); return fmaxf(a, o);
}

DEV void dma16(v4u srd, u32 voff, u32 soff, u32 lds) {
  asm volatile("s_mov_b32 m0, %3\n\ts_nop 0\n\tbuffer_load_dwordx4 %0, %1, %2 offen sc1 lds" :: "v"(voff), "s"(srd), "s"(soff), "s"(lds) : "memory");
}
template <int A, int IMM> DEV void ld_agpr(v4u srd, u32 voff, u32 soff) {
  asm volatile("buffer_load_dwordx4 a[%c3:%c4], %0, %1, %2 offen offset:%c5" WSC :: "v"(voff), "s"(srd), "s"(soff), "i"(A), "i"(A + 3), "i"(IMM) : "memory");
}
template <int IMM> DEV void ld_vgpr(v4u& d, v4u srd, u32 voff, u32 soff) {
  asm volatile("buffer_load_dwordx4 %0, %1, %2, %3 offen offset:%c4 sc1" : "=v"(d) : "v"(voff), "s"(srd), "s"(soff), "i"(IMM) : "memory");
}
template <int ACC, int A, bool FIRST> DEV void mfma_aav(const v4u& b) {
  if constexpr (FIRST) asm volatile("v_mfma_f32_32x32x16_bf16 a[%c1:%c2], a[%c3:%c4], %0, 0" :: "v"(b), "i"(ACC), "i"(ACC + 15), "i"(A), "i"(A + 3));
  else asm volatile("v_mfma_f32_32x32x16_bf16 a[%c1:%c2], a[%c3:%c4], %0, a[%c1:%c2]" :: "v"(b), "i"(ACC), "i"(ACC + 15), "i"(A), "i"(A + 3));
}
template <int ACC, bool FIRST> DEV void mfma_avv(const v4u& a, const v4u& b) {
  if constexpr (FIRST) asm volatile("v_mfma_f32_32x32x16_bf16 a[%c2:%c3], %0, %1, 0" :: "v"(a), "v"(b), "i"(ACC), "i"(ACC + 15));
  else asm volatile("v_mfma_f32_32x32x16_bf16 a[%c2:%c3], %0, %1, a[%c2:%c3]" :: "v"(a), "v"(b), "i"(ACC), "i"(ACC + 15));
}
template <int A, int OFF> DEV void dsw_agpr(u32 ad) {
  asm volatile("ds_write_b128 %0, a[%c1:%c2] offset:%c3" :: "v"(ad), "i"(A), "i"(A + 3), "i"(OFF) : "memory");
}
template <int OFF> DEV void dsw_halves(u32 ad, u32 v) {
  asm volatile("ds_write_b16 %0, %1 offset:%c2\n\tds_write_b16_d16_hi %0, %1 offset:%c3" :: "v"(ad), "v"(v), "i"(OFF), "i"(OFF + 2) : "memory");
}
DEV void ld_flag(u32& d, v4u srd, u32 voff, u32 soff) {
  asm volatile("buffer_load_dword %0, %1, %2, %3 offen sc1" : "=v"(d) : "v"(voff), "s"(srd), "s"(soff) : "memory");
}
DEV void wait0_pin(u32& d) { asm volatile("s_waitcnt vmcnt(0)" : "+v"(d) :: "memory"); }
DEV void pin2(float& a, float& b) { asm volatile("" : "+v"(a), "+v"(b)); }
template <int A> DEV float accrd() { float v; asm volatile("v_accvgpr_read_b32 %0, a%c1" : "=v"(v) : "i"(A)); return v; }
DEV float x_mul(float a, float b) { float d; asm volatile("v_mul_f32 %0, %1, %2" : "=v"(d) : "v"(a), "v"(b)); return d; }
DEV float x_fma(float a, float b, float c) { float d; asm volatile("v_fma_f32 %0, %1, %2, %3" : "=v"(d) : "v"(a), "v"(b), "v"(c)); return d; }
DEV float x_fnma(float a, float b, float c) { float d; asm volatile("v_fma_f32 %0, -%1, %2, %3" : "=v"(d) : "v"(a), "v"(b), "v"(c)); return d; }
DEV float x_exp(float a) { float d; asm volatile("v_exp_f32 %0, %1" : "=v"(d) : "v"(a)); return d; }
DEV float x_rcp(float a) { float d; asm volatile("v_rcp_f32 %0, %1" : "=v"(d) : "v"(a)); return d; }
DEV u32 x_pk(float a, float b) { u32 d; asm volatile("v_cvt_pk_bf16_f32 %0, %1, %2" : "=v"(d) : "v"(a), "v"(b)); return d; }
DEV u32 x_cap(u32 a) { u32 d; asm volatile("v_min_u32 %0, -2, %1" : "=v"(d) : "v"(a)); return d; }
DEV void x_max(u32& m, const v4u& v) { asm volatile("v_max3_u32 %0, %0, %1, %2\n\tv_max3_u32 %0, %0, %3, %4" : "+v"(m) : "v"(v[0]), "v"(v[1]), "v"(v[2]), "v"(v[3])); }
template <int OFF> DEV void dsr128(v4u& d, u32 ad) { asm volatile("ds_read_b128 %0, %1 offset:%c2" : "=v"(d) : "v"(ad), "i"(OFF) : "memory"); }
template <int N> DEV void waitl3(v4u (&a)[3]) { asm volatile("s_waitcnt lgkmcnt(%c3)" : "+v"(a[0]), "+v"(a[1]), "+v"(a[2]) : "i"(N) : "memory"); }
template <int N> DEV void waitv8(v4u (&a)[8]) {
  asm volatile("s_waitcnt vmcnt(%c8)" : "+v"(a[0]), "+v"(a[1]), "+v"(a[2]), "+v"(a[3]), "+v"(a[4]), "+v"(a[5]), "+v"(a[6]), "+v"(a[7]) : "i"(N) : "memory");
}
struct Sw { float dn, p, v; };
#define SW_A "v_rcp_f32 %[rc], %[dn]\n\tv_accvgpr_read_b32 %[ag], a%c[q]\n\tv_accvgpr_read_b32 %[au], a%c[q8]\n\t"
#define SW_B "v_fma_f32 %[ag], -%[m], %[cg], %[ag]\n\tv_fma_f32 %[au], -%[m], %[cu], %[au]\n\tv_mul_f32 %[rc], %[p], %[rc]\n\tv_mul_f32 %[dn], %[ag], %[rl]\n\t"
#define SW_C0 "v_exp_f32 %[dn], %[dn]\n\tv_mul_f32 %[p], %[ag], %[au]\n\tv_mov_b32 %[v], %[rc]\n\tv_fma_f32 %[dn], %[dn], %[s2], %[s2]"
#define SW_C1 "v_exp_f32 %[dn], %[dn]\n\tv_mul_f32 %[p], %[ag], %[au]\n\tv_cvt_pk_bf16_f32 %[o], %[v], %[rc]\n\tv_fma_f32 %[dn], %[dn], %[s2], %[s2]\n\tv_min_u32 %[o], -2, %[o]"
template <bool FIRST, int OFF> DEV void p2_a(const v4u& c0, v4u (&n)[3], const v4u& b, u32 ad) {
#define P2A(Z) asm volatile("s_waitcnt lgkmcnt(3)\n\tv_mfma_f32_32x32x16_bf16 a[0:15], %[w0], %[b], " Z "\n\t" \
    "ds_read_b128 %[n0], %[ad] offset:%c[of]\n\tds_read_b128 %[n1], %[ad] offset:%c[of]+1024\n\tds_read_b128 %[n2], %[ad] offset:%c[of]+2048" \
    : [n0] "=&v"(n[0]), [n1] "=&v"(n[1]), [n2] "=&v"(n[2]) : [w0] "v"(c0), [b] "v"(b), [ad] "v"(ad), [of] "i"(OFF) : "memory")
  if constexpr (FIRST) P2A("0"); else P2A("a[0:15]");
}
template <bool FIRST, bool ODD, int Q>
DEV void p2_b(const v4u& c1, const v4u& c2, const v4u& b, Sw& st, u32& o, float m, float rl, float s2, float cg, float cu) {
  float ag, au, t0;
#define P2B(Z1, Z2, RC, C) asm volatile("v_accvgpr_read_b32 %[ag], a%c[q]\n\tv_accvgpr_read_b32 %[au], a%c[q8]\n\t" \
    "v_mfma_f32_32x32x16_bf16 a[16:31], %[w1], %[b], " Z1 "\n\tv_rcp_f32 " RC ", %[dn]\n\t" \
    "v_fma_f32 %[ag], -%[m], %[cg], %[ag]\n\tv_fma_f32 %[au], -%[m], %[cu], %[au]\n\tv_mul_f32 %[dn], %[ag], %[rl]\n\tv_mul_f32 " RC ", %[p], " RC "\n\t" \
    "v_mfma_f32_32x32x16_bf16 a[32:47], %[w2], %[b], " Z2 "\n\tv_exp_f32 %[dn], %[dn]\n\tv_mul_f32 %[p], %[ag], %[au]\n\t" C \
    : [dn] "+v"(st.dn), [p] "+v"(st.p), [v] "+v"(st.v), [o] "=&v"(o), [t0] "=&v"(t0), [ag] "=&v"(ag), [au] "=&v"(au) \
    : [w1] "v"(c1), [w2] "v"(c2), [b] "v"(b), [m] "v"(m), [rl] "v"(rl), [s2] "v"(s2), [cg] "v"(cg), [cu] "v"(cu), [q] "i"(Q), [q8] "i"(Q + 8))
#define P2_EVEN "v_fma_f32 %[dn], %[dn], %[s2], %[s2]"
#define P2_ODD "v_cvt_pk_bf16_f32 %[o], %[v], %[t0]\n\tv_fma_f32 %[dn], %[dn], %[s2], %[s2]\n\tv_min_u32 %[o], -2, %[o]"
  if constexpr (FIRST) P2B("0", "0", "%[v]", P2_EVEN);
  else if constexpr (ODD) P2B("a[16:31]", "a[32:47]", "%[t0]", P2_ODD);
  else P2B("a[16:31]", "a[32:47]", "%[v]", P2_EVEN);
}
DEV void set_m0(u32 lds) { asm volatile("s_mov_b32 m0, %0" :: "s"(lds) : "memory"); }
DEV void dma16w(v4u srd, u32 voff, u32 soff) { asm volatile("buffer_load_dwordx4 %0, %1, %2 offen" WSC " lds" :: "v"(voff), "s"(srd), "s"(soff) : "memory"); }
DEV void dma16n(v4u srd, u32 voff, u32 soff) { asm volatile("buffer_load_dwordx4 %0, %1, %2 offen sc1 lds" :: "v"(voff), "s"(srd), "s"(soff) : "memory"); }
#define G2_TXT(Z0, Z1, C) \
  "v_mfma_f32_32x32x16_bf16 a[%c[d0]:%c[d0]+15], a[%c[wa]:%c[wa]+3], %[b], " Z0 "\n\t" SW_A SW_B \
  "v_mfma_f32_32x32x16_bf16 a[%c[d0]+32:%c[d0]+47], a[%c[wa]+4:%c[wa]+7], %[b], " Z1 "\n\t" C
#define G2_OPS : [dn] "+v"(st.dn), [p] "+v"(st.p), [v] "+v"(st.v), [o] "=&v"(o), [rc] "=&v"(rc), [ag] "=&v"(ag), [au] "=&v"(au) \
  : [b] "v"(b), [m] "v"(m), [rl] "v"(rl), [s2] "v"(s2), [cg] "v"(cg), [cu] "v"(cu), [q] "i"(Q), [q8] "i"(Q + 8), [d0] "i"(D0), [wa] "i"(WA)
template <bool FIRST, bool ODD, int Q, int D0, int WA>
DEV void g2_pair(const v4u& b, Sw& st, u32& o, float m, float rl, float s2, float cg, float cu) {
  float rc, ag, au;
  if constexpr (FIRST && ODD) asm volatile(G2_TXT("0", "0", SW_C1) G2_OPS);
  else if constexpr (FIRST) asm volatile(G2_TXT("0", "0", SW_C0) G2_OPS);
  else if constexpr (ODD) asm volatile(G2_TXT("a[%c[d0]:%c[d0]+15]", "a[%c[d0]+32:%c[d0]+47]", SW_C1) G2_OPS);
  else asm volatile(G2_TXT("a[%c[d0]:%c[d0]+15]", "a[%c[d0]+32:%c[d0]+47]", SW_C0) G2_OPS);
}
template <int A, int OFF> DEV void dsr_agpr(u32 ad) { asm volatile("ds_read_b128 a[%c1:%c2], %0 offset:%c3" :: "v"(ad), "i"(A), "i"(A + 3), "i"(OFF) : "memory"); }
DEV void mfma16(v4f& c, const v4u& a, const v4u& b) { asm volatile("v_mfma_f32_16x16x32_f16 %0, %1, %2, %0" : "+v"(c) : "v"(a), "v"(b)); }
DEV void mfma16z(v4f& c, const v4u& a, const v4u& b) { asm volatile("v_mfma_f32_16x16x32_f16 %0, %1, %2, 0" : "=v"(c) : "v"(a), "v"(b)); }
constexpr int A_W = 0, A_ACC1 = 128, A_ACC2 = 0, A_PRE = 192;

constexpr int TCOL = 528, NCOL = 63, TAB_B = NCOL * TCOL;
constexpr int KC_B = 4640, UC_B = 4224;
constexpr int OFF_K = TAB_B, OFF_U = OFF_K + 4 * KC_B, OFF_X = OFF_U + 2 * UC_B, CH_B = OFF_X + 4096;
constexpr int L_RT1 = 2 * CH_B, L_RT2 = L_RT1 + 128 * 144, L_MISC = L_RT2 + 64 * 144;
constexpr int L_W2 = 131072;
constexpr int L_CSP = L_MISC, L_CS = L_CSP + 4096, L_MR = L_CS + 1024, LDS_B = L_MR + 2048;
static_assert(L_W2 >= L_RT1 && L_W2 + 24576 <= L_MISC && LDS_B <= 163840 && L_MISC % 16 == 0, "lds map");

typedef v2f cf;
DEV void cmul2(cf& r0, cf a0, cf b0, cf& r1, cf a1, cf b1) {
  asm("v_pk_mul_f32 %0, %2, %3 op_sel_hi:[0,1]\n\t"
      "v_pk_mul_f32 %1, %4, %5 op_sel_hi:[0,1]\n\t"
      "v_pk_fma_f32 %0, %2, %3, %0 op_sel:[1,1,0] op_sel_hi:[1,0,1] neg_lo:[1,0,0]\n\t"
      "v_pk_fma_f32 %1, %4, %5, %1 op_sel:[1,1,0] op_sel_hi:[1,0,1] neg_lo:[1,0,0]"
      : "=&v"(r0), "=&v"(r1) : "v"(a0), "v"(b0), "v"(a1), "v"(b1));
}
DEV cf cexp_p(float ar, float ai, float p) {
  const float e = __builtin_amdgcn_exp2f(ar * p * 1.44269504088896340736f);
  const float xx = ai * p;
  const float chi = 0.15915493667125702f, clo = 6.4206383e-09f;
  const float th = xx * chi;
  float tl = __builtin_fmaf(xx, chi, -th);
  tl = __builtin_fmaf(xx, clo, tl);
  const float fr = (th - __builtin_rintf(th)) + tl;
  return cf{e * __builtin_amdgcn_cosf(fr), e * __builtin_amdgcn_sinf(fr)};
}
template <int CTRL> DEV u32 dppu(u32 v) { return (u32)__builtin_amdgcn_update_dpp(0, (int)v, CTRL, 0xf, 0xf, true); }
DEV u32 wave_umax(u32 v) {
  v = max(v, dppu<0xB1>(v)); v = max(v, dppu<0x4E>(v)); v = max(v, dppu<0x141>(v)); v = max(v, dppu<0x140>(v));
  auto r = __builtin_amdgcn_permlane16_swap(v, v, false, false); v = max(r[0], r[1]);
  auto q = __builtin_amdgcn_permlane32_swap(v, v, false, false); return max(q[0], q[1]);
}

struct ConvSched { int mc[36], mq[36], need[36], ru[16], ri[16]; };
constexpr ConvSched mk_sched() {
  ConvSched s{};
  bool hu[8] = {}, hk[8] = {};
  int nm = 0, nr = 0;
  for (int q = 0; q < 8; ++q)
    for (int c = 7; c >= q; --c) {
      if (!hu[q]) { hu[q] = true; s.ru[nr] = 1; s.ri[nr] = q; ++nr; }
      if (!hk[c - q]) { hk[c - q] = true; s.ru[nr] = 0; s.ri[nr] = c - q; ++nr; }
      s.mc[nm] = c; s.mq[nm] = q; s.need[nm] = nr; ++nm;
    }
  return s;
}
constexpr ConvSched CSCH = mk_sched();

template <class Ex> DEV void conv_main(u32 ub, u32 kb, v4f (&acc)[8], Ex&& extra) {
  v4u U[2][8], K[2][8];
  auto rd = [&](auto r, auto i) {
    if constexpr (CSCH.ru[i]) dsr128<64 * r + 512 * CSCH.ri[i]>(U[r & 1][CSCH.ri[i]], ub);
    else dsr128<64 * r + 512 * (7 - CSCH.ri[i])>(K[r & 1][CSCH.ri[i]], kb);
  };
  sfor<0, 16>([&](auto i) { rd(IC<0>{}, i); });
  SB();
  sfor<0, 4>([&](auto r) {
    constexpr int cur = r & 1;
    sfor<0, 36>([&](auto m) {
      if constexpr (m % 3 == 0) {
        constexpr int n = 16 - CSCH.need[m + 2] + (r < 3 ? (m < 32 ? (m + 1) / 2 : 16) : 0);
        if constexpr (m == 0 || CSCH.need[m + 2] > CSCH.need[m > 0 ? m - 1 : 0]) asm volatile("s_waitcnt lgkmcnt(%c0)" :: "i"(n < 15 ? n : 15) : "memory");
      }
      constexpr int c = CSCH.mc[m], q = CSCH.mq[m];
      if constexpr (r == 0 && q == 0) mfma16z(acc[c], U[cur][q], K[cur][c - q]);
      else mfma16(acc[c], U[cur][q], K[cur][c - q]);
      if constexpr (r < 3 && m < 32 && m % 2 == 0) rd(IC<r + 1>{}, IC<m / 2>{});
      extra(r, m);
      SB();
    });
  });
  asm volatile("s_nop 7" : "+v"(acc[0]), "+v"(acc[1]), "+v"(acc[2]), "+v"(acc[3]), "+v"(acc[4]), "+v"(acc[5]), "+v"(acc[6]), "+v"(acc[7]));
}

__global__ __launch_bounds__(256, 1) void k_fused(const u16* __restrict__ x, const u16* __restrict__ lar_, const u16* __restrict__ aim_,
                                                  const u16* __restrict__ cre_, const u16* __restrict__ cim_, const u16* __restrict__ ldt_,
                                                  const u16* __restrict__ dsk_, const u16* __restrict__ wg, const u16* __restrict__ wu,
                                                  const u16* __restrict__ wd, char* __restrict__ ws, u16* __restrict__ out) {
  extern __shared__ __attribute__((aligned(16))) char smx[];
  LAS char* sm = (LAS char*)smx;
  asm volatile("" ::: "a0", "a255");
  const int t = threadIdx.x, lane = t & 63, w = __builtin_amdgcn_readfirstlane(t >> 6), b = blockIdx.x;
  STAMP(0);
  const int xcd = b & 7, bi = b >> 3;
  u32 pfv;
  {
    u64 pc; u32 len;
    asm volatile("s_getpc_b64 %0\n.Lkf_here:\n\ts_mov_b32 %1, .Lkf_end-.Lkf_here" : "=s"(pc), "=s"(len));
    const u32 off = min((u32)((bi * 32 + (t & 31)) * 64), len);
    asm volatile("global_load_dword %0, %1, %2" : "=v"(pfv) : "v"(off), "s"(pc));
  }
  SB();
  const auto rs0 = __builtin_amdgcn_make_buffer_rsrc(ws, 0, WS_FIX, 0x00020000);
  const v4u srd0 = {(u32)(uintptr_t)ws, (u32)((uintptr_t)ws >> 32) & 0xffffu, WS_FIX, 0x00020000u};
  const int pr = xcd * 32 + bi;
  const int cw = w >> 1, nh = w & 1, ch = 2 * pr + cw;
  const int mt = 4 * (xcd >> 1) + (bi & 3), j = 8 * (xcd & 1) + (bi >> 2);
  const int e2 = j & 1, dg = j >> 1;
  const int G = lane >> 4, q4 = (lane >> 2) & 3, p4 = lane & 3, i16 = lane & 15;
  const u32 lane16 = lane * 16;
  LAS char* cb = sm + cw * CH_B;

  v4u xa[2];
#pragma unroll
  for (int i = 0; i < 2; ++i) xa[i] = *(const v4u*)(x + (size_t)(8 * b + 2 * w + i) * D + lane * 8);
  u32 xq[4][2];
#pragma unroll
  for (int i = 0; i < 4; ++i)
#pragma unroll
    for (int k = 0; k < 2; ++k) xq[i][k] = *(const u32*)(x + (size_t)(8 * b + 2 * (lane & 3) + k) * D + 2 * (64 * w + 16 * i + (lane >> 2)));
  const u32 ep = __builtin_amdgcn_raw_buffer_load_b32(rs0, WS_EPOCH, 0, 16);
  const int n = 64 * nh + lane;
  const u32 r_lar = lar_[ch * NS + n], r_aim = aim_[ch * NS + n], r_cre = cre_[ch * NS + n], r_cim = cim_[ch * NS + n];
  const u32 r_cre2 = cre_[ch * NS + (n ^ 64)], r_cim2 = cim_[ch * NS + (n ^ 64)];
  const u32 r_ldt = ldt_[ch], r_dsk = dsk_[ch];
  SB();
  {
    const v4u s = {~0u, ~0u, ~0u, ~0u};
#pragma unroll
    for (int i = 0; i < 2; ++i) __builtin_amdgcn_raw_buffer_store_b128(s, rs0, t * 16 + i * HROW, WS_HT + pr * 2 * HROW, 16);
  }
  SB();
  const int rb = b < 224 ? b : 223;
  const int mat = rb / 112, cg = (rb % 112) % 28, rq = (rb % 112) / 28, dgp = rb & 7, fb = rb >> 3;
  SB();
  {
    const v4u z = {0, 0, 0, 0};
    const int tt = t & 127;
    LAS char* c2 = sm + (t >> 7) * CH_B;
    *(LAS v4u*)(c2 + OFF_K + (tt / 33) * KC_B + (255 + tt % 33) * 16) = z;
    if (tt < 4) *(LAS v4u*)(c2 + OFF_K + 3 * KC_B + (255 + 29 + tt) * 16) = z;
    if (tt < 16) *(LAS v4u*)(c2 + OFF_U + (tt >> 3) * UC_B + (tt & 7) * 16) = z;
  }
  const int g16 = 4 * cg + w, jj = g16 / 7, ptw = g16 % 7, hb = G & 1;
  LAS char* rt1 = sm + L_RT1 + (16 * (G >> 1) + 8 * hb + q4) * 144 + 32 * w + 8 * p4;
  LAS char* rt2 = sm + L_RT2 + (16 * (G >> 1) + 4 * hb + q4) * 144 + 32 * w + 8 * p4;
  LAS char* rtw = sm + L_RT1 + (t >> 3) * 144 + (t & 7) * 16;
  u32 vo1 = ((ptw < 4 ? 1 : 3) * (8 * rq + (G >> 1)) + (ptw < 4 ? ptw * WPT : 4 * WPT - 4 + ptw)) * 1024 + (32 * hb + 16 * mat + i16) * 16, vo2[2];
  const u32 so1 = WS_WT1 + jj * WJ, inc1 = ptw < 4 ? 2048 : 6144;
#pragma unroll
  for (int it = 0; it < 2; ++it) {
    const int sx = 4 * fb + 2 * it + (G >> 1), jp = sx / 7, pp = sx % 7;
    const int pos = pp < 4 ? (pp >> 1) * 8 + (jp & 3) * 2 + (pp & 1) : 16 + (jp & 3) * 3 + (pp - 4);
    vo2[it] = ((jp >> 2) * WDW + pos * 2 + (w >> 1)) * 1024 + (32 * hb + (i16 & 3) + 8 * (i16 >> 2) + 4 * (w & 1)) * 16;
  }
  LAS char* ap[4];
  {
    const int c2 = (i16 + ((i16 >> 1) & 1)) >> 1, c4 = (i16 + 1 + ((i16 >> 2) & 1)) >> 2, c8 = (i16 + 3 + ((i16 >> 3) & 1)) >> 3;
    ap[0] = cb + (nh ? 32 + c2 : 16 + i16) * TCOL + G * 16;
    ap[1] = cb + (41 + 5 * nh + c4) * TCOL + G * 16;
    ap[2] = cb + (51 + 3 * nh + c8) * TCOL + G * 16;
    ap[3] = cb + i16 * TCOL + G * 16;
  }
  LAS char* kp = cb + OFF_K + 16 + (2038 - 256 * nh - 16 * i16 - 4 * G - 3 * 512) * 2;
  u32 ub = cw * CH_B + OFF_U + (i16 & 1) * UC_B + ((i16 >> 1) + G) * 16 + 256 * nh;
  u32 kb = cw * CH_B + OFF_K + (i16 & 3) * KC_B + 16 + (248 - 8 * (i16 >> 2) + G) * 16 - 7 * 512 + 256 * nh;
  u32 fo;
  {
    const int gq = lane < 8 ? 7 * j + w : 7 * j + 4 + ((lane - 8) >> 3), k = lane < 60 ? lane - 32 : 0;
    fo = (lane < 32 ? ((lane & 1) * 112 + ((lane >> 1) & 3) * 28 + (gq >> 2)) * 4 + (gq & 3) : ((7 * w + (k >> 2)) * 8 + dg) * 4 + (k & 3)) * 128;
  }
  LAS char* he[2];
  he[0] = cb + (16 * G + 2 * (i16 & 3) + 64 * (i16 >> 2) + 256 * nh) * 2; he[1] = he[0] + (3 - 2 * nh) * 512;
  u32 ho = (256 * ((lane >> 5) ? 3 - nh : nh) + (lane & 31) * 8) * 2;
  u32 t16 = t * 16;
  asm volatile("" : "+v"(rt1), "+v"(rt2), "+v"(rtw), "+v"(vo1), "+v"(vo2[0]), "+v"(vo2[1]), "+v"(ap[0]), "+v"(ap[1]), "+v"(ap[2]), "+v"(ap[3]),
               "+v"(kp), "+v"(ub), "+v"(kb), "+v"(fo), "+v"(he[0]), "+v"(he[1]), "+v"(ho), "+v"(t16));
  SB();
  const u32 tag = __builtin_amdgcn_readfirstlane(ep) + 1u;
  char* const wr = ws + WS_ROT + (size_t)(tag & 3u) * WS_USED;
  const auto rs = __builtin_amdgcn_make_buffer_rsrc(wr, 0, WS_USED, 0x00020000);
  const v4u srd = {(u32)(uintptr_t)wr, (u32)((uintptr_t)wr >> 32) & 0xffffu, WS_USED, 0x00020000u};
#pragma unroll
  for (int i = 0; i < 4; ++i)
    __builtin_amdgcn_raw_buffer_store_b128(v4u{xq[i][0], tag, xq[i][1], tag}, rs, (16 * i + (lane >> 2)) * 16384 + (lane & 3) * 16, WS_XG + w * (64 * 16384) + b * 64, 16);
  SB();
  v4u wa[4], wb[2];
  {
    const u16* W = mat ? wu : wg;
#pragma unroll
    for (int i = 0; i < 4; ++i) wa[i] = *(const v4u*)(W + (size_t)(128 * rq + (t >> 3) + 32 * i) * F + 64 * cg + (t & 7) * 8);
#pragma unroll
    for (int i = 0; i < 2; ++i) wb[i] = *(const v4u*)(wd + (size_t)(64 * fb + (t >> 3) + 32 * i) * D + 64 * dgp + (t & 7) * 8);
  }
  SB();
  {
    float v[2][8], s[2], q[2];
#pragma unroll
    for (int i = 0; i < 2; ++i) {
#pragma unroll
      for (int k = 0; k < 4; ++k) { v[i][2 * k] = bflo(xa[i][k]); v[i][2 * k + 1] = bfhi(xa[i][k]); }
      s[i] = ((v[i][0] + v[i][1]) + (v[i][2] + v[i][3])) + ((v[i][4] + v[i][5]) + (v[i][6] + v[i][7]));
    }
#pragma unroll
    for (int i = 0; i < 2; ++i) s[i] = wave_sum(s[i]) * (1.0f / D);
#pragma unroll
    for (int i = 0; i < 2; ++i) {
      q[i] = 0;
#pragma unroll
      for (int k = 0; k < 8; ++k) { const float d = v[i][k] - s[i]; q[i] += d * d; }
    }
#pragma unroll
    for (int i = 0; i < 2; ++i) q[i] = wave_sum(q[i]) * (1.0f / D);
    const float m = (lane & 1) ? s[1] : s[0], vv = (lane & 1) ? q[1] : q[0];
    const u32 pk = pkbf(m, __builtin_sqrtf(rbf(rbf(vv) + 1e-5f)));
    SB();
    WAITV(10);
    if (lane < 16) __builtin_amdgcn_raw_buffer_store_b64(v2u{pk, tag}, rs, (lane >> 1) * 16384 + (lane & 1) * 8, WS_GRAN + (8 * b + 2 * w) * 8, 16);
    SB();
  }
  STAMP(1);
  const float dt = rbf(expf(bf2f(r_ldt)));
  const float ar = -rbf(expf(bf2f(r_lar))) * dt, ai = bf2f(r_aim) * dt;
  int ex = (int)(wave_umax(max(max(r_cre & 0x7fffu, r_cim & 0x7fffu), max(r_cre2 & 0x7fffu, r_cim2 & 0x7fffu))) >> 7);
  ex = ex < 8 ? 8 : (ex > 250 ? 250 : ex);
  const float scl = __uint_as_float((u32)(257 - ex) << 23);
  const float yscl = 2.0f * dt * __uint_as_float((u32)(ex - 3) << 23);
  {
    LAS char* tb = cb + n * 4;
#define PUT(col, v) *(LAS u32*)(tb + (col) * TCOL) = pkh((v)[0], (v)[1])
    cf zp[17], zb[8], a[16], pa, pb;
    zp[1] = cexp_p(ar, ai, 1.0f); zp[16] = cexp_p(ar, ai, 16.0f); zb[1] = cexp_p(ar, ai, 256.0f);
    const cf wc = {zp[16][0], -zp[16][1]};
    a[0] = cf{bf2f(r_cre) * scl, -bf2f(r_cim) * scl};
    *(LAS u32*)(tb + 16 * TCOL) = 0x00003c00u;
    PUT(17, zp[1]); PUT(32, zb[1]); PUT(0, a[0]);
    cmul2(zp[2], zp[1], zp[1], zb[2], zb[1], zb[1]);
    cmul2(zp[3], zp[2], zp[1], zp[4], zp[2], zp[2]);
    cmul2(zb[3], zb[2], zb[1], zb[4], zb[2], zb[2]);
    cmul2(a[1], a[0], wc, zp[8], zp[4], zp[4]);
    PUT(18, zp[2]); PUT(19, zp[3]); PUT(20, zp[4]); PUT(41, zb[2]); PUT(46, zb[3]); PUT(51, zb[4]);
    cmul2(a[2], a[1], wc, zp[5], zp[4], zp[1]);
    cmul2(a[3], a[2], wc, zp[6], zp[4], zp[2]);
    cmul2(a[4], a[3], wc, zp[7], zp[4], zp[3]);
    PUT(1, a[1]); PUT(2, a[2]); PUT(3, a[3]); PUT(4, a[4]); PUT(21, zp[5]); PUT(22, zp[6]); PUT(23, zp[7]); PUT(24, zp[8]);
    sfor<9, 16>([&](auto i) { cmul2(a[i - 4], a[i - 5], wc, zp[i], zp[8], zp[i - 8]); PUT(i - 4, a[i - 4]); PUT(16 + i, zp[i]); });
    cmul2(a[12], a[11], wc, zb[5], zb[4], zb[1]);
    cmul2(a[13], a[12], wc, zb[6], zb[4], zb[2]);
    cmul2(a[14], a[13], wc, zb[7], zb[4], zb[3]);
    cmul2(a[15], a[14], wc, pa, zb[1], zp[16]);
    PUT(12, a[12]); PUT(13, a[13]); PUT(14, a[14]); PUT(15, a[15]); PUT(54, zb[5]); PUT(57, zb[6]); PUT(60, zb[7]); PUT(40, pa);
    sfor<0, 3>([&](auto i) { cmul2(pa, zb[1], zp[4 * i + 2], pb, zb[1], zp[4 * i + 4]); PUT(33 + 2 * i, pa); PUT(34 + 2 * i, pb); });
    cmul2(pa, zb[1], zp[14], pb, zb[1], zp[14]); PUT(39, pa);
    sfor<1, 5>([&](auto i) { cmul2(pa, zb[2], zp[4 * i], pb, zb[3], zp[4 * i]); PUT(41 + i, pa); PUT(46 + i, pb); });
    sfor<4, 8>([&](auto m) { cmul2(pa, zb[m], zp[8], pb, zb[m], zp[16]); PUT(52 + 3 * (m - 4), pa); PUT(53 + 3 * (m - 4), pb); });
#undef PUT
  }
  STAMP(2);
#pragma unroll
  for (int i = 0; i < 4; ++i) *(LAS v4u*)(rtw + 32 * i * 144) = wa[i];
#pragma unroll
  for (int i = 0; i < 2; ++i) *(LAS v4u*)(rtw + (L_RT2 - L_RT1) + 32 * i * 144) = wb[i];
  BAR();
  STAMP(3);
  v4u o1[6];
#pragma unroll
  for (int it = 0; it < 4; ++it) o1[it] = trfrag(rt1 + it * 4608, 4 * 144);
#pragma unroll
  for (int it = 0; it < 2; ++it) o1[4 + it] = trfrag(rt2 + it * 4608, 8 * 144);
  auto rt_st = [&](int it) {
    if (b < 224) {
      if (it < 4) __builtin_amdgcn_raw_buffer_store_b128(o1[it], rs, vo1, so1 + it * inc1, 16);
      else __builtin_amdgcn_raw_buffer_store_b128(o1[it], rs, vo2[it - 4], WS_WT2 + dgp * WJ, 16);
    }
  };
  {
    float ca = 0, cc = 0;
#pragma unroll
    for (int it = 0; it < 6; ++it) rt_st(it);
#pragma unroll
    for (int it = 0; it < 4; ++it) {
      ca = dot2bf(o1[it][0], 0x3f803f80u, ca); cc = dot2bf(o1[it][1], 0x3f803f80u, cc);
      ca = dot2bf(o1[it][2], 0x3f803f80u, ca); cc = dot2bf(o1[it][3], 0x3f803f80u, cc);
    }
    ca = sum32x(sum16x(ca + cc));
    if (lane < 16 && b < 224) __builtin_amdgcn_raw_buffer_store_b32(__float_as_uint(ca), rs, lane * 4, WS_CSUM + jj * 4096 + (rq * 224 + mat * 112 + ptw * 16) * 4, 16);
  }
  SB();
  STAMP(18);
  v4u g[8];
  u32 fl;
  auto gather = [&]() {
    sfor<0, 4>([&](auto i) { ld_vgpr<0>(g[i], srd, t16, WS_XG + pr * 16384 + i * 4096); });
    sfor<0, 4>([&](auto i) { ld_vgpr<0>(g[4 + i], srd, t16, WS_GRAN + xcd * 16384 + i * 4096); });
  };
  gather();
  STAMP(22);
  const u32 sW1 = WS_WT1 + j * WJ + w * WPT * 1024, sW2 = WS_WT1 + j * WJ + 4 * WPT * 1024;
  auto ld_w1 = [&](auto k) { ld_agpr<A_W + 4 * k, (k & 3) * 1024>(srd, lane16, sW1 + (k >> 2) * 4096); };
  {
    v4u Bf[8], Af[2][8];
#pragma unroll
    for (int ks = 0; ks < 8; ++ks) { Bf[ks] = *(LAS v4u*)(ap[3] + ks * 64); Af[0][ks] = *(LAS v4u*)(ap[0] + ks * 64); }
    SB();
    v4f d[4];
    sfor<0, 4>([&](auto mm) {
      if constexpr (mm == 2) {
    SB();
    WAITV(0);
    asm volatile("" :: "v"(pfv));
    if (lane == 0 && b < 224) __builtin_amdgcn_raw_buffer_store_b32(tag, rs, 0, WS_WFLAG + (b * 4 + w) * 128, 16);
    SB();
      }
      sfor<0, 8>([&](auto ks) {
        if constexpr (ks == 0) mfma16z(d[mm], Af[mm & 1][ks], Bf[ks]);
        else mfma16(d[mm], Af[mm & 1][ks], Bf[ks]);
        if constexpr (mm < 3) Af[(mm & 1) ^ 1][ks] = *(LAS v4u*)(ap[mm < 2 ? mm + 1 : 2] + (mm == 2 ? 6 * TCOL : 0) + ks * 64);
        SB();
      });
    });
    asm volatile("s_nop 7" : "+v"(d[0]), "+v"(d[1]), "+v"(d[2]), "+v"(d[3]));
    sfor<0, 4>([&](auto mm) {
      const u32 p0 = pkh(d[mm][3], d[mm][2]), p1 = pkh(d[mm][1], d[mm][0]);
#pragma unroll
      for (int e = 0; e < 4; ++e) {
        LAS char* k2 = kp + (3 - mm) * 1024 + e * (KC_B + 4);
        if (e & 1) *(LAS v2u*)k2 = v2u{p0, p1};
        else { *(LAS u32*)k2 = p0; *(LAS u32*)(k2 + 4) = p1; }
      }
      SB();
    });
  }
  STAMP(4);
  {
    waitv8<0>(g);
    for (int spin = 0; spin < SPIN_CAP; ++spin) {
      u32 df = 0;
#pragma unroll
      for (int i = 0; i < 8; ++i) df |= (g[i][1] ^ tag) | (g[i][3] ^ tag);
      asm volatile("" : "+v"(df));
      if (__all(df == 0)) break;
      COUNT(0);
      __builtin_amdgcn_s_sleep(10);
      gather();
      waitv8<0>(g);
      }
    STAMP(5);
    ld_flag(fl, srd, fo, WS_WFLAG);
    LAS char* cc[2] = {sm + t * 4, sm + CH_B + t * 4};
    asm volatile("" : "+v"(cc[1]));
    const u32 ub0 = t * 4, ub1 = CH_B + t * 4;
    sfor<0, 4>([&](auto i) {
      const float m0 = bflo(g[4 + i][0]), m1 = bflo(g[4 + i][2]);
      const float r0 = __builtin_amdgcn_rcpf(bfhi(g[4 + i][0])), r1 = __builtin_amdgcn_rcpf(bfhi(g[4 + i][2]));
#pragma unroll
      for (int c = 0; c < 2; ++c) {
        const u32 d = pkbf((c ? bfhi(g[i][0]) : bflo(g[i][0])) - m0, (c ? bfhi(g[i][2]) : bflo(g[i][2])) - m1);
        const u32 o = pkbf(bflo(d) * r0, bfhi(d) * r1);
        const u32 uh = pkh(bflo(o), bfhi(o));
        LAS char* c2 = cc[c] + i * 1024;
        *(LAS u32*)(c2 + OFF_U + UC_B + 112) = uh;
        dsw_halves<OFF_U + 114 + i * 1024>(c ? ub1 : ub0, uh);
        *(LAS u32*)(c2 + OFF_X) = __builtin_amdgcn_perm(g[i][2], g[i][0], c ? 0x07060302u : 0x05040100u);
      }
    });
  }
  STAMP(16);
  wait0_pin(fl);
  for (int spin = 0; spin < SPIN_CAP && !__all(fl == tag); ++spin) { __builtin_amdgcn_s_sleep(4); ld_flag(fl, srd, fo, WS_WFLAG); wait0_pin(fl); COUNT(1); }
  STAMP(17);
  sfor<0, 6>([&](auto k) { ld_w1(k); });
  BAR();
  if (b == 0 && t == 0) __builtin_amdgcn_raw_buffer_store_b32(tag, rs0, 0, WS_EPOCH, 16);
  STAMP(6);
  const u32 sWD = WS_WT2 + dg * WJ + w * WDW * 1024;
  auto pre_wd = [&](auto f) {
    if constexpr (f < 12) ld_agpr<A_PRE + 4 * f, (f & 3) * 1024>(srd, lane16, sWD + (e2 * 16 + (f & 12)) * 1024);
    else ld_agpr<240 + 4 * (f - 12), (f & 1) * 1024>(srd, lane16, sWD + 32768 + ((f - 12) >> 1) * 6144);
  };
  v4f acc[4];
  v2h uq[8];
  u32 xv[8], sk[8];
  const float dsk = bf2f(r_dsk);
  {
    auto extra = [&](auto r, auto m) {
      constexpr int sl = r * 36 + m;
      if constexpr (sl == 1 || sl == 36 || sl == 72 || sl == 108 || sl == 143) STAMP(sl == 1 ? 23 : sl == 36 ? 25 : sl == 72 ? 27 : sl == 108 ? 29 : 31);
      if constexpr (sl % 8 == 5 && sl < 56) {
        constexpr int i = sl / 8;
        __builtin_amdgcn_raw_buffer_store_b128(v4u{~0u, ~0u, ~0u, ~0u}, rs, lane16 + (i & 3) * 1024, WS_ACT + ((mt * 4 + w) * AT + j * 7 + (j >> 2) + (i & 4)) * 1024, 16);
      }
      if constexpr (r == 3 && m < 8) {
        uq[m] = *(LAS v2h*)(he[(m >> 1) & 1] + OFF_U + UC_B + 112 + (m >> 2) * 2048 + (m & 1) * 16);
        xv[m] = *(LAS u32*)(he[(m >> 1) & 1] + OFF_X + (m >> 2) * 2048 + (m & 1) * 16);
      }
      if constexpr (sl % 6 == 3 && sl < 132) {
        constexpr int k = sl / 6 < 16 ? 6 + sl / 6 : 16 + sl / 6;
        if constexpr (k < 32) ld_w1(IC<k>{});
        else { set_m0(L_W2 + (w * 6 + k - 32) * 1024); asm volatile("s_nop 0"); dma16w(srd, lane16, sW2 + (w * 6 + k - 32) * 1024); }
      }
    };
    v4f ac[8];
    conv_main(ub, kb, ac, extra);
    LAS char* xp = cb + lane16;
    auto give = [&](auto N) {
      sfor<0, 4>([&](auto a) {
        *(LAS v4f*)(xp + (4 * N + a) * 1024) = ac[4 * (a >> 1) + ((a & 1) ? 2 + N : 1 - N)];
        acc[a] = ac[4 * (a >> 1) + ((a & 1) ? 3 - N : N)];
      });
    };
    if (nh == 0) give(IC<0>{}); else give(IC<1>{});
#pragma unroll
    for (int m = 0; m < 8; ++m) { sk[m] = pkbf((float)uq[m][0] * dsk, (float)uq[m][1] * dsk); asm volatile("" : "+v"(sk[m])); }
    BAR();
#pragma unroll
    for (int a = 0; a < 4; ++a) acc[a] += *(LAS v4f*)(xp + (4 * (1 - nh) + a) * 1024);
  }
  STAMP(7);
  {
#pragma unroll
    for (int m = 0; m < 8; ++m) {
      const u32 yb = pkbf(acc[m >> 1][2 * (m & 1)] * yscl, acc[m >> 1][2 * (m & 1) + 1] * yscl);
      const u32 zz = pkbf(bflo(yb) + bflo(sk[m]), bfhi(yb) + bfhi(sk[m]));
      *(LAS u32*)(he[(m >> 1) & 1] + OFF_X + (m >> 2) * 2048 + (m & 1) * 16) =
          min(pkbf(bflo(xv[m]) + bflo(zz), bfhi(xv[m]) + bfhi(zz)), 0xfffffffeu);
    }
    WAITV(13);
    WAITL0();
#pragma unroll
    for (int i = 0; i < 2; ++i)
      __builtin_amdgcn_raw_buffer_store_b128(*(LAS v4u*)(cb + OFF_X + ho + i * 2048), rs0, ho + i * 2048, WS_HT + ch * HROW, 16);
  }
  STAMP(21);
  sfor<0, 16>([&](auto f) { pre_wd(f); });
  BAR();
  STAMP(8);
  const u32 vtok = (lane >> 4) * HROW + (((lane & 15) ^ (4 * (lane >> 4))) * 16);
  const u32 stok = WS_HT + mt * 256 + w * 16 * HROW;
  auto tok_dma = [&](int c) {
#pragma unroll
    for (int i = 0; i < 4; ++i) dma16(srd0, vtok, stok + (64 * c + 4 * i) * HROW, c * 16384 + (4 * w + i) * 1024);
  };
  dma16(srd, lane16, WS_CSUM + j * 4096 + w * 1024, L_CSP + w * 1024);
#pragma unroll
  for (int c = 0; c < 3; ++c) tok_dma(c);
  LAS char* tp[4][2];
#pragma unroll
  for (int i = 0; i < 4; ++i) {
    tp[i][0] = sm + (8 * (G >> 1) + q4) * 256 + 8 * (p4 & 1) + (((4 * (w ^ i) + 2 * (G & 1) + (p4 >> 1)) ^ (4 * q4)) * 16);
    tp[i][1] = tp[i][0] + 65536;
    asm volatile("" : "+v"(tp[i][1]));
  }
  LAS char* vp[2];
  vp[0] = sm + w * 4096 + lane16; vp[1] = vp[0] + 65536;
  asm volatile("" : "+v"(vp[1]));
  v4u own[32], vq[4];
  u32 vm = 0;
  float sA = 0, sB = 0, qA = 0, qB = 0;
  auto mx = [&](u32 m, const v4u& v) { return max(max(m, max(v[0], v[1])), max(v[2], v[3])); };
  auto own_rd = [&](auto s) { own[s] = trfrag(tp[0][s >> 4] + (s & 15) * 4096, 1024); };
  auto own_acc = [&](auto s) {
#pragma unroll
    for (int e = 0; e < 4; e += 2) {
      sA = dot2bf(own[s][e], 0x3f803f80u, sA); qA = dot2bf(own[s][e], own[s][e], qA);
      sB = dot2bf(own[s][e + 1], 0x3f803f80u, sB); qB = dot2bf(own[s][e + 1], own[s][e + 1], qB);
    }
    pin2(sA, qA); pin2(sB, qB);
  };
  auto v_rd = [&](auto c, auto i) { vq[i] = *(LAS v4u*)(vp[c >> 2] + (c & 3) * 16384 + i * 1024); };
  auto fix = [&](auto c0, auto c1) {
    for (int spin = 0; spin < SPIN_CAP && __any(vm == ~0u); ++spin) {
      COUNT(2 + (c0 + 2) / 4);
#pragma unroll
      for (int c = c0; c < c1; ++c) tok_dma(c);
      WAITV(0);
      vm = 0;
      sfor<c0, c1>([&](auto c) {
        sfor<0, 4>([&](auto i) { v_rd(c, i); });
        sfor<0, 4>([&](auto i) { vm = mx(vm, vq[i]); });
      });
    }
    vm = 0;
  };
  STAMP(52);
  WAITV(8);
  STAMP(53);
  sfor<0, 4>([&](auto i) { v_rd(IC<0>{}, i); });
  sfor<0, 4>([&](auto i) { vm = mx(vm, vq[i]); });
  SB();
  WAITV(4);
  sfor<0, 4>([&](auto i) { v_rd(IC<1>{}, i); });
  sfor<0, 4>([&](auto i) { vm = mx(vm, vq[i]); });
  fix(IC<0>{}, IC<2>{});
  BAR();
  STAMP(9);
  auto w2_lds = [&](int k) { return (k / 3) * 16384 + (3 * w + k % 3) * 1024; };
  auto w2_src = [&](int k) { return sW2 + (2 + k / 3) * 12288 + (3 * w + k % 3) * 1024; };
  auto ld_wd = [&](auto f) { ld_agpr<A_W + 4 * f, (f & 3) * 1024>(srd, lane16, sWD + ((f < 16 ? e2 : 1 - e2) * 16 + (f & 12)) * 1024); };
  v4u hres[2];
  {
    v4u ot[2][3];
    float lm = 0, lv = 0;
    LAS char* hp = sm + (64 * dg + 32 * (w >> 1) + 16 * (G >> 1) + q4) * 256 + 8 * (p4 & 1) +
                   (((4 * (2 * e2 + (w & 1)) + 2 * (G & 1) + (p4 >> 1)) ^ (4 * q4)) * 16);
    auto ot_rd = [&](auto s, auto i) { ot[s & 1][i] = trfrag(tp[i + 1][s >> 4] + (s & 15) * 4096, 1024); };
    own_rd(IC<0>{});
    sfor<0, 3>([&](auto i) { ot_rd(IC<0>{}, i); });
    if (t < 224) {
      LAS float* cp = (LAS float*)(sm + L_CSP) + t;
      *(LAS float*)(sm + L_CS + t * 4) = (cp[0] + cp[224]) + (cp[448] + cp[672]);
    }
    SB();
    sfor<0, 32>([&](auto s) {
      constexpr int vc = s == 2 ? 2 : s == 5 ? 3 : s == 10 ? 4 : s == 13 ? 5 : s == 17 ? 6 : s == 19 ? 7 : -1;
      constexpr int vy = s == 2 ? 4 : s == 5 ? 5 : s < 17 ? 9 : s == 17 ? 11 : 8;
      constexpr bool vmx = s == 3 || s == 6 || s == 11 || s == 14 || s == 18 || s == 20;
      constexpr int o0 = s < 23 ? s + 1 : s < 27 ? 24 + 2 * (s - 23) : -1, o1 = (s >= 23 && s < 27) ? o0 + 1 : -1;
      constexpr int p0 = s < 15 ? (s / 3) * 4 + (s % 3 == 0 ? 0 : s % 3 + 1) : -1, p1 = (s < 15 && s % 3 == 0) ? p0 + 1 : -1;
      if constexpr (s == 16 || s == 20 || s == 23 || s == 24 || s == 27) STAMP(40 + s - 16);
      if constexpr (s == 5) sfor<0, 6>([&](auto k) { own_acc(k); });
      if constexpr (s == 12) sfor<6, 13>([&](auto k) { own_acc(k); });
      if constexpr (s == 20) sfor<13, 21>([&](auto k) { own_acc(k); });
      if constexpr (s == 28) sfor<21, 32>([&](auto k) { own_acc(k); });
      if constexpr (s == 20) STAMP(45);
      if constexpr (vc >= 0) WAITV(vy);
      if constexpr (p0 >= 0) set_m0((3 + p0 / 4) * 16384 + (4 * w + p0 % 4) * 1024);
      if constexpr (s >= 15) set_m0(w2_lds(s - 15));
      mfma_aav<A_ACC1 + 16, A_W + 4 * s, s == 0>(ot[s & 1][0]);
      if constexpr (p0 >= 0) dma16n(srd0, vtok, stok + (64 * (3 + p0 / 4) + 4 * (p0 % 4)) * HROW);
      if constexpr (s >= 15) dma16w(srd, lane16, w2_src(s - 15));
      if constexpr (vc >= 6) { v_rd(IC<vc>{}, IC<0>{}); v_rd(IC<vc>{}, IC<1>{}); }
      if constexpr (vmx) { x_max(vm, vq[0]); x_max(vm, vq[1]); }
      SB();
      mfma_aav<A_ACC1 + 32, A_W + 4 * s, s == 0>(ot[s & 1][1]);
      if constexpr (s < 31) { ot_rd(IC<s + 1>{}, IC<0>{}); ot_rd(IC<s + 1>{}, IC<1>{}); }
      if constexpr (p1 >= 0) set_m0((3 + p1 / 4) * 16384 + (4 * w + p1 % 4) * 1024);
      SB();
      mfma_aav<A_ACC1 + 48, A_W + 4 * s, s == 0>(ot[s & 1][2]);
      if constexpr (p1 >= 0) dma16n(srd0, vtok, stok + (64 * (3 + p1 / 4) + 4 * (p1 % 4)) * HROW);
      if constexpr (s < 15 && s % 3 != 0) ld_w1(IC<22 + 2 * (s / 3) + s % 3 - 1>{});
      if constexpr (s >= 15) ld_wd(IC<s - 3>{});
      if constexpr (vc >= 0 && vc < 6) { v_rd(IC<vc>{}, IC<0>{}); v_rd(IC<vc>{}, IC<1>{}); }
      if constexpr (vc >= 0) { v_rd(IC<vc>{}, IC<2>{}); v_rd(IC<vc>{}, IC<3>{}); }
      if constexpr (o1 >= 0) own_rd(IC<o1>{});
      if constexpr (vmx) { x_max(vm, vq[2]); x_max(vm, vq[3]); }
      SB();
      mfma_aav<A_ACC1, A_W + 4 * s, s == 0>(own[s]);
      if constexpr (s < 31) ot_rd(IC<s + 1>{}, IC<2>{});
      if constexpr (o0 >= 0) own_rd(IC<o0>{});
      if constexpr (s == 1 || s == 7 || s == 16 || s == 23) {
        if ((dg >> 1) == (s + 1) / 8) { hres[0] = trfrag(hp, 1024); hres[1] = trfrag(hp + 2048, 1024); }
      }
      SB();
      if constexpr (s == 6) { fix(IC<2>{}, IC<4>{}); BAR(); STAMP(19); SB(); }
      if constexpr (s == 14) { fix(IC<4>{}, IC<6>{}); BAR(); STAMP(20); SB(); }
      if constexpr (s == 22) { fix(IC<6>{}, IC<8>{}); BAR(); SB(); }
      if constexpr (s == 28) {
        lm = sum32x(sA + sB) * (1.0f / D);
        lv = rbf(rbf(fmaxf(sum32x(qA + qB) * (1.0f / D) - lm * lm, 0.0f)) + 1e-5f);
        pin2(lm, lv);
        SB();
      }
      if constexpr (s == 29) { lv = rbf(__builtin_sqrtf(lv)); pin2(lm, lv); SB(); }
      if constexpr (s == 30) {
        if (lane < 32) *(LAS v4f*)(sm + L_MR + (w * 32 + lane) * 16) = v4f{rbf(lm), -1.44269504088896340736f * __builtin_amdgcn_rcpf(lv), lv * lv, 0.0f};
        SB();
      }
    });
  }
  const int hh = lane >> 5;
  v4f mr[4], cs[4][4];
  auto cs_rd = [&](int k, int pt) {
    LAS char* cp = sm + L_CS + (pt * 16 + 4 * hh) * 4;
    cs[k][0] = *(LAS v4f*)cp; cs[k][1] = *(LAS v4f*)(cp + 32); cs[k][2] = *(LAS v4f*)(cp + 448); cs[k][3] = *(LAS v4f*)(cp + 480);
  };
  u32 wp[3] = {lane16, 65536 + lane16, L_W2 + lane16};
  asm volatile("" : "+v"(wp[1]), "+v"(wp[2]));
  v4u w2[3][3];
  sfor<0, 6>([&](auto i) { dsr128<i * 1024>(w2[i / 3][i % 3], wp[2]); });
  cs_rd(0, w);
  WAITV(11);
  BAR();
  STAMP(10);
  const u32 sAct = WS_ACT + ((mt * 4) * AT + j * 7 + (j >> 2)) * 1024;
  const u32 sAin = WS_ACT + ((mt * 4 + 2 * e2) * AT + 29 * w) * 1024;
  v4u af[7][8];
  auto ld_af = [&](auto g, auto i) {
    if constexpr (g < 4) {
      constexpr int k = (i & 1) ^ (g & 1);
      ld_vgpr<(i & 1) * 1024>(af[g][i], srd, lane16, sAin + (k * AT + 7 * (i >> 1) + (g < 2 ? 2 * e2 : 2 - 2 * e2)) * 1024);
    } else {
      ld_vgpr<(g - 4) * 1024>(af[g][i], srd, lane16, sAin + ((i & 1) * AT + 7 * (i >> 1) + 4) * 1024);
    }
  };
  float ag[8], au[8], tg[8], tu[8], xe[8], pp[8], dn[8], rc[8], vv[8];
  v4u ob[2];
  auto s0 = [&](auto q) { constexpr int A = (q < 32 ? A_ACC1 + 16 * (q >> 3) : A_ACC2 + 16 * ((q >> 3) - 4)) + (q & 7); ag[q & 7] = accrd<A>(); au[q & 7] = accrd<A + 8>(); };
  auto s1 = [&](auto q) {
    constexpr int e = q & 7, U = q < 32 ? q >> 3 : 0, C = q < 32 ? 0 : (q >> 3) - 3;
    tg[e] = x_fnma(mr[U][0], cs[C][e >> 2][e & 3], ag[e]);
    tu[e] = x_fnma(mr[U][0], cs[C][2 + (e >> 2)][e & 3], au[e]);
    xe[e] = x_mul(tg[e], mr[U][1]);
  };
  auto s2a = [&](auto q) { constexpr int e = q & 7; xe[e] = x_exp(xe[e]); pp[e] = x_mul(tg[e], tu[e]); };
  auto s2b = [&](auto q) { constexpr int e = q & 7, U = q < 32 ? q >> 3 : 0; dn[e] = x_fma(xe[e], mr[U][2], mr[U][2]); };
  auto s3 = [&](auto q) { rc[q & 7] = x_rcp(dn[q & 7]); };
  auto s4a = [&](auto q) { constexpr int e = q & 7; vv[e] = x_mul(pp[e], rc[e]); };
  auto s4b = [&](auto q) { constexpr int e = q & 7; if constexpr (e & 1) ob[(q >> 3) & 1][e >> 1] = x_cap(x_pk(vv[e - 1], vv[e])); };
  auto s5 = [&](auto q) {
    constexpr int un = q >> 3;
    if constexpr ((q & 7) == 7)
      __builtin_amdgcn_raw_buffer_store_b128(ob[un & 1], rs, lane16, un < 4 ? sAct + ((w ^ un) * AT + w) * 1024 : sAct + (w * AT + un) * 1024, 16);
  };
  Sw st;
  auto breg = [](int o) { return o < 2 ? 240 + 8 * o : o < 10 ? 8 * (o - 2) : A_PRE + 8 * (o - 10); };
  auto wb_lds = [&](int x) { return x < 8 ? 98304 + w * 8192 + x * 1024 : x < 14 ? (x - 8) * 16384 + 12288 + w * 1024 : L_W2 + w * 6144 + (x - 14) * 1024; };
  {
#pragma unroll
    for (int i = 0; i < 4; ++i) mr[i] = *(LAS v4f*)(sm + L_MR + ((w ^ i) * 32 + (lane & 31)) * 16);
    asm volatile("s_nop 7" ::: "memory");
    s0(IC<0>{}); s1(IC<0>{}); s2a(IC<0>{}); s2b(IC<0>{});
    st.dn = dn[0]; st.p = pp[0]; st.v = 0;
    dma16(srd, lane16, w2_src(17), w2_lds(17));
    SB();
    sfor<0, 32>([&](auto s) {
      if constexpr (s == 1 || s == 8 || s == 16 || s == 24) STAMP(24 + (s >> 2));
      if constexpr (s == 20) { WAITV(19); BARX(); }
      constexpr int q = s < 31 ? s + 1 : 31, e = q & 7, U = q >> 3;
      constexpr int r = s < 30 ? s + 2 : 31, g = r >> 2;
      constexpr int off = (g < 2 ? g * 12288 : g < 6 ? (g - 2) * 16384 : (g - 6) * 16384) + (r & 3) * 3072;
      constexpr int x = (s >= 3 && s < 17) ? s - 3 : (s >= 20 && s < 26) ? s - 6 : -1;
      if constexpr (x >= 0) set_m0(wb_lds(x));
      p2_a<s == 0, off>(w2[s % 3][0], w2[(s + 2) % 3], own[s], wp[g < 2 ? 2 : g < 6 ? 0 : 1]);
      if constexpr (s < 3) ld_wd(IC<29 + s>{});
      if constexpr (x >= 0) dma16w(srd, lane16, sWD + 32768 + (3 * ((2 + (x >> 1)) & 3) + ((2 + (x >> 1)) >> 2)) * 2048 + (x & 1) * 1024);
      if constexpr (s >= 24) { ld_af(IC<(s >= 28)>{}, IC<2 * (s & 3)>{}); ld_af(IC<(s >= 28)>{}, IC<2 * (s & 3) + 1>{}); }
      if constexpr (s > 0) s5(IC<s - 1>{});
      u32 o;
      p2_b<s == 0, (s & 1) != 0, A_ACC1 + 16 * U + e>(w2[s % 3][1], w2[s % 3][2], own[s], st, o,
                                                      mr[U][0], mr[U][1], mr[U][2], cs[0][e >> 2][e & 3], cs[0][2 + (e >> 2)][e & 3]);
      if constexpr (s & 1) ob[(s >> 3) & 1][(s & 7) >> 1] = o;
      if constexpr (s == 31) s5(s);
      SB();
    });
    waitl3<0>(w2[0]); waitl3<0>(w2[1]); waitl3<0>(w2[2]);
  }
  STAMP(11);
  cs_rd(1, 4); cs_rd(2, 5); cs_rd(3, 6);
  asm volatile("s_nop 7\n\ts_nop 3" ::: "memory");
  sfor<32, 40>([&](auto q) { s0(q); });
  sfor<32, 40>([&](auto q) { s1(q); });
  sfor<32, 40>([&](auto q) { s2a(q); });
  sfor<32, 40>([&](auto q) { s2b(q); });
  sfor<32, 40>([&](auto q) { s3(q); });
  sfor<32, 40>([&](auto q) { s4a(q); });
  sfor<32, 40>([&](auto q) { s4b(q); });
  s5(IC<39>{});
  SB();
  STAMP(12);
  u32 am = 0;
  auto af_fix = [&](auto g) {
    for (int spin = 0; spin < SPIN_CAP && __any(am == ~0u); ++spin) {
      COUNT(g < 2 ? 5 : g < 4 ? 6 : 7);
      sfor<0, 8>([&](auto i) { ld_af(g, i); });
      waitv8<0>(af[g]);
      am = 0;
      sfor<0, 8>([&](auto i) { x_max(am, af[g][i]); });
    }
    am = 0;
  };
  s0(IC<40>{}); s1(IC<40>{}); s2a(IC<40>{});
  waitv8<10>(af[0]);
  s2b(IC<40>{});
  st.dn = dn[0]; st.p = pp[0];
  sfor<0, 8>([&](auto i) { x_max(am, af[0][i]); });
  af_fix(IC<0>{});
  const u32 wbp[4] = {98304 + w * 8192 + lane16, 12288 + w * 1024 + lane16, 65536 + 12288 + w * 1024 + lane16, L_W2 + w * 6144 + lane16};
  auto wb_rd = [&](auto x) {
    dsr_agpr<breg(2 + (x >> 1)) + 4 * (x & 1), (x < 8 ? x * 1024 : x < 14 ? ((x - 8) & 3) * 16384 : (x - 14) * 1024)>(wbp[x < 8 ? 0 : x < 12 ? 1 : x < 14 ? 2 : 3]);
  };
  SB();
  STAMP(13);
  float hf[16];
  GAS u16* op = (GAS u16*)out + (size_t)(128 * mt + 64 * e2 + 32 * (w & 1) + (lane & 31)) * D + 64 * dg + 32 * (w >> 1) + 16 * hh;
  sfor<0, 6>([&](auto g) {
    constexpr int WV[6] = {10, 5, 9, 8, 8, 0};
    sfor<0, 16>([&](auto m) {
      if constexpr (m == 12 && g < 6) waitv8<WV[g < 6 ? g : 0]>(af[g < 6 ? g + 1 : 0]);
      if constexpr (g < 2) {
        if constexpr ((m & 1) == 0) {
          constexpr int i = m >> 1, k = (i & 1) ^ g, q = 40 + 8 * g + i, nq = q < 55 ? q + 1 : 55, e = nq & 7, C = (nq >> 3) - 3;
          u32 o;
          g2_pair<(g == 0 && m < 4), (i & 1) != 0, A_ACC2 + 16 * (C - 1) + e, A_ACC1 + 16 * k, (i < 6 ? A_PRE : A_W) + 8 * i>(
              af[g][i], st, o, mr[0][0], mr[0][1], mr[0][2], cs[C][e >> 2][e & 3], cs[C][2 + (e >> 2)][e & 3]);
          if constexpr (i & 1) ob[(q >> 3) & 1][i >> 1] = o;
        }
      } else if constexpr (g < 4) {
        constexpr int i = m >> 1, dt = m & 1, k = (i & 1) ^ (g & 1);
        mfma_aav<A_ACC1 + 32 * dt + 16 * k, A_W + 64 + 8 * i + 4 * dt, false>(af[g][i]);
      } else {
        constexpr int jl = m >> 2, dt = (m >> 1) & 1, k = m & 1;
        mfma_aav<A_ACC1 + 32 * dt + 16 * k, breg(4 * (g - 4) + jl) + 4 * dt, false>(af[g][2 * jl + k]);
      }
      if constexpr (g == 0 && m < 8) ld_af(IC<2>{}, m);
      if constexpr (g == 3 && m >= 8 && m < 12) wb_rd(IC<8 + m>{});
      if constexpr (g == 1 && m >= 8) ld_af(IC<3>{}, IC<m - 8>{});
      if constexpr (g == 2 && m < 8) ld_af(IC<4>{}, m);
      if constexpr (g == 2 && m >= 8 && m < 12) { wb_rd(IC<3 * (m - 8)>{}); wb_rd(IC<3 * (m - 8) + 1>{}); wb_rd(IC<3 * (m - 8) + 2>{}); }
      if constexpr (g == 2 && m >= 12) wb_rd(m);
      if constexpr (g == 3 && m < 8) ld_af(IC<5>{}, m);
      if constexpr (g == 4 && m >= 4 && m < 12) ld_af(IC<6>{}, IC<m - 4>{});
      if constexpr (m >= 12 && g < 6) { x_max(am, af[g < 6 ? g + 1 : 0][2 * (m - 12)]); x_max(am, af[g < 6 ? g + 1 : 0][2 * (m - 12) + 1]); }
      if constexpr (g < 2 && m == 15) s5(IC<47 + 8 * g>{});
      if constexpr (g == 5 && m < 8) { hf[2 * m] = bflo(hres[m >> 2][m & 3]); hf[2 * m + 1] = bfhi(hres[m >> 2][m & 3]); pin2(hf[2 * m], hf[2 * m + 1]); }
      if constexpr (g == 4 && m == 0) asm volatile("" : "+v"(op));
      SB();
    });
    if constexpr (g < 6) af_fix(IC<g + 1>{});
    if constexpr (g == 3) BAR();
    STAMP(32 + g);
    SB();
  });
  float a[16];
  auto xch = [&](auto W) {
    const u32 ad = W * 4096 + lane16;
    sfor<0, 16>([&](auto m) {
      constexpr int k = W ^ (((m >> 2) + 1) & 3), jl = m & 3, kp = W ^ (m >> 2);
      mfma_aav<A_ACC1 + 16 * k, breg(8 + jl) + 4 * (k >> 1), false>(af[6][2 * jl + (k & 1)]);
      if constexpr (m >= 4 && jl >= 2) {
        dsw_agpr<A_ACC1 + 16 * kp + 8 * (jl - 2), kp * 16384 + (jl - 2) * 2048>(ad);
        dsw_agpr<A_ACC1 + 16 * kp + 8 * (jl - 2) + 4, kp * 16384 + (jl - 2) * 2048 + 1024>(ad);
      }
      SB();
    });
    asm volatile("s_nop 7\n\ts_nop 7\n\ts_nop 3" ::: "memory");
    sfor<0, 16>([&](auto e) { a[e] = accrd<A_ACC1 + 16 * W + e>(); });
  };
  STAMP(14);
  if (w == 0) xch(IC<0>{}); else if (w == 1) xch(IC<1>{}); else if (w == 2) xch(IC<2>{}); else xch(IC<3>{});
  BAR();
#pragma unroll
  for (int k = 1; k < 4; ++k) {
#pragma unroll
    for (int e = 0; e < 4; ++e) {
      const v4f p = *(LAS v4f*)(sm + w * 16384 + (w ^ k) * 4096 + e * 1024 + lane16);
#pragma unroll
      for (int c = 0; c < 4; ++c) a[4 * e + c] += p[c];
    }
  }
  {
    u32 o[8];
#pragma unroll
    for (int e = 0; e < 8; ++e) {
      const u32 f = pkbf(a[2 * e], a[2 * e + 1]);
      o[e] = pkbf(hf[2 * e] + bflo(f), hf[2 * e + 1] + bfhi(f));
    }
    *(GAS v4u*)op = v4u{o[0], o[1], o[2], o[3]};
    *(GAS v4u*)(op + 8) = v4u{o[4], o[5], o[6], o[7]};
  }
  STAMP(15);
  asm volatile(".Lkf_end:");
}

extern "C" void kernel_launch(void* const* d_in, const int* in_sizes, int n_in, void* d_out, int out_size, void* d_ws, size_t ws_size,
                              hipStream_t stream) {
  (void)hipFuncSetAttribute((const void*)k_fused, hipFuncAttributeMaxDynamicSharedMemorySize, LDS_B);
  k_fused<<<256, 256, LDS_B, stream>>>((const u16*)d_in[0], (const u16*)d_in[1], (const u16*)d_in[2], (const u16*)d_in[3], (const u16*)d_in[4],
                                       (const u16*)d_in[5], (const u16*)d_in[6], (const u16*)d_in[8], (const u16*)d_in[9],
                                       (const u16*)d_in[10], (char*)d_ws, (u16*)d_out);
}
```

```cpp
#include <hip/hip_runtime.h>
#include <hip/hip_bf16.h>
#include <stdint.h>
#include <type_traits>

typedef unsigned short u16;
typedef unsigned int u32;
typedef unsigned long long u64;
typedef short v4s __attribute__((ext_vector_type(4)));
typedef __bf16 v2bf __attribute__((ext_vector_type(2)));
typedef _Float16 v2h __attribute__((ext_vector_type(2)));
typedef float v2f __attribute__((ext_vector_type(2)));
typedef float v4f __attribute__((ext_vector_type(4)));
typedef u32 v2u __attribute__((ext_vector_type(2)));
typedef u32 v4u __attribute__((ext_vector_type(4)));
#define LAS __attribute__((address_space(3)))
#define GAS __attribute__((address_space(1)))
#define DEV __device__ __forceinline__

constexpr int L = 2048, D = 512, NS = 128, F = 1792;

constexpr u32 WS_EPOCH = 0;
constexpr u32 WS_WFLAG = 512 << 10;
constexpr u32 WS_GRAN  = 64 << 10;
constexpr u32 WS_CSUM  = 256 << 10;
constexpr u32 WS_HT    = 1 << 20;
constexpr u32 WS_WT1   = 4 << 20;
constexpr u32 WS_WT2   = 8 << 20;
constexpr u32 WS_ACT   = 12 << 20;
constexpr u32 WS_XG    = 20 << 20;
constexpr u32 HROW = 4352, WJ = 233472 + 4096, WPT = 33, WDW = 57, AT = 117;
constexpr u32 WS_USED  = 24 << 20;
constexpr u32 WS_FIX   = 4 << 20;
constexpr size_t WS_ROT = 40 << 20;

template <int I> using IC = std::integral_constant<int, I>;
template <int B, int E, class Fn> DEV void sfor(Fn&& f) {
  if constexpr (B < E) { f(IC<B>{}); sfor<B + 1, E>(f); }
}

DEV float bf2f(u32 b) { return __uint_as_float(b << 16); }
DEV float bflo(u32 p) { return __uint_as_float(p << 16); }
DEV float bfhi(u32 p) { return __uint_as_float(p & 0xffff0000u); }
DEV u32 pkbf(float a, float b) { v2f f = {a, b}; return __builtin_bit_cast(u32, __builtin_convertvector(f, v2bf)); }
DEV u32 pkh(float a, float b) { v2f f = {a, b}; return __builtin_bit_cast(u32, __builtin_convertvector(f, v2h)); }
DEV float rbf(float a) { return bflo(pkbf(a, a)); }
DEV float dot2bf(u32 a, u32 b, float c) { return __builtin_amdgcn_fdot2_f32_bf16(__builtin_bit_cast(v2bf, a), __builtin_bit_cast(v2bf, b), c, false); }
DEV v2u trread(LAS char* p) { return __builtin_bit_cast(v2u, __builtin_amdgcn_ds_read_tr16_b64_v4i16((LAS v4s*)p)); }
DEV v4u trfrag(LAS char* p, int second) { const v2u a = trread(p), b = trread(p + second); return v4u{a[0], a[1], b[0], b[1]}; }
#define WAITV(n) asm volatile("s_waitcnt vmcnt(%0)" :: "i"(n) : "memory")
#define WAITL0() asm volatile("s_waitcnt lgkmcnt(0)" ::: "memory")
#define BAR() asm volatile("s_waitcnt lgkmcnt(0)\n\ts_barrier" ::: "memory")
#define BARX() asm volatile("s_barrier" ::: "memory")
#define SB() __builtin_amdgcn_sched_barrier(0)
#define WSC " sc1"
constexpr int SPIN_CAP = 1 << 20;

template <int CTRL> DEV float dpp(float v) { return __builtin_bit_cast(float, __builtin_amdgcn_update_dpp(0, __builtin_bit_cast(int, v), CTRL, 0xf, 0xf, true)); }
DEV float swap16_other(float v, float& o) { auto r = __builtin_amdgcn_permlane16_swap(__float_as_uint(v), __float_as_uint(v), false, false); o = __uint_as_float(r[1]); return __uint_as_float(r[0]); }
DEV float swap32_other(float v, float& o) { auto r = __builtin_amdgcn_permlane32_swap(__float_as_uint(v), __float_as_uint(v), false, false); o = __uint_as_float(r[1]); return __uint_as_float(r[0]); }
DEV float sum16x(float v) { float o; const float a = swap16_other(v, o); return a + o; }
DEV float sum32x(float v) { float o; const float a = swap32_other(v, o); return a + o; }
DEV float wave_sum(float v) {
  v += dpp<0xB1>(v); v += dpp<0x4E>(v); v += dpp<0x141>(v); v += dpp<0x140>(v);
  return sum32x(sum16x(v));
}
DEV float wave_max(float v) {
  v = fmaxf(v, dpp<0xB1>(v)); v = fmaxf(v, dpp<0x4E>(v)); v = fmaxf(v, dpp<0x141>(v)); v = fmaxf(v, dpp<0x140>(v));
  float o; float a = swap16_other(v, o); v = fmaxf(a, o); a = swap32_other(v, o); return fmaxf(a, o);
}

DEV void dma16(v4u srd, u32 voff, u32 soff, u32 lds) {
  asm volatile("s_mov_b32 m0, %3\n\ts_nop 0\n\tbuffer_load_dwordx4 %0, %1, %2 offen sc1 lds" :: "v"(voff), "s"(srd), "s"(soff), "s"(lds) : "memory");
}
template <int A, int IMM> DEV void ld_agpr(v4u srd, u32 voff, u32 soff) {
  asm volatile("buffer_load_dwordx4 a[%c3:%c4], %0, %1, %2 offen offset:%c5" WSC :: "v"(voff), "s"(srd), "s"(soff), "i"(A), "i"(A + 3), "i"(IMM) : "memory");
}
template <int IMM> DEV void ld_vgpr(v4u& d, v4u srd, u32 voff, u32 soff) {
  asm volatile("buffer_load_dwordx4 %0, %1, %2, %3 offen offset:%c4 sc1" : "=v"(d) : "v"(voff), "s"(srd), "s"(soff), "i"(IMM) : "memory");
}
template <int ACC, int A, bool FIRST> DEV void mfma_aav(const v4u& b) {
  if constexpr (FIRST) asm volatile("v_mfma_f32_32x32x16_bf16 a[%c1:%c2], a[%c3:%c4], %0, 0" :: "v"(b), "i"(ACC), "i"(ACC + 15), "i"(A), "i"(A + 3));
  else asm volatile("v_mfma_f32_32x32x16_bf16 a[%c1:%c2], a[%c3:%c4], %0, a[%c1:%c2]" :: "v"(b), "i"(ACC), "i"(ACC + 15), "i"(A), "i"(A + 3));
}
template <int ACC, bool FIRST> DEV void mfma_avv(const v4u& a, const v4u& b) {
  if constexpr (FIRST) asm volatile("v_mfma_f32_32x32x16_bf16 a[%c2:%c3], %0, %1, 0" :: "v"(a), "v"(b), "i"(ACC), "i"(ACC + 15));
  else asm volatile("v_mfma_f32_32x32x16_bf16 a[%c2:%c3], %0, %1, a[%c2:%c3]" :: "v"(a), "v"(b), "i"(ACC), "i"(ACC + 15));
}
template <int A, int OFF> DEV void dsw_agpr(u32 ad) {
  asm volatile("ds_write_b128 %0, a[%c1:%c2] offset:%c3" :: "v"(ad), "i"(A), "i"(A + 3), "i"(OFF) : "memory");
}
template <int OFF> DEV void dsw_halves(u32 ad, u32 v) {
  asm volatile("ds_write_b16 %0, %1 offset:%c2\n\tds_write_b16_d16_hi %0, %1 offset:%c3" :: "v"(ad), "v"(v), "i"(OFF), "i"(OFF + 2) : "memory");
}
DEV void ld_flag(u32& d, v4u srd, u32 voff, u32 soff) {
  asm volatile("buffer_load_dword %0, %1, %2, %3 offen sc1" : "=v"(d) : "v"(voff), "s"(srd), "s"(soff) : "memory");
}
DEV void wait0_pin(u32& d) { asm volatile("s_waitcnt vmcnt(0)" : "+v"(d) :: "memory"); }
DEV void pin2(float& a, float& b) { asm volatile("" : "+v"(a), "+v"(b)); }
template <int A> DEV float accrd() { float v; asm volatile("v_accvgpr_read_b32 %0, a%c1" : "=v"(v) : "i"(A)); return v; }
DEV float x_mul(float a, float b) { float d; asm volatile("v_mul_f32 %0, %1, %2" : "=v"(d) : "v"(a), "v"(b)); return d; }
DEV float x_fma(float a, float b, float c) { float d; asm volatile("v_fma_f32 %0, %1, %2, %3" : "=v"(d) : "v"(a), "v"(b), "v"(c)); return d; }
DEV float x_fnma(float a, float b, float c) { float d; asm volatile("v_fma_f32 %0, -%1, %2, %3" : "=v"(d) : "v"(a), "v"(b), "v"(c)); return d; }
DEV float x_exp(float a) { float d; asm volatile("v_exp_f32 %0, %1" : "=v"(d) : "v"(a)); return d; }
DEV float x_rcp(float a) { float d; asm volatile("v_rcp_f32 %0, %1" : "=v"(d) : "v"(a)); return d; }
DEV u32 x_pk(float a, float b) { u32 d; asm volatile("v_cvt_pk_bf16_f32 %0, %1, %2" : "=v"(d) : "v"(a), "v"(b)); return d; }
DEV u32 x_cap(u32 a) { u32 d; asm volatile("v_min_u32 %0, -2, %1" : "=v"(d) : "v"(a)); return d; }
DEV void x_max(u32& m, const v4u& v) { asm volatile("v_max3_u32 %0, %0, %1, %2\n\tv_max3_u32 %0, %0, %3, %4" : "+v"(m) : "v"(v[0]), "v"(v[1]), "v"(v[2]), "v"(v[3])); }
template <int OFF> DEV void dsr128(v4u& d, u32 ad) { asm volatile("ds_read_b128 %0, %1 offset:%c2" : "=v"(d) : "v"(ad), "i"(OFF) : "memory"); }
template <int N> DEV void waitl3(v4u (&a)[3]) { asm volatile("s_waitcnt lgkmcnt(%c3)" : "+v"(a[0]), "+v"(a[1]), "+v"(a[2]) : "i"(N) : "memory"); }
template <int N> DEV void waitv8(v4u (&a)[8]) {
  asm volatile("s_waitcnt vmcnt(%c8)" : "+v"(a[0]), "+v"(a[1]), "+v"(a[2]), "+v"(a[3]), "+v"(a[4]), "+v"(a[5]), "+v"(a[6]), "+v"(a[7]) : "i"(N) : "memory");
}
struct Sw { float dn, p, v; };
#define SW_A "v_rcp_f32 %[rc], %[dn]\n\tv_accvgpr_read_b32 %[ag], a%c[q]\n\tv_accvgpr_read_b32 %[au], a%c[q8]\n\t"
#define SW_B "v_fma_f32 %[ag], -%[m], %[cg], %[ag]\n\tv_fma_f32 %[au], -%[m], %[cu], %[au]\n\tv_mul_f32 %[rc], %[p], %[rc]\n\tv_mul_f32 %[dn], %[ag], %[rl]\n\t"
#define SW_C0 "v_exp_f32 %[dn], %[dn]\n\tv_mul_f32 %[p], %[ag], %[au]\n\tv_mov_b32 %[v], %[rc]\n\tv_fma_f32 %[dn], %[dn], %[s2], %[s2]"
#define SW_C1 "v_exp_f32 %[dn], %[dn]\n\tv_mul_f32 %[p], %[ag], %[au]\n\tv_cvt_pk_bf16_f32 %[o], %[v], %[rc]\n\tv_fma_f32 %[dn], %[dn], %[s2], %[s2]\n\tv_min_u32 %[o], -2, %[o]"
template <bool FIRST, int OFF> DEV void p2_a(const v4u& c0, v4u (&n)[3], const v4u& b, u32 ad) {
#define P2A(Z) asm volatile("s_waitcnt lgkmcnt(3)\n\tv_mfma_f32_32x32x16_bf16 a[0:15], %[w0], %[b], " Z "\n\t" \
    "ds_read_b128 %[n0], %[ad] offset:%c[of]\n\tds_read_b128 %[n1], %[ad] offset:%c[of]+1024\n\tds_read_b128 %[n2], %[ad] offset:%c[of]+2048" \
    : [n0] "=&v"(n[0]), [n1] "=&v"(n[1]), [n2] "=&v"(n[2]) : [w0] "v"(c0), [b] "v"(b), [ad] "v"(ad), [of] "i"(OFF) : "memory")
  if constexpr (FIRST) P2A("0"); else P2A("a[0:15]");
}
template <bool FIRST, bool ODD, int Q>
DEV void p2_b(const v4u& c1, const v4u& c2, const v4u& b, Sw& st, u32& o, float m, float rl, float s2, float cg, float cu) {
  float ag, au, t0;
#define P2B(Z1, Z2, RC, C) asm volatile("v_accvgpr_read_b32 %[ag], a%c[q]\n\tv_accvgpr_read_b32 %[au], a%c[q8]\n\t" \
    "v_mfma_f32_32x32x16_bf16 a[16:31], %[w1], %[b], " Z1 "\n\tv_rcp_f32 " RC ", %[dn]\n\t" \
    "v_fma_f32 %[ag], -%[m], %[cg], %[ag]\n\tv_fma_f32 %[au], -%[m], %[cu], %[au]\n\tv_mul_f32 %[dn], %[ag], %[rl]\n\tv_mul_f32 " RC ", %[p], " RC "\n\t" \
    "v_mfma_f32_32x32x16_bf16 a[32:47], %[w2], %[b], " Z2 "\n\tv_exp_f32 %[dn], %[dn]\n\tv_mul_f32 %[p], %[ag], %[au]\n\t" C \
    : [dn] "+v"(st.dn), [p] "+v"(st.p), [v] "+v"(st.v), [o] "=&v"(o), [t0] "=&v"(t0), [ag] "=&v"(ag), [au] "=&v"(au) \
    : [w1] "v"(c1), [w2] "v"(c2), [b] "v"(b), [m] "v"(m), [rl] "v"(rl), [s2] "v"(s2), [cg] "v"(cg), [cu] "v"(cu), [q] "i"(Q), [q8] "i"(Q + 8))
#define P2_EVEN "v_fma_f32 %[dn], %[dn], %[s2], %[s2]"
#define P2_ODD "v_cvt_pk_bf16_f32 %[o], %[v], %[t0]\n\tv_fma_f32 %[dn], %[dn], %[s2], %[s2]\n\tv_min_u32 %[o], -2, %[o]"
  if constexpr (FIRST) P2B("0", "0", "%[v]", P2_EVEN);
  else if constexpr (ODD) P2B("a[16:31]", "a[32:47]", "%[t0]", P2_ODD);
  else P2B("a[16:31]", "a[32:47]", "%[v]", P2_EVEN);
}
DEV void set_m0(u32 lds) { asm volatile("s_mov_b32 m0, %0" :: "s"(lds) : "memory"); }
DEV void dma16w(v4u srd, u32 voff, u32 soff) { asm volatile("buffer_load_dwordx4 %0, %1, %2 offen" WSC " lds" :: "v"(voff), "s"(srd), "s"(soff) : "memory"); }
DEV void dma16n(v4u srd, u32 voff, u32 soff) { asm volatile("buffer_load_dwordx4 %0, %1, %2 offen sc1 lds" :: "v"(voff), "s"(srd), "s"(soff) : "memory"); }
#define G2_TXT(Z0, Z1, C) \
  "v_mfma_f32_32x32x16_bf16 a[%c[d0]:%c[d0]+15], a[%c[wa]:%c[wa]+3], %[b], " Z0 "\n\t" SW_A SW_B \
  "v_mfma_f32_32x32x16_bf16 a[%c[d0]+32:%c[d0]+47], a[%c[wa]+4:%c[wa]+7], %[b], " Z1 "\n\t" C
#define G2_OPS : [dn] "+v"(st.dn), [p] "+v"(st.p), [v] "+v"(st.v), [o] "=&v"(o), [rc] "=&v"(rc), [ag] "=&v"(ag), [au] "=&v"(au) \
  : [b] "v"(b), [m] "v"(m), [rl] "v"(rl), [s2] "v"(s2), [cg] "v"(cg), [cu] "v"(cu), [q] "i"(Q), [q8] "i"(Q + 8), [d0] "i"(D0), [wa] "i"(WA)
template <bool FIRST, bool ODD, int Q, int D0, int WA>
DEV void g2_pair(const v4u& b, Sw& st, u32& o, float m, float rl, float s2, float cg, float cu) {
  float rc, ag, au;
  if constexpr (FIRST && ODD) asm volatile(G2_TXT("0", "0", SW_C1) G2_OPS);
  else if constexpr (FIRST) asm volatile(G2_TXT("0", "0", SW_C0) G2_OPS);
  else if constexpr (ODD) asm volatile(G2_TXT("a[%c[d0]:%c[d0]+15]", "a[%c[d0]+32:%c[d0]+47]", SW_C1) G2_OPS);
  else asm volatile(G2_TXT("a[%c[d0]:%c[d0]+15]", "a[%c[d0]+32:%c[d0]+47]", SW_C0) G2_OPS);
}
template <int A, int OFF> DEV void dsr_agpr(u32 ad) { asm volatile("ds_read_b128 a[%c1:%c2], %0 offset:%c3" :: "v"(ad), "i"(A), "i"(A + 3), "i"(OFF) : "memory"); }
DEV void mfma16(v4f& c, const v4u& a, const v4u& b) { asm volatile("v_mfma_f32_16x16x32_f16 %0, %1, %2, %0" : "+v"(c) : "v"(a), "v"(b)); }
DEV void mfma16z(v4f& c, const v4u& a, const v4u& b) { asm volatile("v_mfma_f32_16x16x32_f16 %0, %1, %2, 0" : "=v"(c) : "v"(a), "v"(b)); }
constexpr int A_W = 0, A_ACC1 = 128, A_ACC2 = 0, A_PRE = 192;

constexpr int TCOL = 528, NCOL = 63, TAB_B = NCOL * TCOL;
constexpr int KC_B = 4640, UC_B = 4224;
constexpr int OFF_K = TAB_B, OFF_U = OFF_K + 4 * KC_B, OFF_X = OFF_U + 2 * UC_B, CH_B = OFF_X + 4096;
constexpr int L_RT1 = 2 * CH_B, L_RT2 = L_RT1 + 128 * 144, L_MISC = L_RT2 + 64 * 144;
constexpr int L_W2 = 131072;
constexpr int L_CSP = L_MISC, L_CS = L_CSP + 4096, L_MR = L_CS + 1024, LDS_B = L_MR + 2048;
static_assert(L_W2 >= L_RT1 && L_W2 + 24576 <= L_MISC && LDS_B <= 163840 && L_MISC % 16 == 0, "lds map");

typedef v2f cf;
DEV void cmul2(cf& r0, cf a0, cf b0, cf& r1, cf a1, cf b1) {
  asm("v_pk_mul_f32 %0, %2, %3 op_sel_hi:[0,1]\n\t"
      "v_pk_mul_f32 %1, %4, %5 op_sel_hi:[0,1]\n\t"
      "v_pk_fma_f32 %0, %2, %3, %0 op_sel:[1,1,0] op_sel_hi:[1,0,1] neg_lo:[1,0,0]\n\t"
      "v_pk_fma_f32 %1, %4, %5, %1 op_sel:[1,1,0] op_sel_hi:[1,0,1] neg_lo:[1,0,0]"
      : "=&v"(r0), "=&v"(r1) : "v"(a0), "v"(b0), "v"(a1), "v"(b1));
}
DEV cf cexp_p(float ar, float ai, float p) {
  const float e = __builtin_amdgcn_exp2f(ar * p * 1.44269504088896340736f);
  const float xx = ai * p;
  const float chi = 0.15915493667125702f, clo = 6.4206383e-09f;
  const float th = xx * chi;
  float tl = __builtin_fmaf(xx, chi, -th);
  tl = __builtin_fmaf(xx, clo, tl);
  const float fr = (th - __builtin_rintf(th)) + tl;
  return cf{e * __builtin_amdgcn_cosf(fr), e * __builtin_amdgcn_sinf(fr)};
}
template <int CTRL> DEV u32 dppu(u32 v) { return (u32)__builtin_amdgcn_update_dpp(0, (int)v, CTRL, 0xf, 0xf, true); }
DEV u32 wave_umax(u32 v) {
  v = max(v, dppu<0xB1>(v)); v = max(v, dppu<0x4E>(v)); v = max(v, dppu<0x141>(v)); v = max(v, dppu<0x140>(v));
  auto r = __builtin_amdgcn_permlane16_swap(v, v, false, false); v = max(r[0], r[1]);
  auto q = __builtin_amdgcn_permlane32_swap(v, v, false, false); return max(q[0], q[1]);
}

struct ConvSched { int mc[36], mq[36], need[36], ru[16], ri[16]; };
constexpr ConvSched mk_sched() {
  ConvSched s{};
  bool hu[8] = {}, hk[8] = {};
  int nm = 0, nr = 0;
  for (int q = 0; q < 8; ++q)
    for (int c = 7; c >= q; --c) {
      if (!hu[q]) { hu[q] = true; s.ru[nr] = 1; s.ri[nr] = q; ++nr; }
      if (!hk[c - q]) { hk[c - q] = true; s.ru[nr] = 0; s.ri[nr] = c - q; ++nr; }
      s.mc[nm] = c; s.mq[nm] = q; s.need[nm] = nr; ++nm;
    }
  return s;
}
constexpr ConvSched CSCH = mk_sched();

template <class Ex> DEV void conv_main(u32 ub, u32 kb, v4f (&acc)[8], Ex&& extra) {
  v4u U[2][8], K[2][8];
  auto rd = [&](auto r, auto i) {
    if constexpr (CSCH.ru[i]) dsr128<64 * r + 512 * CSCH.ri[i]>(U[r & 1][CSCH.ri[i]], ub);
    else dsr128<64 * r + 512 * (7 - CSCH.ri[i])>(K[r & 1][CSCH.ri[i]], kb);
  };
  sfor<0, 16>([&](auto i) { rd(IC<0>{}, i); });
  SB();
  sfor<0, 4>([&](auto r) {
    constexpr int cur = r & 1;
    sfor<0, 36>([&](auto m) {
      if constexpr (m % 3 == 0) {
        constexpr int n = 16 - CSCH.need[m + 2] + (r < 3 ? (m < 32 ? (m + 1) / 2 : 16) : 0);
        if constexpr (m == 0 || CSCH.need[m + 2] > CSCH.need[m > 0 ? m - 1 : 0]) asm volatile("s_waitcnt lgkmcnt(%c0)" :: "i"(n < 15 ? n : 15) : "memory");
      }
      constexpr int c = CSCH.mc[m], q = CSCH.mq[m];
      if constexpr (r == 0 && q == 0) mfma16z(acc[c], U[cur][q], K[cur][c - q]);
      else mfma16(acc[c], U[cur][q], K[cur][c - q]);
      if constexpr (r < 3 && m < 32 && m % 2 == 0) rd(IC<r + 1>{}, IC<m / 2>{});
      extra(r, m);
      SB();
    });
  });
  asm volatile("s_nop 7" : "+v"(acc[0]), "+v"(acc[1]), "+v"(acc[2]), "+v"(acc[3]), "+v"(acc[4]), "+v"(acc[5]), "+v"(acc[6]), "+v"(acc[7]));
}

__global__ __launch_bounds__(256, 1) void k_fused(const u16* __restrict__ x, const u16* __restrict__ lar_, const u16* __restrict__ aim_,
                                                  const u16* __restrict__ cre_, const u16* __restrict__ cim_, const u16* __restrict__ ldt_,
                                                  const u16* __restrict__ dsk_, const u16* __restrict__ wg, const u16* __restrict__ wu,
                                                  const u16* __restrict__ wd, char* __restrict__ ws, u16* __restrict__ out) {
  extern __shared__ __attribute__((aligned(16))) char smx[];
  LAS char* sm = (LAS char*)smx;
  asm volatile("" ::: "a0", "a255");
  const int t = threadIdx.x, lane = t & 63, w = __builtin_amdgcn_readfirstlane(t >> 6), b = blockIdx.x;
  const int xcd = b & 7, bi = b >> 3;
  u32 pfv;
  {
    u64 pc; u32 len;
    asm volatile("s_getpc_b64 %0\n.Lkf_here:\n\ts_mov_b32 %1, .Lkf_end-.Lkf_here" : "=s"(pc), "=s"(len));
    const u32 off = min((u32)((bi * 32 + (t & 31)) * 64), len);
    asm volatile("global_load_dword %0, %1, %2" : "=v"(pfv) : "v"(off), "s"(pc));
  }
  SB();
  const auto rs0 = __builtin_amdgcn_make_buffer_rsrc(ws, 0, WS_FIX, 0x00020000);
  const v4u srd0 = {(u32)(uintptr_t)ws, (u32)((uintptr_t)ws >> 32) & 0xffffu, WS_FIX, 0x00020000u};
  const int pr = xcd * 32 + bi;
  const int cw = w >> 1, nh = w & 1, ch = 2 * pr + cw;
  const int mt = 4 * (xcd >> 1) + (bi & 3), j = 8 * (xcd & 1) + (bi >> 2);
  const int e2 = j & 1, dg = j >> 1;
  const int G = lane >> 4, q4 = (lane >> 2) & 3, p4 = lane & 3, i16 = lane & 15;
  const u32 lane16 = lane * 16;
  LAS char* cb = sm + cw * CH_B;

  v4u xa[2];
#pragma unroll
  for (int i = 0; i < 2; ++i) xa[i] = *(const v4u*)(x + (size_t)(8 * b + 2 * w + i) * D + lane * 8);
  u32 xq[4][2];
#pragma unroll
  for (int i = 0; i < 4; ++i)
#pragma unroll
    for (int k = 0; k < 2; ++k) xq[i][k] = *(const u32*)(x + (size_t)(8 * b + 2 * (lane & 3) + k) * D + 2 * (64 * w + 16 * i + (lane >> 2)));
  const u32 ep = __builtin_amdgcn_raw_buffer_load_b32(rs0, WS_EPOCH, 0, 16);
  const int n = 64 * nh + lane;
  const u32 r_lar = lar_[ch * NS + n], r_aim = aim_[ch * NS + n], r_cre = cre_[ch * NS + n], r_cim = cim_[ch * NS + n];
  const u32 r_cre2 = cre_[ch * NS + (n ^ 64)], r_cim2 = cim_[ch * NS + (n ^ 64)];
  const u32 r_ldt = ldt_[ch], r_dsk = dsk_[ch];
  SB();
  {
    const v4u s = {~0u, ~0u, ~0u, ~0u};
#pragma unroll
    for (int i = 0; i < 2; ++i) __builtin_amdgcn_raw_buffer_store_b128(s, rs0, t * 16 + i * HROW, WS_HT + pr * 2 * HROW, 16);
  }
  SB();
  const int rb = b < 224 ? b : 223;
  const int mat = rb / 112, cg = (rb % 112) % 28, rq = (rb % 112) / 28, dgp = rb & 7, fb = rb >> 3;
  SB();
  {
    const v4u z = {0, 0, 0, 0};
    const int tt = t & 127;
    LAS char* c2 = sm + (t >> 7) * CH_B;
    *(LAS v4u*)(c2 + OFF_K + (tt / 33) * KC_B + (255 + tt % 33) * 16) = z;
    if (tt < 4) *(LAS v4u*)(c2 + OFF_K + 3 * KC_B + (255 + 29 + tt) * 16) = z;
    if (tt < 16) *(LAS v4u*)(c2 + OFF_U + (tt >> 3) * UC_B + (tt & 7) * 16) = z;
  }
  const int g16 = 4 * cg + w, jj = g16 / 7, ptw = g16 % 7, hb = G & 1;
  LAS char* rt1 = sm + L_RT1 + (16 * (G >> 1) + 8 * hb + q4) * 144 + 32 * w + 8 * p4;
  LAS char* rt2 = sm + L_RT2 + (16 * (G >> 1) + 4 * hb + q4) * 144 + 32 * w + 8 * p4;
  LAS char* rtw = sm + L_RT1 + (t >> 3) * 144 + (t & 7) * 16;
  u32 vo1 = ((ptw < 4 ? 1 : 3) * (8 * rq + (G >> 1)) + (ptw < 4 ? ptw * WPT : 4 * WPT - 4 + ptw)) * 1024 + (32 * hb + 16 * mat + i16) * 16, vo2[2];
  const u32 so1 = WS_WT1 + jj * WJ, inc1 = ptw < 4 ? 2048 : 6144;
#pragma unroll
  for (int it = 0; it < 2; ++it) {
    const int sx = 4 * fb + 2 * it + (G >> 1), jp = sx / 7, pp = sx % 7;
    const int pos = pp < 4 ? (pp >> 1) * 8 + (jp & 3) * 2 + (pp & 1) : 16 + (jp & 3) * 3 + (pp - 4);
    vo2[it] = ((jp >> 2) * WDW + pos * 2 + (w >> 1)) * 1024 + (32 * hb + (i16 & 3) + 8 * (i16 >> 2) + 4 * (w & 1)) * 16;
  }
  LAS char* ap[4];
  {
    const int c2 = (i16 + ((i16 >> 1) & 1)) >> 1, c4 = (i16 + 1 + ((i16 >> 2) & 1)) >> 2, c8 = (i16 + 3 + ((i16 >> 3) & 1)) >> 3;
    ap[0] = cb + (nh ? 32 + c2 : 16 + i16) * TCOL + G * 16;
    ap[1] = cb + (41 + 5 * nh + c4) * TCOL + G * 16;
    ap[2] = cb + (51 + 3 * nh + c8) * TCOL + G * 16;
    ap[3] = cb + i16 * TCOL + G * 16;
  }
  LAS char* kp = cb + OFF_K + 16 + (2038 - 256 * nh - 16 * i16 - 4 * G - 3 * 512) * 2;
  u32 ub = cw * CH_B + OFF_U + (i16 & 1) * UC_B + ((i16 >> 1) + G) * 16 + 256 * nh;
  u32 kb = cw * CH_B + OFF_K + (i16 & 3) * KC_B + 16 + (248 - 8 * (i16 >> 2) + G) * 16 - 7 * 512 + 256 * nh;
  u32 fo;
  {
    const int gq = lane < 8 ? 7 * j + w : 7 * j + 4 + ((lane - 8) >> 3), k = lane < 60 ? lane - 32 : 0;
    fo = (lane < 32 ? ((lane & 1) * 112 + ((lane >> 1) & 3) * 28 + (gq >> 2)) * 4 + (gq & 3) : ((7 * w + (k >> 2)) * 8 + dg) * 4 + (k & 3)) * 128;
  }
  LAS char* he[2];
  he[0] = cb + (16 * G + 2 * (i16 & 3) + 64 * (i16 >> 2) + 256 * nh) * 2; he[1] = he[0] + (3 - 2 * nh) * 512;
  u32 ho = (256 * ((lane >> 5) ? 3 - nh : nh) + (lane & 31) * 8) * 2;
  u32 t16 = t * 16;
  asm volatile("" : "+v"(rt1), "+v"(rt2), "+v"(rtw), "+v"(vo1), "+v"(vo2[0]), "+v"(vo2[1]), "+v"(ap[0]), "+v"(ap[1]), "+v"(ap[2]), "+v"(ap[3]),
               "+v"(kp), "+v"(ub), "+v"(kb), "+v"(fo), "+v"(he[0]), "+v"(he[1]), "+v"(ho), "+v"(t16));
  SB();
  const u32 tag = __builtin_amdgcn_readfirstlane(ep) + 1u;
  char* const wr = ws + WS_ROT + (size_t)(tag & 3u) * WS_USED;
  const auto rs = __builtin_amdgcn_make_buffer_rsrc(wr, 0, WS_USED, 0x00020000);
  const v4u srd = {(u32)(uintptr_t)wr, (u32)((uintptr_t)wr >> 32) & 0xffffu, WS_USED, 0x00020000u};
#pragma unroll
  for (int i = 0; i < 4; ++i)
    __builtin_amdgcn_raw_buffer_store_b128(v4u{xq[i][0], tag, xq[i][1], tag}, rs, (16 * i + (lane >> 2)) * 16384 + (lane & 3) * 16, WS_XG + w * (64 * 16384) + b * 64, 16);
  SB();
  v4u wa[4], wb[2];
  {
    const u16* W = mat ? wu : wg;
#pragma unroll
    for (int i = 0; i < 4; ++i) wa[i] = *(const v4u*)(W + (size_t)(128 * rq + (t >> 3) + 32 * i) * F + 64 * cg + (t & 7) * 8);
#pragma unroll
    for (int i = 0; i < 2; ++i) wb[i] = *(const v4u*)(wd + (size_t)(64 * fb + (t >> 3) + 32 * i) * D + 64 * dgp + (t & 7) * 8);
  }
  SB();
  {
    float v[2][8], s[2], q[2];
#pragma unroll
    for (int i = 0; i < 2; ++i) {
#pragma unroll
      for (int k = 0; k < 4; ++k) { v[i][2 * k] = bflo(xa[i][k]); v[i][2 * k + 1] = bfhi(xa[i][k]); }
      s[i] = ((v[i][0] + v[i][1]) + (v[i][2] + v[i][3])) + ((v[i][4] + v[i][5]) + (v[i][6] + v[i][7]));
    }
#pragma unroll
    for (int i = 0; i < 2; ++i) s[i] = wave_sum(s[i]) * (1.0f / D);
#pragma unroll
    for (int i = 0; i < 2; ++i) {
      q[i] = 0;
#pragma unroll
      for (int k = 0; k < 8; ++k) { const float d = v[i][k] - s[i]; q[i] += d * d; }
    }
#pragma unroll
    for (int i = 0; i < 2; ++i) q[i] = wave_sum(q[i]) * (1.0f / D);
    const float m = (lane & 1) ? s[1] : s[0], vv = (lane & 1) ? q[1] : q[0];
    const u32 pk = pkbf(m, __builtin_sqrtf(rbf(rbf(vv) + 1e-5f)));
    SB();
    WAITV(10);
    if (lane < 16) __builtin_amdgcn_raw_buffer_store_b64(v2u{pk, tag}, rs, (lane >> 1) * 16384 + (lane & 1) * 8, WS_GRAN + (8 * b + 2 * w) * 8, 16);
    SB();
  }
  const float dt = rbf(expf(bf2f(r_ldt)));
  const float ar = -rbf(expf(bf2f(r_lar))) * dt, ai = bf2f(r_aim) * dt;
  int ex = (int)(wave_umax(max(max(r_cre & 0x7fffu, r_cim & 0x7fffu), max(r_cre2 & 0x7fffu, r_cim2 & 0x7fffu))) >> 7);
  ex = ex < 8 ? 8 : (ex > 250 ? 250 : ex);
  const float scl = __uint_as_float((u32)(257 - ex) << 23);
  const float yscl = 2.0f * dt * __uint_as_float((u32)(ex - 3) << 23);
  {
    LAS char* tb = cb + n * 4;
#define PUT(col, v) *(LAS u32*)(tb + (col) * TCOL) = pkh((v)[0], (v)[1])
    cf zp[17], zb[8], a[16], pa, pb;
    zp[1] = cexp_p(ar, ai, 1.0f); zp[16] = cexp_p(ar, ai, 16.0f); zb[1] = cexp_p(ar, ai, 256.0f);
    const cf wc = {zp[16][0], -zp[16][1]};
    a[0] = cf{bf2f(r_cre) * scl, -bf2f(r_cim) * scl};
    *(LAS u32*)(tb + 16 * TCOL) = 0x00003c00u;
    PUT(17, zp[1]); PUT(32, zb[1]); PUT(0, a[0]);
    cmul2(zp[2], zp[1], zp[1], zb[2], zb[1], zb[1]);
    cmul2(zp[3], zp[2], zp[1], zp[4], zp[2], zp[2]);
    cmul2(zb[3], zb[2], zb[1], zb[4], zb[2], zb[2]);
    cmul2(a[1], a[0], wc, zp[8], zp[4], zp[4]);
    PUT(18, zp[2]); PUT(19, zp[3]); PUT(20, zp[4]); PUT(41, zb[2]); PUT(46, zb[3]); PUT(51, zb[4]);
    cmul2(a[2], a[1], wc, zp[5], zp[4], zp[1]);
    cmul2(a[3], a[2], wc, zp[6], zp[4], zp[2]);
    cmul2(a[4], a[3], wc, zp[7], zp[4], zp[3]);
    PUT(1, a[1]); PUT(2, a[2]); PUT(3, a[3]); PUT(4, a[4]); PUT(21, zp[5]); PUT(22, zp[6]); PUT(23, zp[7]); PUT(24, zp[8]);
    sfor<9, 16>([&](auto i) { cmul2(a[i - 4], a[i - 5], wc, zp[i], zp[8], zp[i - 8]); PUT(i - 4, a[i - 4]); PUT(16 + i, zp[i]); });
    cmul2(a[12], a[11], wc, zb[5], zb[4], zb[1]);
    cmul2(a[13], a[12], wc, zb[6], zb[4], zb[2]);
    cmul2(a[14], a[13], wc, zb[7], zb[4], zb[3]);
    cmul2(a[15], a[14], wc, pa, zb[1], zp[16]);
    PUT(12, a[12]); PUT(13, a[13]); PUT(14, a[14]); PUT(15, a[15]); PUT(54, zb[5]); PUT(57, zb[6]); PUT(60, zb[7]); PUT(40, pa);
    sfor<0, 3>([&](auto i) { cmul2(pa, zb[1], zp[4 * i + 2], pb, zb[1], zp[4 * i + 4]); PUT(33 + 2 * i, pa); PUT(34 + 2 * i, pb); });
    cmul2(pa, zb[1], zp[14], pb, zb[1], zp[14]); PUT(39, pa);
    sfor<1, 5>([&](auto i) { cmul2(pa, zb[2], zp[4 * i], pb, zb[3], zp[4 * i]); PUT(41 + i, pa); PUT(46 + i, pb); });
    sfor<4, 8>([&](auto m) { cmul2(pa, zb[m], zp[8], pb, zb[m], zp[16]); PUT(52 + 3 * (m - 4), pa); PUT(53 + 3 * (m - 4), pb); });
#undef PUT
  }
#pragma unroll
  for (int i = 0; i < 4; ++i) *(LAS v4u*)(rtw + 32 * i * 144) = wa[i];
#pragma unroll
  for (int i = 0; i < 2; ++i) *(LAS v4u*)(rtw + (L_RT2 - L_RT1) + 32 * i * 144) = wb[i];
  BAR();
  v4u o1[6];
#pragma unroll
  for (int it = 0; it < 4; ++it) o1[it] = trfrag(rt1 + it * 4608, 4 * 144);
#pragma unroll
  for (int it = 0; it < 2; ++it) o1[4 + it] = trfrag(rt2 + it * 4608, 8 * 144);
  auto rt_st = [&](int it) {
    if (b < 224) {
      if (it < 4) __builtin_amdgcn_raw_buffer_store_b128(o1[it], rs, vo1, so1 + it * inc1, 16);
      else __builtin_amdgcn_raw_buffer_store_b128(o1[it], rs, vo2[it - 4], WS_WT2 + dgp * WJ, 16);
    }
  };
  {
    float ca = 0, cc = 0;
#pragma unroll
    for (int it = 0; it < 6; ++it) rt_st(it);
#pragma unroll
    for (int it = 0; it < 4; ++it) {
      ca = dot2bf(o1[it][0], 0x3f803f80u, ca); cc = dot2bf(o1[it][1], 0x3f803f80u, cc);
      ca = dot2bf(o1[it][2], 0x3f803f80u, ca); cc = dot2bf(o1[it][3], 0x3f803f80u, cc);
    }
    ca = sum32x(sum16x(ca + cc));
    if (lane < 16 && b < 224) __builtin_amdgcn_raw_buffer_store_b32(__float_as_uint(ca), rs, lane * 4, WS_CSUM + jj * 4096 + (rq * 224 + mat * 112 + ptw * 16) * 4, 16);
  }
  SB();
  v4u g[8];
  u32 fl;
  auto gather = [&]() {
    sfor<0, 4>([&](auto i) { ld_vgpr<0>(g[i], srd, t16, WS_XG + pr * 16384 + i * 4096); });
    sfor<0, 4>([&](auto i) { ld_vgpr<0>(g[4 + i], srd, t16, WS_GRAN + xcd * 16384 + i * 4096); });
  };
  gather();
  const u32 sW1 = WS_WT1 + j * WJ + w * WPT * 1024, sW2 = WS_WT1 + j * WJ + 4 * WPT * 1024;
  auto ld_w1 = [&](auto k) { ld_agpr<A_W + 4 * k, (k & 3) * 1024>(srd, lane16, sW1 + (k >> 2) * 4096); };
  {
    v4u Bf[8], Af[2][8];
#pragma unroll
    for (int ks = 0; ks < 8; ++ks) { Bf[ks] = *(LAS v4u*)(ap[3] + ks * 64); Af[0][ks] = *(LAS v4u*)(ap[0] + ks * 64); }
    SB();
    v4f d[4];
    sfor<0, 4>([&](auto mm) {
      if constexpr (mm == 2) {
    SB();
    WAITV(0);
    asm volatile("" :: "v"(pfv));
    if (lane == 0 && b < 224) __builtin_amdgcn_raw_buffer_store_b32(tag, rs, 0, WS_WFLAG + (b * 4 + w) * 128, 16);
    SB();
      }
      sfor<0, 8>([&](auto ks) {
        if constexpr (ks == 0) mfma16z(d[mm], Af[mm & 1][ks], Bf[ks]);
        else mfma16(d[mm], Af[mm & 1][ks], Bf[ks]);
        if constexpr (mm < 3) Af[(mm & 1) ^ 1][ks] = *(LAS v4u*)(ap[mm < 2 ? mm + 1 : 2] + (mm == 2 ? 6 * TCOL : 0) + ks * 64);
        SB();
      });
    });
    asm volatile("s_nop 7" : "+v"(d[0]), "+v"(d[1]), "+v"(d[2]), "+v"(d[3]));
    sfor<0, 4>([&](auto mm) {
      const u32 p0 = pkh(d[mm][3], d[mm][2]), p1 = pkh(d[mm][1], d[mm][0]);
#pragma unroll
      for (int e = 0; e < 4; ++e) {
        LAS char* k2 = kp + (3 - mm) * 1024 + e * (KC_B + 4);
        if (e & 1) *(LAS v2u*)k2 = v2u{p0, p1};
        else { *(LAS u32*)k2 = p0; *(LAS u32*)(k2 + 4) = p1; }
      }
      SB();
    });
  }
  {
    waitv8<0>(g);
    for (int spin = 0; spin < SPIN_CAP; ++spin) {
      u32 df = 0;
#pragma unroll
      for (int i = 0; i < 8; ++i) df |= (g[i][1] ^ tag) | (g[i][3] ^ tag);
      asm volatile("" : "+v"(df));
      if (__all(df == 0)) break;
      __builtin_amdgcn_s_sleep(10);
      gather();
      waitv8<0>(g);
      }
    ld_flag(fl, srd, fo, WS_WFLAG);
    LAS char* cc[2] = {sm + t * 4, sm + CH_B + t * 4};
    asm volatile("" : "+v"(cc[1]));
    const u32 ub0 = t * 4, ub1 = CH_B + t * 4;
    sfor<0, 4>([&](auto i) {
      const float m0 = bflo(g[4 + i][0]), m1 = bflo(g[4 + i][2]);
      const float r0 = __builtin_amdgcn_rcpf(bfhi(g[4 + i][0])), r1 = __builtin_amdgcn_rcpf(bfhi(g[4 + i][2]));
#pragma unroll
      for (int c = 0; c < 2; ++c) {
        const u32 d = pkbf((c ? bfhi(g[i][0]) : bflo(g[i][0])) - m0, (c ? bfhi(g[i][2]) : bflo(g[i][2])) - m1);
        const u32 o = pkbf(bflo(d) * r0, bfhi(d) * r1);
        const u32 uh = pkh(bflo(o), bfhi(o));
        LAS char* c2 = cc[c] + i * 1024;
        *(LAS u32*)(c2 + OFF_U + UC_B + 112) = uh;
        dsw_halves<OFF_U + 114 + i * 1024>(c ? ub1 : ub0, uh);
        *(LAS u32*)(c2 + OFF_X) = __builtin_amdgcn_perm(g[i][2], g[i][0], c ? 0x07060302u : 0x05040100u);
      }
    });
  }
  wait0_pin(fl);
  for (int spin = 0; spin < SPIN_CAP && !__all(fl == tag); ++spin) { __builtin_amdgcn_s_sleep(8); ld_flag(fl, srd, fo, WS_WFLAG); wait0_pin(fl); }
  sfor<0, 6>([&](auto k) { ld_w1(k); });
  BAR();
  if (b == 0 && t == 0) __builtin_amdgcn_raw_buffer_store_b32(tag, rs0, 0, WS_EPOCH, 16);
  const u32 sWD = WS_WT2 + dg * WJ + w * WDW * 1024;
  auto pre_wd = [&](auto f) {
    if constexpr (f < 12) ld_agpr<A_PRE + 4 * f, (f & 3) * 1024>(srd, lane16, sWD + (e2 * 16 + (f & 12)) * 1024);
    else ld_agpr<240 + 4 * (f - 12), (f & 1) * 1024>(srd, lane16, sWD + 32768 + ((f - 12) >> 1) * 6144);
  };
  v4f acc[4];
  v2h uq[8];
  u32 xv[8], sk[8];
  const float dsk = bf2f(r_dsk);
  {
    auto extra = [&](auto r, auto m) {
      constexpr int sl = r * 36 + m;
      if constexpr (sl % 8 == 5 && sl < 56) {
        constexpr int i = sl / 8;
        __builtin_amdgcn_raw_buffer_store_b128(v4u{~0u, ~0u, ~0u, ~0u}, rs, lane16 + (i & 3) * 1024, WS_ACT + ((mt * 4 + w) * AT + j * 7 + (j >> 2) + (i & 4)) * 1024, 16);
      }
      if constexpr (r == 3 && m < 8) {
        uq[m] = *(LAS v2h*)(he[(m >> 1) & 1] + OFF_U + UC_B + 112 + (m >> 2) * 2048 + (m & 1) * 16);
        xv[m] = *(LAS u32*)(he[(m >> 1) & 1] + OFF_X + (m >> 2) * 2048 + (m & 1) * 16);
      }
      if constexpr (sl % 6 == 3 && sl < 132) {
        constexpr int k = sl / 6 < 16 ? 6 + sl / 6 : 16 + sl / 6;
        if constexpr (k < 32) ld_w1(IC<k>{});
        else { set_m0(L_W2 + (w * 6 + k - 32) * 1024); asm volatile("s_nop 0"); dma16w(srd, lane16, sW2 + (w * 6 + k - 32) * 1024); }
      }
    };
    v4f ac[8];
    conv_main(ub, kb, ac, extra);
    LAS char* xp = cb + lane16;
    auto give = [&](auto N) {
      sfor<0, 4>([&](auto a) {
        *(LAS v4f*)(xp + (4 * N + a) * 1024) = ac[4 * (a >> 1) + ((a & 1) ? 2 + N : 1 - N)];
        acc[a] = ac[4 * (a >> 1) + ((a & 1) ? 3 - N : N)];
      });
    };
    if (nh == 0) give(IC<0>{}); else give(IC<1>{});
#pragma unroll
    for (int m = 0; m < 8; ++m) { sk[m] = pkbf((float)uq[m][0] * dsk, (float)uq[m][1] * dsk); asm volatile("" : "+v"(sk[m])); }
    BAR();
#pragma unroll
    for (int a = 0; a < 4; ++a) acc[a] += *(LAS v4f*)(xp + (4 * (1 - nh) + a) * 1024);
  }
  {
#pragma unroll
    for (int m = 0; m < 8; ++m) {
      const u32 yb = pkbf(acc[m >> 1][2 * (m & 1)] * yscl, acc[m >> 1][2 * (m & 1) + 1] * yscl);
      const u32 zz = pkbf(bflo(yb) + bflo(sk[m]), bfhi(yb) + bfhi(sk[m]));
      *(LAS u32*)(he[(m >> 1) & 1] + OFF_X + (m >> 2) * 2048 + (m & 1) * 16) =
          min(pkbf(bflo(xv[m]) + bflo(zz), bfhi(xv[m]) + bfhi(zz)), 0xfffffffeu);
    }
    WAITV(13);
    WAITL0();
#pragma unroll
    for (int i = 0; i < 2; ++i)
      __builtin_amdgcn_raw_buffer_store_b128(*(LAS v4u*)(cb + OFF_X + ho + i * 2048), rs0, ho + i * 2048, WS_HT + ch * HROW, 16);
  }
  sfor<0, 16>([&](auto f) { pre_wd(f); });
  BAR();
  const u32 vtok = (lane >> 4) * HROW + (((lane & 15) ^ (4 * (lane >> 4))) * 16);
  const u32 stok = WS_HT + mt * 256 + w * 16 * HROW;
  auto tok_dma = [&](int c) {
#pragma unroll
    for (int i = 0; i < 4; ++i) dma16(srd0, vtok, stok + (64 * c + 4 * i) * HROW, c * 16384 + (4 * w + i) * 1024);
  };
  dma16(srd, lane16, WS_CSUM + j * 4096 + w * 1024, L_CSP + w * 1024);
#pragma unroll
  for (int c = 0; c < 3; ++c) tok_dma(c);
  LAS char* tp[4][2];
#pragma unroll
  for (int i = 0; i < 4; ++i) {
    tp[i][0] = sm + (8 * (G >> 1) + q4) * 256 + 8 * (p4 & 1) + (((4 * (w ^ i) + 2 * (G & 1) + (p4 >> 1)) ^ (4 * q4)) * 16);
    tp[i][1] = tp[i][0] + 65536;
    asm volatile("" : "+v"(tp[i][1]));
  }
  LAS char* vp[2];
  vp[0] = sm + w * 4096 + lane16; vp[1] = vp[0] + 65536;
  asm volatile("" : "+v"(vp[1]));
  v4u own[32], vq[4];
  u32 vm = 0;
  float sA = 0, sB = 0, qA = 0, qB = 0;
  auto mx = [&](u32 m, const v4u& v) { return max(max(m, max(v[0], v[1])), max(v[2], v[3])); };
  auto own_rd = [&](auto s) { own[s] = trfrag(tp[0][s >> 4] + (s & 15) * 4096, 1024); };
  auto own_acc = [&](auto s) {
#pragma unroll
    for (int e = 0; e < 4; e += 2) {
      sA = dot2bf(own[s][e], 0x3f803f80u, sA); qA = dot2bf(own[s][e], own[s][e], qA);
      sB = dot2bf(own[s][e + 1], 0x3f803f80u, sB); qB = dot2bf(own[s][e + 1], own[s][e + 1], qB);
    }
    pin2(sA, qA); pin2(sB, qB);
  };
  auto v_rd = [&](auto c, auto i) { vq[i] = *(LAS v4u*)(vp[c >> 2] + (c & 3) * 16384 + i * 1024); };
  auto fix = [&](auto c0, auto c1) {
    for (int spin = 0; spin < SPIN_CAP && __any(vm == ~0u); ++spin) {
      __builtin_amdgcn_s_sleep(4);
#pragma unroll
      for (int c = c0; c < c1; ++c) tok_dma(c);
      WAITV(0);
      vm = 0;
      sfor<c0, c1>([&](auto c) {
        sfor<0, 4>([&](auto i) { v_rd(c, i); });
        sfor<0, 4>([&](auto i) { vm = mx(vm, vq[i]); });
      });
    }
    vm = 0;
  };
  WAITV(8);
  sfor<0, 4>([&](auto i) { v_rd(IC<0>{}, i); });
  sfor<0, 4>([&](auto i) { vm = mx(vm, vq[i]); });
  SB();
  WAITV(4);
  sfor<0, 4>([&](auto i) { v_rd(IC<1>{}, i); });
  sfor<0, 4>([&](auto i) { vm = mx(vm, vq[i]); });
  fix(IC<0>{}, IC<2>{});
  BAR();
  auto w2_lds = [&](int k) { return (k / 3) * 16384 + (3 * w + k % 3) * 1024; };
  auto w2_src = [&](int k) { return sW2 + (2 + k / 3) * 12288 + (3 * w + k % 3) * 1024; };
  auto ld_wd = [&](auto f) { ld_agpr<A_W + 4 * f, (f & 3) * 1024>(srd, lane16, sWD + ((f < 16 ? e2 : 1 - e2) * 16 + (f & 12)) * 1024); };
  v4u hres[2];
  {
    v4u ot[2][3];
    float lm = 0, lv = 0;
    LAS char* hp = sm + (64 * dg + 32 * (w >> 1) + 16 * (G >> 1) + q4) * 256 + 8 * (p4 & 1) +
                   (((4 * (2 * e2 + (w & 1)) + 2 * (G & 1) + (p4 >> 1)) ^ (4 * q4)) * 16);
    auto ot_rd = [&](auto s, auto i) { ot[s & 1][i] = trfrag(tp[i + 1][s >> 4] + (s & 15) * 4096, 1024); };
    own_rd(IC<0>{});
    sfor<0, 3>([&](auto i) { ot_rd(IC<0>{}, i); });
    if (t < 224) {
      LAS float* cp = (LAS float*)(sm + L_CSP) + t;
      *(LAS float*)(sm + L_CS + t * 4) = (cp[0] + cp[224]) + (cp[448] + cp[672]);
    }
    SB();
    sfor<0, 32>([&](auto s) {
      constexpr int vc = s == 2 ? 2 : s == 5 ? 3 : s == 10 ? 4 : s == 13 ? 5 : s == 17 ? 6 : s == 19 ? 7 : -1;
      constexpr int vy = s == 2 ? 4 : s == 5 ? 5 : s < 17 ? 9 : s == 17 ? 11 : 8;
      constexpr bool vmx = s == 3 || s == 6 || s == 11 || s == 14 || s == 18 || s == 20;
      constexpr int o0 = s < 23 ? s + 1 : s < 27 ? 24 + 2 * (s - 23) : -1, o1 = (s >= 23 && s < 27) ? o0 + 1 : -1;
      constexpr int p0 = s < 15 ? (s / 3) * 4 + (s % 3 == 0 ? 0 : s % 3 + 1) : -1, p1 = (s < 15 && s % 3 == 0) ? p0 + 1 : -1;
      if constexpr (s == 5) sfor<0, 6>([&](auto k) { own_acc(k); });
      if constexpr (s == 12) sfor<6, 13>([&](auto k) { own_acc(k); });
      if constexpr (s == 20) sfor<13, 21>([&](auto k) { own_acc(k); });
      if constexpr (s == 28) sfor<21, 32>([&](auto k) { own_acc(k); });
      if constexpr (vc >= 0) WAITV(vy);
      if constexpr (p0 >= 0) set_m0((3 + p0 / 4) * 16384 + (4 * w + p0 % 4) * 1024);
      if constexpr (s >= 15) set_m0(w2_lds(s - 15));
      mfma_aav<A_ACC1 + 16, A_W + 4 * s, s == 0>(ot[s & 1][0]);
      if constexpr (p0 >= 0) dma16n(srd0, vtok, stok + (64 * (3 + p0 / 4) + 4 * (p0 % 4)) * HROW);
      if constexpr (s >= 15) dma16w(srd, lane16, w2_src(s - 15));
      if constexpr (vc >= 6) { v_rd(IC<vc>{}, IC<0>{}); v_rd(IC<vc>{}, IC<1>{}); }
      if constexpr (vmx) { x_max(vm, vq[0]); x_max(vm, vq[1]); }
      SB();
      mfma_aav<A_ACC1 + 32, A_W + 4 * s, s == 0>(ot[s & 1][1]);
      if constexpr (s < 31) { ot_rd(IC<s + 1>{}, IC<0>{}); ot_rd(IC<s + 1>{}, IC<1>{}); }
      if constexpr (p1 >= 0) set_m0((3 + p1 / 4) * 16384 + (4 * w + p1 % 4) * 1024);
      SB();
      mfma_aav<A_ACC1 + 48, A_W + 4 * s, s == 0>(ot[s & 1][2]);
      if constexpr (p1 >= 0) dma16n(srd0, vtok, stok + (64 * (3 + p1 / 4) + 4 * (p1 % 4)) * HROW);
      if constexpr (s < 15 && s % 3 != 0) ld_w1(IC<22 + 2 * (s / 3) + s % 3 - 1>{});
      if constexpr (s >= 15) ld_wd(IC<s - 3>{});
      if constexpr (vc >= 0 && vc < 6) { v_rd(IC<vc>{}, IC<0>{}); v_rd(IC<vc>{}, IC<1>{}); }
      if constexpr (vc >= 0) { v_rd(IC<vc>{}, IC<2>{}); v_rd(IC<vc>{}, IC<3>{}); }
      if constexpr (o1 >= 0) own_rd(IC<o1>{});
      if constexpr (vmx) { x_max(vm, vq[2]); x_max(vm, vq[3]); }
      SB();
      mfma_aav<A_ACC1, A_W + 4 * s, s == 0>(own[s]);
      if constexpr (s < 31) ot_rd(IC<s + 1>{}, IC<2>{});
      if constexpr (o0 >= 0) own_rd(IC<o0>{});
      if constexpr (s == 1 || s == 7 || s == 16 || s == 23) {
        if ((dg >> 1) == (s + 1) / 8) { hres[0] = trfrag(hp, 1024); hres[1] = trfrag(hp + 2048, 1024); }
      }
      SB();
      if constexpr (s == 6) { fix(IC<2>{}, IC<4>{}); BAR(); SB(); }
      if constexpr (s == 14) { fix(IC<4>{}, IC<6>{}); BAR(); SB(); }
      if constexpr (s == 22) { fix(IC<6>{}, IC<8>{}); BAR(); SB(); }
      if constexpr (s == 28) {
        lm = sum32x(sA + sB) * (1.0f / D);
        lv = rbf(rbf(fmaxf(sum32x(qA + qB) * (1.0f / D) - lm * lm, 0.0f)) + 1e-5f);
        pin2(lm, lv);
        SB();
      }
      if constexpr (s == 29) { lv = rbf(__builtin_sqrtf(lv)); pin2(lm, lv); SB(); }
      if constexpr (s == 30) {
        if (lane < 32) *(LAS v4f*)(sm + L_MR + (w * 32 + lane) * 16) = v4f{rbf(lm), -1.44269504088896340736f * __builtin_amdgcn_rcpf(lv), lv * lv, 0.0f};
        SB();
      }
    });
  }
  const int hh = lane >> 5;
  v4f mr[4], cs[4][4];
  auto cs_rd = [&](int k, int pt) {
    LAS char* cp = sm + L_CS + (pt * 16 + 4 * hh) * 4;
    cs[k][0] = *(LAS v4f*)cp; cs[k][1] = *(LAS v4f*)(cp + 32); cs[k][2] = *(LAS v4f*)(cp + 448); cs[k][3] = *(LAS v4f*)(cp + 480);
  };
  u32 wp[3] = {lane16, 65536 + lane16, L_W2 + lane16};
  asm volatile("" : "+v"(wp[1]), "+v"(wp[2]));
  v4u w2[3][3];
  sfor<0, 6>([&](auto i) { dsr128<i * 1024>(w2[i / 3][i % 3], wp[2]); });
  cs_rd(0, w);
  WAITV(11);
  BAR();
  const u32 sAct = WS_ACT + ((mt * 4) * AT + j * 7 + (j >> 2)) * 1024;
  const u32 sAin = WS_ACT + ((mt * 4 + 2 * e2) * AT + 29 * w) * 1024;
  v4u af[7][8];
  auto ld_af = [&](auto g, auto i) {
    if constexpr (g < 4) {
      constexpr int k = (i & 1) ^ (g & 1);
      ld_vgpr<(i & 1) * 1024>(af[g][i], srd, lane16, sAin + (k * AT + 7 * (i >> 1) + (g < 2 ? 2 * e2 : 2 - 2 * e2)) * 1024);
    } else {
      ld_vgpr<(g - 4) * 1024>(af[g][i], srd, lane16, sAin + ((i & 1) * AT + 7 * (i >> 1) + 4) * 1024);
    }
  };
  float ag[8], au[8], tg[8], tu[8], xe[8], pp[8], dn[8], rc[8], vv[8];
  v4u ob[2];
  auto s0 = [&](auto q) { constexpr int A = (q < 32 ? A_ACC1 + 16 * (q >> 3) : A_ACC2 + 16 * ((q >> 3) - 4)) + (q & 7); ag[q & 7] = accrd<A>(); au[q & 7] = accrd<A + 8>(); };
  auto s1 = [&](auto q) {
    constexpr int e = q & 7, U = q < 32 ? q >> 3 : 0, C = q < 32 ? 0 : (q >> 3) - 3;
    tg[e] = x_fnma(mr[U][0], cs[C][e >> 2][e & 3], ag[e]);
    tu[e] = x_fnma(mr[U][0], cs[C][2 + (e >> 2)][e & 3], au[e]);
    xe[e] = x_mul(tg[e], mr[U][1]);
  };
  auto s2a = [&](auto q) { constexpr int e = q & 7; xe[e] = x_exp(xe[e]); pp[e] = x_mul(tg[e], tu[e]); };
  auto s2b = [&](auto q) { constexpr int e = q & 7, U = q < 32 ? q >> 3 : 0; dn[e] = x_fma(xe[e], mr[U][2], mr[U][2]); };
  auto s3 = [&](auto q) { rc[q & 7] = x_rcp(dn[q & 7]); };
  auto s4a = [&](auto q) { constexpr int e = q & 7; vv[e] = x_mul(pp[e], rc[e]); };
  auto s4b = [&](auto q) { constexpr int e = q & 7; if constexpr (e & 1) ob[(q >> 3) & 1][e >> 1] = x_cap(x_pk(vv[e - 1], vv[e])); };
  auto s5 = [&](auto q) {
    constexpr int un = q >> 3;
    if constexpr ((q & 7) == 7)
      __builtin_amdgcn_raw_buffer_store_b128(ob[un & 1], rs, lane16, un < 4 ? sAct + ((w ^ un) * AT + w) * 1024 : sAct + (w * AT + un) * 1024, 16);
  };
  Sw st;
  auto breg = [](int o) { return o < 2 ? 240 + 8 * o : o < 10 ? 8 * (o - 2) : A_PRE + 8 * (o - 10); };
  auto wb_lds = [&](int x) { return x < 8 ? 98304 + w * 8192 + x * 1024 : x < 14 ? (x - 8) * 16384 + 12288 + w * 1024 : L_W2 + w * 6144 + (x - 14) * 1024; };
  {
#pragma unroll
    for (int i = 0; i < 4; ++i) mr[i] = *(LAS v4f*)(sm + L_MR + ((w ^ i) * 32 + (lane & 31)) * 16);
    asm volatile("s_nop 7" ::: "memory");
    s0(IC<0>{}); s1(IC<0>{}); s2a(IC<0>{}); s2b(IC<0>{});
    st.dn = dn[0]; st.p = pp[0]; st.v = 0;
    dma16(srd, lane16, w2_src(17), w2_lds(17));
    SB();
    sfor<0, 32>([&](auto s) {
      if constexpr (s == 20) { WAITV(19); BARX(); }
      constexpr int q = s < 31 ? s + 1 : 31, e = q & 7, U = q >> 3;
      constexpr int r = s < 30 ? s + 2 : 31, g = r >> 2;
      constexpr int off = (g < 2 ? g * 12288 : g < 6 ? (g - 2) * 16384 : (g - 6) * 16384) + (r & 3) * 3072;
      constexpr int x = (s >= 3 && s < 17) ? s - 3 : (s >= 20 && s < 26) ? s - 6 : -1;
      if constexpr (x >= 0) set_m0(wb_lds(x));
      p2_a<s == 0, off>(w2[s % 3][0], w2[(s + 2) % 3], own[s], wp[g < 2 ? 2 : g < 6 ? 0 : 1]);
      if constexpr (s < 3) ld_wd(IC<29 + s>{});
      if constexpr (x >= 0) dma16w(srd, lane16, sWD + 32768 + (3 * ((2 + (x >> 1)) & 3) + ((2 + (x >> 1)) >> 2)) * 2048 + (x & 1) * 1024);
      if constexpr (s >= 24) { ld_af(IC<(s >= 28)>{}, IC<2 * (s & 3)>{}); ld_af(IC<(s >= 28)>{}, IC<2 * (s & 3) + 1>{}); }
      if constexpr (s > 0) s5(IC<s - 1>{});
      u32 o;
      p2_b<s == 0, (s & 1) != 0, A_ACC1 + 16 * U + e>(w2[s % 3][1], w2[s % 3][2], own[s], st, o,
                                                      mr[U][0], mr[U][1], mr[U][2], cs[0][e >> 2][e & 3], cs[0][2 + (e >> 2)][e & 3]);
      if constexpr (s & 1) ob[(s >> 3) & 1][(s & 7) >> 1] = o;
      if constexpr (s == 31) s5(s);
      SB();
    });
    waitl3<0>(w2[0]); waitl3<0>(w2[1]); waitl3<0>(w2[2]);
  }
  cs_rd(1, 4); cs_rd(2, 5); cs_rd(3, 6);
  asm volatile("s_nop 7\n\ts_nop 3" ::: "memory");
  sfor<32, 40>([&](auto q) { s0(q); });
  sfor<32, 40>([&](auto q) { s1(q); });
  sfor<32, 40>([&](auto q) { s2a(q); });
  sfor<32, 40>([&](auto q) { s2b(q); });
  sfor<32, 40>([&](auto q) { s3(q); });
  sfor<32, 40>([&](auto q) { s4a(q); });
  sfor<32, 40>([&](auto q) { s4b(q); });
  s5(IC<39>{});
  SB();
  u32 am = 0;
  auto af_fix = [&](auto g) {
    for (int spin = 0; spin < SPIN_CAP && __any(am == ~0u); ++spin) {
      sfor<0, 8>([&](auto i) { ld_af(g, i); });
      waitv8<0>(af[g]);
      am = 0;
      sfor<0, 8>([&](auto i) { x_max(am, af[g][i]); });
    }
    am = 0;
  };
  s0(IC<40>{}); s1(IC<40>{}); s2a(IC<40>{});
  waitv8<10>(af[0]);
  s2b(IC<40>{});
  st.dn = dn[0]; st.p = pp[0];
  sfor<0, 8>([&](auto i) { x_max(am, af[0][i]); });
  af_fix(IC<0>{});
  const u32 wbp[4] = {98304 + w * 8192 + lane16, 12288 + w * 1024 + lane16, 65536 + 12288 + w * 1024 + lane16, L_W2 + w * 6144 + lane16};
  auto wb_rd = [&](auto x) {
    dsr_agpr<breg(2 + (x >> 1)) + 4 * (x & 1), (x < 8 ? x * 1024 : x < 14 ? ((x - 8) & 3) * 16384 : (x - 14) * 1024)>(wbp[x < 8 ? 0 : x < 12 ? 1 : x < 14 ? 2 : 3]);
  };
  SB();
  float hf[16];
  GAS u16* op = (GAS u16*)out + (size_t)(128 * mt + 64 * e2 + 32 * (w & 1) + (lane & 31)) * D + 64 * dg + 32 * (w >> 1) + 16 * hh;
  sfor<0, 6>([&](auto g) {
    constexpr int WV[6] = {10, 5, 9, 8, 8, 0};
    sfor<0, 16>([&](auto m) {
      if constexpr (m == 12 && g < 6) waitv8<WV[g < 6 ? g : 0]>(af[g < 6 ? g + 1 : 0]);
      if constexpr (g < 2) {
        if constexpr ((m & 1) == 0) {
          constexpr int i = m >> 1, k = (i & 1) ^ g, q = 40 + 8 * g + i, nq = q < 55 ? q + 1 : 55, e = nq & 7, C = (nq >> 3) - 3;
          u32 o;
          g2_pair<(g == 0 && m < 4), (i & 1) != 0, A_ACC2 + 16 * (C - 1) + e, A_ACC1 + 16 * k, (i < 6 ? A_PRE : A_W) + 8 * i>(
              af[g][i], st, o, mr[0][0], mr[0][1], mr[0][2], cs[C][e >> 2][e & 3], cs[C][2 + (e >> 2)][e & 3]);
          if constexpr (i & 1) ob[(q >> 3) & 1][i >> 1] = o;
        }
      } else if constexpr (g < 4) {
        constexpr int i = m >> 1, dt = m & 1, k = (i & 1) ^ (g & 1);
        mfma_aav<A_ACC1 + 32 * dt + 16 * k, A_W + 64 + 8 * i + 4 * dt, false>(af[g][i]);
      } else {
        constexpr int jl = m >> 2, dt = (m >> 1) & 1, k = m & 1;
        mfma_aav<A_ACC1 + 32 * dt + 16 * k, breg(4 * (g - 4) + jl) + 4 * dt, false>(af[g][2 * jl + k]);
      }
      if constexpr (g == 0 && m < 8) ld_af(IC<2>{}, m);
      if constexpr (g == 3 && m >= 8 && m < 12) wb_rd(IC<8 + m>{});
      if constexpr (g == 1 && m >= 8) ld_af(IC<3>{}, IC<m - 8>{});
      if constexpr (g == 2 && m < 8) ld_af(IC<4>{}, m);
      if constexpr (g == 2 && m >= 8 && m < 12) { wb_rd(IC<3 * (m - 8)>{}); wb_rd(IC<3 * (m - 8) + 1>{}); wb_rd(IC<3 * (m - 8) + 2>{}); }
      if constexpr (g == 2 && m >= 12) wb_rd(m);
      if constexpr (g == 3 && m < 8) ld_af(IC<5>{}, m);
      if constexpr (g == 4 && m >= 4 && m < 12) ld_af(IC<6>{}, IC<m - 4>{});
      if constexpr (m >= 12 && g < 6) { x_max(am, af[g < 6 ? g + 1 : 0][2 * (m - 12)]); x_max(am, af[g < 6 ? g + 1 : 0][2 * (m - 12) + 1]); }
      if constexpr (g < 2 && m == 15) s5(IC<47 + 8 * g>{});
      if constexpr (g == 5 && m < 8) { hf[2 * m] = bflo(hres[m >> 2][m & 3]); hf[2 * m + 1] = bfhi(hres[m >> 2][m & 3]); pin2(hf[2 * m], hf[2 * m + 1]); }
      if constexpr (g == 4 && m == 0) asm volatile("" : "+v"(op));
      SB();
    });
    if constexpr (g < 6) af_fix(IC<g + 1>{});
    if constexpr (g == 3) BAR();
    SB();
  });
  float a[16];
  auto xch = [&](auto W) {
    const u32 ad = W * 4096 + lane16;
    sfor<0, 16>([&](auto m) {
      constexpr int k = W ^ (((m >> 2) + 1) & 3), jl = m & 3, kp = W ^ (m >> 2);
      mfma_aav<A_ACC1 + 16 * k, breg(8 + jl) + 4 * (k >> 1), false>(af[6][2 * jl + (k & 1)]);
      if constexpr (m >= 4 && jl >= 2) {
        dsw_agpr<A_ACC1 + 16 * kp + 8 * (jl - 2), kp * 16384 + (jl - 2) * 2048>(ad);
        dsw_agpr<A_ACC1 + 16 * kp + 8 * (jl - 2) + 4, kp * 16384 + (jl - 2) * 2048 + 1024>(ad);
      }
      SB();
    });
    asm volatile("s_nop 7\n\ts_nop 7\n\ts_nop 3" ::: "memory");
    sfor<0, 16>([&](auto e) { a[e] = accrd<A_ACC1 + 16 * W + e>(); });
  };
  if (w == 0) xch(IC<0>{}); else if (w == 1) xch(IC<1>{}); else if (w == 2) xch(IC<2>{}); else xch(IC<3>{});
  BAR();
#pragma unroll
  for (int k = 1; k < 4; ++k) {
#pragma unroll
    for (int e = 0; e < 4; ++e) {
      const v4f p = *(LAS v4f*)(sm + w * 16384 + (w ^ k) * 4096 + e * 1024 + lane16);
#pragma unroll
      for (int c = 0; c < 4; ++c) a[4 * e + c] += p[c];
    }
  }
  {
    u32 o[8];
#pragma unroll
    for (int e = 0; e < 8; ++e) {
      const u32 f = pkbf(a[2 * e], a[2 * e + 1]);
      o[e] = pkbf(hf[2 * e] + bflo(f), hf[2 * e + 1] + bfhi(f));
    }
    *(GAS v4u*)op = v4u{o[0], o[1], o[2], o[3]};
    *(GAS v4u*)(op + 8) = v4u{o[4], o[5], o[6], o[7]};
  }
  asm volatile(".Lkf_end:");
}

extern "C" void kernel_launch(void* const* d_in, const int* in_sizes, int n_in, void* d_out, int out_size, void* d_ws, size_t ws_size,
                              hipStream_t stream) {
  (void)hipFuncSetAttribute((const void*)k_fused, hipFuncAttributeMaxDynamicSharedMemorySize, LDS_B);
  k_fused<<<256, 256, LDS_B, stream>>>((const u16*)d_in[0], (const u16*)d_in[1], (const u16*)d_in[2], (const u16*)d_in[3], (const u16*)d_in[4],
                                       (const u16*)d_in[5], (const u16*)d_in[6], (const u16*)d_in[8], (const u16*)d_in[9],
                                       (const u16*)d_in[10], (char*)d_ws, (u16*)d_out);
}
```
